```python
import torch

def _softmax(x, axis=-1):
    return torch.softmax(x, dim=axis)
def _gelu(x):
    return torch.nn.functional.gelu(x, approximate="tanh")
def _silu(x):
    return torch.nn.functional.silu(x)
def _sigmoid(x):
    return torch.sigmoid(x)
def _layernorm(x, axis=-1, eps=1e-5):
    m = x.mean(dim=axis, keepdim=True)
    v = x.var(dim=axis, keepdim=True, unbiased=False)
    return (x - m) / torch.sqrt(v + eps)
def _scatter_add(tgt, idx, src, axis=0):
    return tgt.clone().index_add_(axis, idx, src)
def _segment_sum(src, seg, n):
    out = torch.zeros((n,) + src.shape[1:], dtype=src.dtype)
    return out.index_add_(0, seg, src)
def _pad(x, pw):
    flat = tuple(v for pair in reversed(pw) for v in pair)
    return torch.nn.functional.pad(x, flat)

kernel_name = 'sequential_preLN__sliding_block_cached__glu_gated_glu__decode'

def setup_inputs():

    _w = (lambda a: (lambda t: t.to(torch.bfloat16) if t.is_floating_point() else t)(torch.as_tensor(a)))
    def _A_setup():
        B, Sq, D = 8, 1, 512
        Hq, Hkv, Sk, Dh = 32, 8, 4096, 128
        assert Hq % Hkv == 0, (Hq, Hkv)
        return {
            "x": _w((lambda *s: torch.randn(*s, dtype=torch.bfloat16) * 0.05)(B, Sq, D)),
            "k_cache": _w((lambda *s: torch.randn(*s, dtype=torch.bfloat16) * 3.2)(B, Hkv, Sk, Dh)),
            "v_cache": _w((lambda *s: torch.randn(*s, dtype=torch.bfloat16) * 0.4)(B, Hkv, Sk, Dh)),
            "wq": _w((lambda *s: torch.randn(*s, dtype=torch.bfloat16) * 3.2)(D, Hq * Dh) * (D ** -0.5)),
            "wo": _w((lambda *s: torch.randn(*s, dtype=torch.bfloat16) * 0.4)(Hq * Dh, D) * ((Hq * Dh) ** -0.5)),
        }
    _A_in = _A_setup()

    def _B_setup():
        B, S, D, F = 8, 1, 512, 1792
        return {
            "x": _w((lambda *s: torch.randn(*s, dtype=torch.bfloat16) * 0.05)(B, S, D)),
            "w_gate": _w((lambda *s: torch.randn(*s, dtype=torch.bfloat16) * 0.1)(D, F)),
            "w_up": _w((lambda *s: torch.randn(*s, dtype=torch.bfloat16) * 0.05)(D, F)),
            "w_down": _w((lambda *s: torch.randn(*s, dtype=torch.bfloat16) * 0.05)(F, D)),
        }
    _B_in = _B_setup()

    return {
        "A_x": _A_in["x"],
        "A_k_cache": _A_in["k_cache"],
        "A_v_cache": _A_in["v_cache"],
        "A_wq": _A_in["wq"],
        "A_wo": _A_in["wo"],
        "B_x": _B_in["x"],
        "B_w_gate": _B_in["w_gate"],
        "B_w_up": _B_in["w_up"],
        "B_w_down": _B_in["w_down"],
    }

def reference(A_x, A_k_cache, A_v_cache, A_wq, A_wo, B_x, B_w_gate, B_w_up, B_w_down):
    def _A_ref(x, k_cache, v_cache, wq, wo):
        B, Sq, D = x.shape
        Hq, Dh = 32, 128
        Hkv, Sk = k_cache.shape[1], k_cache.shape[2]
        W = min(4096, max(256, Sk // 4))
        rep = Hq // Hkv
        q = (lambda x, *d: x.permute(*d))((x @ wq).reshape(B, Sq, Hq, Dh), 0, 2, 1, 3)
        k = (lambda x, n, axis: torch.repeat_interleave(x, n, dim=axis))(k_cache[:, :, Sk - W:, :], rep, axis=1)
        v = (lambda x, n, axis: torch.repeat_interleave(x, n, dim=axis))(v_cache[:, :, Sk - W:, :], rep, axis=1)
        scores = torch.einsum("bhqd,bhkd->bhqk", q, k) * (Dh ** -0.5)
        p = _softmax(scores, axis=-1)
        o = torch.einsum("bhqk,bhkd->bhqd", p, v)
        return (lambda x, *d: x.permute(*d))(o, 0, 2, 1, 3).reshape(B, Sq, Hq * Dh) @ wo

    def _B_ref(x, w_gate, w_up, w_down):
        g = torch.einsum("bsd,df->bsf", x, w_gate)
        u = torch.einsum("bsd,df->bsf", x, w_up)
        h = _sigmoid(g) * u
        return torch.einsum("bsf,fd->bsd", h, w_down)

    x = A_x
    h = x + _A_ref(_layernorm(x, axis=-1), A_k_cache, A_v_cache, A_wq, A_wo)
    h2 = h + _B_ref(_layernorm(h, axis=-1), B_w_gate, B_w_up, B_w_down)
    return h2
```

```cpp
#include <hip/hip_runtime.h>
#include <hip/hip_bf16.h>
#include <stdint.h>

namespace {
constexpr int B = 8, D = 512, HQ = 32, HKV = 8, SK = 4096, DH = 128, W = 1024, F = 1792;
constexpr int NSPLIT = 4, KPS = W / NSPLIT;
typedef unsigned short u16;

__device__ __forceinline__ float bf2f(u16 b) { return __builtin_bit_cast(float, (unsigned)b << 16); }
__device__ __forceinline__ u16 f2bf(float f) {
  __bf16 h = (__bf16)f;
  return __builtin_bit_cast(u16, h);
}
__device__ __forceinline__ float rbf(float f) { return bf2f(f2bf(f)); }

__device__ void ln_rows(const float* in  , float* out  , int tid) {
  const int wave = tid >> 6, lane = tid & 63;
  for (int r = wave; r < B; r += 4) {
    float v[8];
    float s = 0.f;
    for (int j = 0; j < 8; ++j) { v[j] = in[r * D + lane + 64 * j]; s += v[j]; }
    for (int o = 32; o; o >>= 1) s += __shfl_xor(s, o);
    const float mean_f = s * (1.0f / D);
    const float m = rbf(mean_f);
    float ss = 0.f;
    for (int j = 0; j < 8; ++j) { const float d = v[j] - mean_f; ss += d * d; }
    for (int o = 32; o; o >>= 1) ss += __shfl_xor(ss, o);
    const float var = rbf(ss * (1.0f / D));
    const float den = rbf(sqrtf(rbf(var + 1e-5f)));
    for (int j = 0; j < 8; ++j) out[r * D + lane + 64 * j] = rbf(rbf(v[j] - m) / den);
  }
}

__global__ void __launch_bounds__(256) k_q(const u16* x, const u16* wq, u16* q) {
  __shared__ float xs[B * D], xn[B * D], red[16][16][B];
  const int tid = threadIdx.x;
  for (int i = tid; i < B * D; i += 256) xs[i] = bf2f(x[i]);
  __syncthreads();
  ln_rows(xs, xn, tid);
  __syncthreads();
  const int c = tid & 15, kc = tid >> 4, col = blockIdx.x * 16 + c;
  float acc[B];
  for (int b = 0; b < B; ++b) acc[b] = 0.f;
  for (int k = kc * 32; k < kc * 32 + 32; ++k) {
    const float w = bf2f(wq[(size_t)k * (HQ * DH) + col]);
    for (int b = 0; b < B; ++b) acc[b] = fmaf(xn[b * D + k], w, acc[b]);
  }
  for (int b = 0; b < B; ++b) red[kc][c][b] = acc[b];
  __syncthreads();
  if (tid < 128) {
    const int b = tid >> 4, cc = tid & 15;
    float s = 0.f;
    for (int j = 0; j < 16; ++j) s += red[j][cc][b];
    q[b * (HQ * DH) + blockIdx.x * 16 + cc] = f2bf(s);
  }
}

__global__ void __launch_bounds__(256) k_attn(const u16* q, const u16* kc, const u16* vc, float* po, float* pml) {
  __shared__ float qs[4][DH], p[4][KPS], red[4][4], o2[2][4][DH];
  const int tid = threadIdx.x, s = blockIdx.x & 3, h = (blockIdx.x >> 2) & 7, b = blockIdx.x >> 5;
  for (int i = tid; i < 4 * DH; i += 256) qs[i >> 7][i & 127] = bf2f(q[b * (HQ * DH) + h * 4 * DH + i]);
  __syncthreads();
  const size_t base = ((size_t)(b * HKV + h) * SK + (SK - W) + s * KPS) * DH;
  const u16* kr = kc + base + (size_t)tid * DH;
  float sc[4] = {0.f, 0.f, 0.f, 0.f};
  for (int d = 0; d < DH; ++d) {
    const float kv = bf2f(kr[d]);
    for (int g = 0; g < 4; ++g) sc[g] = fmaf(qs[g][d], kv, sc[g]);
  }
  float m[4], l[4];
  for (int g = 0; g < 4; ++g) {
    sc[g] = rbf(rbf(sc[g]) * 0.08838834764831845f);
    float mm = sc[g];
    for (int o = 32; o; o >>= 1) mm = fmaxf(mm, __shfl_xor(mm, o));
    if ((tid & 63) == 0) red[g][tid >> 6] = mm;
  }
  __syncthreads();
  for (int g = 0; g < 4; ++g) m[g] = fmaxf(fmaxf(red[g][0], red[g][1]), fmaxf(red[g][2], red[g][3]));
  __syncthreads();
  for (int g = 0; g < 4; ++g) {
    const float e = __expf(sc[g] - m[g]);
    p[g][tid] = e;
    float ss = e;
    for (int o = 32; o; o >>= 1) ss += __shfl_xor(ss, o);
    if ((tid & 63) == 0) red[g][tid >> 6] = ss;
  }
  __syncthreads();
  for (int g = 0; g < 4; ++g) l[g] = red[g][0] + red[g][1] + red[g][2] + red[g][3];
  const int d = tid & 127, half = tid >> 7;
  float acc[4] = {0.f, 0.f, 0.f, 0.f};
  for (int k = half * 128; k < half * 128 + 128; ++k) {
    const float v = bf2f(vc[base + (size_t)k * DH + d]);
    for (int g = 0; g < 4; ++g) acc[g] = fmaf(p[g][k], v, acc[g]);
  }
  for (int g = 0; g < 4; ++g) o2[half][g][d] = acc[g];
  __syncthreads();
  for (int i = tid; i < 4 * DH; i += 256) {
    const int g = i >> 7, dd = i & 127;
    po[((size_t)(b * HQ + h * 4 + g) * NSPLIT + s) * DH + dd] = o2[0][g][dd] + o2[1][g][dd];
  }
  if (tid < 4) {
    pml[((b * HQ + h * 4 + tid) * NSPLIT + s) * 2 + 0] = m[tid];
    pml[((b * HQ + h * 4 + tid) * NSPLIT + s) * 2 + 1] = l[tid];
  }
}

__global__ void __launch_bounds__(256) k_wo(const float* po, const float* pml, const u16* wo, float* part) {
  __shared__ float o[B][DH], red[4][64][B];
  const int tid = threadIdx.x, h = blockIdx.x >> 3, cg = blockIdx.x & 7;
  for (int i = tid; i < B * DH; i += 256) {
    const int b = i >> 7, d = i & 127;
    const float* ml = pml + (b * HQ + h) * NSPLIT * 2;
    float M = fmaxf(fmaxf(ml[0], ml[2]), fmaxf(ml[4], ml[6]));
    float L = 0.f, acc = 0.f;
    for (int s = 0; s < NSPLIT; ++s) {
      const float w = __expf(ml[2 * s] - M);
      L += w * ml[2 * s + 1];
      acc += w * po[((size_t)(b * HQ + h) * NSPLIT + s) * DH + d];
    }
    o[b][d] = rbf(acc / L);
  }
  __syncthreads();
  const int c = tid & 63, kq = tid >> 6, col = cg * 64 + c;
  float acc[B];
  for (int b = 0; b < B; ++b) acc[b] = 0.f;
  for (int k = kq * 32; k < kq * 32 + 32; ++k) {
    const float w = bf2f(wo[(size_t)(h * DH + k) * D + col]);
    for (int b = 0; b < B; ++b) acc[b] = fmaf(o[b][k], w, acc[b]);
  }
  for (int b = 0; b < B; ++b) red[kq][c][b] = acc[b];
  __syncthreads();
  for (int i = tid; i < 64 * B; i += 256) {
    const int b = i >> 6, cc = i & 63;
    part[((size_t)h * B + b) * D + cg * 64 + cc] = red[0][cc][b] + red[1][cc][b] + red[2][cc][b] + red[3][cc][b];
  }
}

__global__ void __launch_bounds__(256) k_h(const u16* x, const float* part, float* hb) {
  const int i = blockIdx.x * 256 + threadIdx.x;
  const int b = i >> 9, c = i & 511;
  float s = 0.f;
  for (int h = 0; h < HQ; ++h) s += part[((size_t)h * B + b) * D + c];
  hb[i] = rbf(bf2f(x[i]) + rbf(s));
}

__global__ void __launch_bounds__(256) k_gu(const float* hb, const u16* wg, const u16* wu, float* hh) {
  __shared__ float xs[B * D], xn[B * D], red[2][16][8][B];
  const int tid = threadIdx.x;
  for (int i = tid; i < B * D; i += 256) xs[i] = hb[i];
  __syncthreads();
  ln_rows(xs, xn, tid);
  __syncthreads();
  const int c = tid & 7, mat = (tid >> 3) & 1, kc = tid >> 4, col = blockIdx.x * 7 + (c < 7 ? c : 0);
  const u16* wm = mat ? wu : wg;
  float acc[B];
  for (int b = 0; b < B; ++b) acc[b] = 0.f;
  for (int k = kc * 32; k < kc * 32 + 32; ++k) {
    const float w = bf2f(wm[(size_t)k * F + col]);
    for (int b = 0; b < B; ++b) acc[b] = fmaf(xn[b * D + k], w, acc[b]);
  }
  for (int b = 0; b < B; ++b) red[mat][kc][c][b] = acc[b];
  __syncthreads();
  if (tid < 56) {
    const int b = tid / 7, cc = tid % 7;
    float g = 0.f, u = 0.f;
    for (int j = 0; j < 16; ++j) { g += red[0][j][cc][b]; u += red[1][j][cc][b]; }
    g = rbf(g); u = rbf(u);
    const float sg = rbf(1.0f / (1.0f + __expf(-g)));
    hh[b * F + blockIdx.x * 7 + cc] = rbf(sg * u);
  }
}

__global__ void __launch_bounds__(256) k_dn(const float* hb, const float* hh, const u16* wd, u16* out) {
  __shared__ float red[32][8][B];
  const int tid = threadIdx.x, c = tid & 7, kc = tid >> 3, col = blockIdx.x * 8 + c;
  float acc[B];
  for (int b = 0; b < B; ++b) acc[b] = 0.f;
  for (int k = kc * 56; k < kc * 56 + 56; ++k) {
    const float w = bf2f(wd[(size_t)k * D + col]);
    for (int b = 0; b < B; ++b) acc[b] = fmaf(hh[b * F + k], w, acc[b]);
  }
  for (int b = 0; b < B; ++b) red[kc][c][b] = acc[b];
  __syncthreads();
  if (tid < 64) {
    const int b = tid >> 3, cc = tid & 7;
    float s = 0.f;
    for (int j = 0; j < 32; ++j) s += red[j][cc][b];
    const int i = b * D + blockIdx.x * 8 + cc;
    out[i] = f2bf(hb[i] + rbf(s));
  }
}
}

extern "C" void kernel_launch(void* const* d_in, const int* in_sizes, int n_in, void* d_out, int out_size, void* d_ws,
                              size_t ws_size, hipStream_t stream) {
  const u16* x = (const u16*)d_in[0];
  const u16* kc = (const u16*)d_in[1];
  const u16* vc = (const u16*)d_in[2];
  const u16* wq = (const u16*)d_in[3];
  const u16* wo = (const u16*)d_in[4];
  const u16* wg = (const u16*)d_in[6];
  const u16* wu = (const u16*)d_in[7];
  const u16* wd = (const u16*)d_in[8];
  char* ws = (char*)d_ws;
  u16* q = (u16*)ws;
  float* po = (float*)(ws + (1 << 16));
  float* pml = (float*)(ws + (1 << 20));
  float* part = (float*)(ws + (2 << 20));
  float* hb = (float*)(ws + (3 << 20));
  float* hh = (float*)(ws + (4 << 20));
  k_q<<<256, 256, 0, stream>>>(x, wq, q);
  k_attn<<<256, 256, 0, stream>>>(q, kc, vc, po, pml);
  k_wo<<<256, 256, 0, stream>>>(po, pml, wo, part);
  k_h<<<16, 256, 0, stream>>>(x, part, hb);
  k_gu<<<256, 256, 0, stream>>>(hb, wg, wu, hh);
  k_dn<<<64, 256, 0, stream>>>(hb, hh, wd, (u16*)d_out);
}
```

```cpp
#include <hip/hip_runtime.h>
#include <hip/hip_bf16.h>
#include <stdint.h>

namespace {

typedef unsigned v4u __attribute__((ext_vector_type(4)));
typedef unsigned v2u __attribute__((ext_vector_type(2)));
typedef float f32x4 __attribute__((ext_vector_type(4)));
typedef __bf16 bf16x8 __attribute__((ext_vector_type(8)));
typedef short s16x4 __attribute__((ext_vector_type(4)));
typedef unsigned long long u64;
#define LAS __attribute__((address_space(3)))
typedef LAS unsigned char* lds_p;

constexpr int NB = 8, D = 512, HQ = 32, HKV = 8, SK = 4096, DH = 128, WIN = 1024, FF = 1792;
constexpr int NTHR = 768;
constexpr int AUX_SC1 = 16;
constexpr unsigned SPIN_CAP = 1u << 16;
#define PD 3
#define QSLEEP 16
#define HPRIO 2

constexpr int WS_EPOCH = 0;
constexpr int WS_QP = 1 << 16;
constexpr int WS_OP = WS_QP + (1 << 20);
constexpr int WS_LS = WS_OP + (1 << 19);
constexpr int WS_RP = WS_LS + (1 << 15);
constexpr int WS_GH = WS_RP + (1 << 20);
constexpr int WS_FP = WS_GH + (1 << 14);
constexpr int WS_END = WS_FP + 256 * 56 * 128;
constexpr int WS_STAMP = 64 << 20;

constexpr int L_WQ = 0;
constexpr int L_RED = 0;
constexpr int L_XN = 16384;
constexpr int L_Q = L_XN + 8 * 288;
constexpr int L_MAX = L_Q + 4 * 320;
constexpr int L_SUM = L_MAX + 128;
constexpr int L_FLAG = L_SUM + 128;
constexpr int L_OS = L_FLAG + 64;
constexpr int L_HN = L_OS + 8 * 288;
constexpr int L_R3 = L_HN + 8 * 1056;
constexpr int L_HH = L_R3 + 8192;
constexpr int L_W = ((L_HH + 8 * 96 + 255) / 256) * 256;
constexpr int L_GT = L_W, L_UP = L_W + 32768, L_DN = L_W + 65536;
constexpr int LDS_BYTES = L_W + 98304;
static_assert(LDS_BYTES <= 160 * 1024 && LDS_BYTES > 80 * 1024, "one workgroup per CU");

__device__ __forceinline__ float bf_lo(unsigned u) { return __builtin_bit_cast(float, u << 16); }
__device__ __forceinline__ float bf_hi(unsigned u) { return __builtin_bit_cast(float, u & 0xffff0000u); }
__device__ __forceinline__ unsigned pack_bf(float lo, float hi) {
  typedef __bf16 bf2 __attribute__((ext_vector_type(2)));
  bf2 r = {(__bf16)lo, (__bf16)hi};
  return __builtin_bit_cast(unsigned, r);
}
__device__ __forceinline__ float rbf(float f) { return bf_lo(pack_bf(f, f)); }
__device__ __forceinline__ float asf(unsigned u) { return __builtin_bit_cast(float, u); }
__device__ __forceinline__ unsigned asu(float f) { return __builtin_bit_cast(unsigned, f); }
template <int CTRL> __device__ __forceinline__ float dpp(float x) {
  return __builtin_bit_cast(float, __builtin_amdgcn_mov_dpp(__builtin_bit_cast(int, x), CTRL, 0xf, 0xf, true));
}
__device__ __forceinline__ float x16_sum(float x) {
  auto s = __builtin_amdgcn_permlane16_swap(asu(x), asu(x), false, false);
  return asf(s[0]) + asf(s[1]);
}
__device__ __forceinline__ float x32_sum(float x) {
  auto s = __builtin_amdgcn_permlane32_swap(asu(x), asu(x), false, false);
  return asf(s[0]) + asf(s[1]);
}
__device__ __forceinline__ float x16_max(float x) {
  auto s = __builtin_amdgcn_permlane16_swap(asu(x), asu(x), false, false);
  return fmaxf(asf(s[0]), asf(s[1]));
}
__device__ __forceinline__ float x32_max(float x) {
  auto s = __builtin_amdgcn_permlane32_swap(asu(x), asu(x), false, false);
  return fmaxf(asf(s[0]), asf(s[1]));
}
__device__ __forceinline__ float wave_sum(float v) {
  v += dpp<0xB1>(v);
  v += dpp<0x4E>(v);
  v += dpp<0x141>(v);
  v += dpp<0x140>(v);
  return x32_sum(x16_sum(v));
}
__device__ __forceinline__ f32x4 mfma(v4u a, v4u b, f32x4 c) {
  return __builtin_amdgcn_mfma_f32_16x16x32_bf16(__builtin_bit_cast(bf16x8, a), __builtin_bit_cast(bf16x8, b), c, 0, 0, 0);
}
__device__ __forceinline__ v2u tr_read(lds_p p) {
  typedef short v4s __attribute__((ext_vector_type(4)));
  return __builtin_bit_cast(v2u, __builtin_amdgcn_ds_read_tr16_b64_v4i16((LAS v4s*)p));
}
__device__ __forceinline__ v4u tr_frag(lds_p p, int row_stride) {
  const v2u a = tr_read(p), b = tr_read(p + 4 * row_stride);
  return v4u{a.x, a.y, b.x, b.y};
}
__device__ __forceinline__ v4u lds_ld128(lds_p p) { return *(const LAS v4u*)p; }
__device__ __forceinline__ void glds16(const void* gsrc, unsigned lds_dst) {
  unsigned keep;
  asm volatile("s_mov_b32 %0, m0\n\ts_mov_b32 m0, %2\n\ts_nop 0\n\tglobal_load_lds_dwordx4 %1, off nt\n\ts_mov_b32 m0, %0"
               : "=&s"(keep) : "v"(gsrc), "s"(lds_dst) : "memory");
}
#define BAR() do { asm volatile("s_waitcnt lgkmcnt(0)" ::: "memory"); __builtin_amdgcn_s_barrier(); asm volatile("" ::: "memory"); } while (0)
__device__ __forceinline__ void lds_arrive(volatile LAS unsigned* p, int lane) {
  asm volatile("" ::: "memory");
  if (lane == 0) __hip_atomic_fetch_add((LAS unsigned*)p, 1u, __ATOMIC_RELAXED, __HIP_MEMORY_SCOPE_WORKGROUP);
  asm volatile("" ::: "memory");
}
__device__ __forceinline__ void lds_wait(volatile LAS unsigned* p, unsigned n) {
  while (*p < n) { }
  asm volatile("" ::: "memory");
}
__device__ __forceinline__ bool all_ok(bool ok) { return __builtin_amdgcn_ballot_w64(ok) == ~0ull; }

__device__ __forceinline__ void layernorm8(float (&f)[8]) {
  float s = ((f[0] + f[1]) + (f[2] + f[3])) + ((f[4] + f[5]) + (f[6] + f[7]));
  const float mean = wave_sum(s) * (1.0f / D);
  float ss = 0.f;
#pragma unroll
  for (int i = 0; i < 8; ++i) { const float d = f[i] - mean; ss = fmaf(d, d, ss); }
  const float var = rbf(wave_sum(ss) * (1.0f / D));
  const float den = rbf(__builtin_sqrtf(rbf(var + 1e-5f)));
  const float inv = __builtin_amdgcn_rcpf(den), mb = rbf(mean);
#pragma unroll
  for (int i = 0; i < 8; ++i) f[i] = rbf(f[i] - mb) * inv;
}

__device__ __forceinline__ void publish_tile(f32x4 d, __amdgpu_buffer_rsrc_t rs, int base, int stride_b, unsigned tag, int lane) {
  const int n = lane & 15, gq = lane >> 4;
  const bool hi = gq >= 2, e = n & 1;
  const float a = hi ? d[2] : d[0], c = hi ? d[3] : d[1];
  const float recv = dpp<0xB1>(e ? a : c);
  const int bb = 4 * (gq & 1) + (hi ? 2 : 0) + (e ? 1 : 0);
  const v4u v = {asu(e ? recv : a), tag, asu(e ? c : recv), tag};
  __builtin_amdgcn_raw_buffer_store_b128(v, rs, base + bb * stride_b + (n >> 1) * 16, 0, AUX_SC1);
}

#define STAMP(i) do { } while (0)

__device__ __forceinline__ void final_cell(__amdgpu_buffer_rsrc_t rs, unsigned tag, int c, int lane, unsigned short* out, unsigned* ws_epoch) {
  const int bq = c >> 5, ct = c & 31, p = lane & 7;
  v2u hv;
  for (unsigned spins = 0;; ++spins) {
    asm volatile("" ::: "memory");
    hv = __builtin_amdgcn_raw_buffer_load_b64(rs, WS_GH + (bq * 256 + 8 * ct + p) * 8, 0, AUX_SC1);
    if (all_ok(hv.y == tag) || spins > SPIN_CAP) break;
    __builtin_amdgcn_s_sleep(2);
  }
  const int off = WS_FP + ((bq * 32 + ct) * 56 + (lane >> 3)) * 128 + p * 16;
  float a0, a1;
  for (unsigned spins = 0;; ++spins) {
    asm volatile("" ::: "memory");
    v4u v[7];
#pragma unroll
    for (int i = 0; i < 7; ++i) v[i] = __builtin_amdgcn_raw_buffer_load_b128(rs, off + i * 8 * 128, 0, AUX_SC1);
    bool ok = true;
    a0 = 0.f; a1 = 0.f;
#pragma unroll
    for (int i = 0; i < 7; ++i) { ok &= (v[i].y == tag) & (v[i].w == tag); a0 += asf(v[i].x); a1 += asf(v[i].z); }
    if (all_ok(ok) || spins > SPIN_CAP) break;
  }
  a0 += dpp<0x128>(a0); a1 += dpp<0x128>(a1);
  a0 = x32_sum(x16_sum(a0)); a1 = x32_sum(x16_sum(a1));
  if (lane < 8) {
    const unsigned o = pack_bf(bf_lo(hv.x) + rbf(a0), bf_hi(hv.x) + rbf(a1));
    *(unsigned*)(out + bq * D + 16 * ct + 2 * p) = o;
  }
  if (c == 0 && lane == 0) __hip_atomic_store(ws_epoch, tag, __ATOMIC_RELAXED, __HIP_MEMORY_SCOPE_AGENT);
}

__global__ void __launch_bounds__(NTHR, 3)
fused_decode(const unsigned short* __restrict__ x, const unsigned short* __restrict__ kc, const unsigned short* __restrict__ vc,
             const unsigned short* __restrict__ wq, const unsigned short* __restrict__ wo, const unsigned short* __restrict__ wg,
             const unsigned short* __restrict__ wu, const unsigned short* __restrict__ wd, unsigned short* __restrict__ out,
             unsigned char* __restrict__ ws) {
  extern __shared__ __attribute__((aligned(16))) unsigned char smem[];
  const lds_p L = (lds_p)smem;
  const unsigned lds0 = (unsigned)(size_t)L;
  const int tid = threadIdx.x, lane = tid & 63;
  const int wv = __builtin_amdgcn_readfirstlane(tid >> 6);
  const int w = wv - 4, st_id = tid - 256;
  const int c = blockIdx.x, j = c & 7, r = c >> 3;
  const int b = r >> 2, s = r & 3;
  const int g = lane >> 4, n16 = lane & 15;
  const bool is_wo = r < 16, is_mlp = (r >= 16) && (r < 24) && (j < 7);
  const __amdgpu_buffer_rsrc_t rs = __builtin_amdgcn_make_buffer_rsrc((void*)ws, 0, WS_END, 0x00020000);
  volatile LAS unsigned* flags = (volatile LAS unsigned*)(L + L_FLAG);
  STAMP(0);

  if (wv >= 4) {
    const size_t tile = ((size_t)(b * HKV + j) * SK + (SK - WIN) + 256 * s + 32 * w) * (DH * 2);
    const unsigned char* kb = (const unsigned char*)kc + tile + 4096;
    const unsigned char* vb = (const unsigned char*)vc + tile;
    const unsigned koff = (lane & 7) * 256 + (2 * g + ((lane >> 3) & 1)) * 16;
    const unsigned voff = (16 * (g >> 1) + 4 * (g & 1)) * 256 + n16 * 16;
    v4u K[8], V[8];
#define LDK(i, imm) asm volatile("global_load_dwordx4 %0, %1, %2 offset:" #imm " nt" : "=v"(K[i]) : "v"(koff), "s"(kb))
#define LDV(i, imm) asm volatile("global_load_dwordx4 %0, %1, %2 offset:" #imm " nt" : "=v"(V[i]) : "v"(voff), "s"(vb))
#define STR2(x) #x
#define STR(x) STR2(x)
#define THR(n) do { if (n >= PD) asm volatile("s_waitcnt vmcnt(%0)" :: "i"(PD - 1)); } while (0)
    LDK(0, -4096); THR(1); LDK(1, -3968); THR(2); LDK(2, -2048); THR(3); LDK(3, -1920);
    __builtin_amdgcn_s_barrier();
    THR(4); LDK(4, 0); THR(5); LDK(5, 128); THR(6); LDK(6, 2048); THR(7); LDK(7, 2176);
    THR(8); LDV(0, 0); THR(9); LDV(1, 256); THR(10); LDV(2, 512); THR(11); LDV(3, 768);
    THR(12); LDV(4, 2048); THR(13); LDV(5, 2304); THR(14); LDV(6, 2560); THR(15); LDV(7, 2816);
    STAMP(1);
    BAR();
    STAMP(2);
    const unsigned tag = flags[1];
    const lds_p qa = L + L_Q + (n16 & 3) * 320 + (2 * g + ((n16 >> 2) & 1)) * 16;
    const v4u q0 = lds_ld128(qa), q1 = lds_ld128(qa + 128);
    asm volatile("s_waitcnt vmcnt(8)" : "+v"(K[0]), "+v"(K[1]), "+v"(K[2]), "+v"(K[3]), "+v"(K[4]), "+v"(K[5]), "+v"(K[6]), "+v"(K[7]));
    STAMP(3);
    f32x4 sa[4];
#pragma unroll
    for (int o = 0; o < 4; ++o) {
      sa[o] = mfma(K[2 * o], q0, f32x4{0.f, 0.f, 0.f, 0.f});
      sa[o] = mfma(K[2 * o + 1], q1, sa[o]);
    }
    float sc[8];
#pragma unroll
    for (int p = 0; p < 2; ++p)
#pragma unroll
      for (int i = 0; i < 4; ++i) {
        auto sw = __builtin_amdgcn_permlane32_swap(asu(sa[p][i]), asu(sa[p + 2][i]), false, false);
        const float v = asf(sw[0]) + dpp<0x104>(asf(sw[1]));
        sc[4 * p + i] = rbf(rbf(v) * 0.08838834764831845f);
      }
    float m = fmaxf(fmaxf(fmaxf(sc[0], sc[1]), fmaxf(sc[2], sc[3])), fmaxf(fmaxf(sc[4], sc[5]), fmaxf(sc[6], sc[7])));
    m = x32_max(x16_max(m));
    if (lane < 4) ((LAS float*)(L + L_MAX))[w * 4 + lane] = m;
    lds_arrive(flags + 2, lane);
    lds_wait(flags + 2, 8);
    {
      const LAS float* mx = (const LAS float*)(L + L_MAX) + (n16 & 3);
      m = mx[0];
#pragma unroll
      for (int i = 1; i < 8; ++i) m = fmaxf(m, mx[4 * i]);
    }
    float pe[8], ls = 0.f;
#pragma unroll
    for (int i = 0; i < 8; ++i) { pe[i] = __builtin_amdgcn_exp2f((sc[i] - m) * 1.4426950408889634f); ls += pe[i]; }
    const v4u P = {pack_bf(pe[0], pe[1]), pack_bf(pe[2], pe[3]), pack_bf(pe[4], pe[5]), pack_bf(pe[6], pe[7])};
    ls = x32_sum(x16_sum(ls));
    if (lane < 4) ((LAS float*)(L + L_SUM))[w * 4 + lane] = ls;
    STAMP(4);
    v4u T[8];
#define XPOSE(n_)                                                                                         \
  _Pragma("unroll") for (int mm = 0; mm < 4; ++mm) {                                                      \
    T[2 * mm][n_] = __builtin_amdgcn_perm(V[2 * n_ + 1][mm], V[2 * n_][mm], 0x05040100u);                 \
    T[2 * mm + 1][n_] = __builtin_amdgcn_perm(V[2 * n_ + 1][mm], V[2 * n_][mm], 0x07060302u);             \
  }
    asm volatile("s_waitcnt vmcnt(6)" : "+v"(V[0]), "+v"(V[1])); XPOSE(0) __builtin_amdgcn_sched_barrier(0);
    asm volatile("s_waitcnt vmcnt(4)" : "+v"(V[2]), "+v"(V[3])); XPOSE(1) __builtin_amdgcn_sched_barrier(0);
    asm volatile("s_waitcnt vmcnt(2)" : "+v"(V[4]), "+v"(V[5])); XPOSE(2) __builtin_amdgcn_sched_barrier(0);
    asm volatile("s_waitcnt vmcnt(0)" : "+v"(V[6]), "+v"(V[7]));
    STAMP(5);
    XPOSE(3)
    f32x4 oa[8];
#pragma unroll
    for (int i = 0; i < 8; ++i) oa[i] = mfma(P, T[i], f32x4{0.f, 0.f, 0.f, 0.f});
    if (lane < 16) {
      LAS f32x4* red = (LAS f32x4*)(L + L_RED + w * 2048) + lane;
#pragma unroll
      for (int hh = 0; hh < 4; ++hh) {
        red[(hh * 2 + 0) * 16] = f32x4{oa[0][hh], oa[1][hh], oa[2][hh], oa[3][hh]};
        red[(hh * 2 + 1) * 16] = f32x4{oa[4][hh], oa[5][hh], oa[6][hh], oa[7][hh]};
      }
    }
    lds_arrive(flags + 3, lane);
    STAMP(6);
    if (w < 3) {
      lds_wait(flags + 3, 8);
      const int hh = w < 2 ? (st_id >> 5) : (lane & 3);
      const LAS float* sm = (const LAS float*)(L + L_SUM) + hh;
      float l = sm[0];
#pragma unroll
      for (int i = 1; i < 8; ++i) l += sm[4 * i];
      if (w < 2) {
        const int u = st_id & 31;
        const LAS f32x4* red = (const LAS f32x4*)(L + L_RED) + hh * 32 + u;
        f32x4 o = red[0];
#pragma unroll
        for (int i = 1; i < 8; ++i) o += red[i * 128];
        const float inv = __builtin_amdgcn_rcpf(l);
        const v4u v = {pack_bf(o[0] * inv, o[1] * inv), tag, pack_bf(o[2] * inv, o[3] * inv), tag};
        const int off = WS_OP + (((b * HQ + 4 * j + hh) * 4 + s) * 64 + 4 * (u & 15) + 2 * (u >> 4)) * 8;
        __builtin_amdgcn_raw_buffer_store_b128(v, rs, off, 0, AUX_SC1);
      } else if (lane < 16) {
        const LAS float* mx = (const LAS float*)(L + L_MAX) + hh;
        float mm = mx[0];
#pragma unroll
        for (int i = 1; i < 8; ++i) mm = fmaxf(mm, mx[4 * i]);
        const float lse = mm * 1.4426950408889634f + __builtin_amdgcn_logf(l);
        __builtin_amdgcn_raw_buffer_store_b64(v2u{asu(lse), tag}, rs, WS_LS + ((b * HKV + j) * 4 + s) * 128 + lane * 8, 0, AUX_SC1);
      }
    }
    STAMP(7);

    if (is_wo) {
      const int h = 4 * j + (r >> 2), ns = r & 3;
      BAR();
      v4u wf[4];
      {
        const lds_p p = L + L_W + (8 * g + (n16 >> 2)) * 256 + (16 * w + 4 * (lane & 3)) * 2;
#pragma unroll
        for (int t = 0; t < 4; ++t) wf[t] = tr_frag(p + t * 32 * 256, 256);
      }
      const int doff = WS_OP + ((w * HQ + h) * 4 + (lane >> 5)) * 512 + (lane & 31) * 16;
      const int loff = WS_LS + ((w * HKV + j) * 4 + (lane & 3)) * 128 + (h & 3) * 8;
      v4u d0, d1; v2u lv;
      for (unsigned spins = 0;; ++spins) {
        asm volatile("" ::: "memory");
        d0 = __builtin_amdgcn_raw_buffer_load_b128(rs, doff, 0, AUX_SC1);
        d1 = __builtin_amdgcn_raw_buffer_load_b128(rs, doff + 1024, 0, AUX_SC1);
        lv = __builtin_amdgcn_raw_buffer_load_b64(rs, loff, 0, AUX_SC1);
        const bool ok = (d0.y == tag) & (d0.w == tag) & (d1.y == tag) & (d1.w == tag) & (lv.y == tag);
        if (all_ok(ok) || spins > SPIN_CAP) break;
      }
      STAMP(8);
      {
        const float lse = asf(lv.x);
        float mx = fmaxf(lse, dpp<0xB1>(lse));
        mx = fmaxf(mx, dpp<0x4E>(mx));
        const float e = __builtin_amdgcn_exp2f(lse - mx);
        float es = e + dpp<0xB1>(e);
        es += dpp<0x4E>(es);
        const float wt = e * __builtin_amdgcn_rcpf(es);
        const float w0 = dpp<0x00>(wt), w1 = dpp<0x55>(wt), w2 = dpp<0xAA>(wt), w3 = dpp<0xFF>(wt);
        const float wa = lane < 32 ? w0 : w1, wb = lane < 32 ? w2 : w3;
        float o0 = x32_sum(fmaf(wa, bf_lo(d0.x), wb * bf_lo(d1.x)));
        float o1 = x32_sum(fmaf(wa, bf_hi(d0.x), wb * bf_hi(d1.x)));
        float o2 = x32_sum(fmaf(wa, bf_lo(d0.z), wb * bf_lo(d1.z)));
        float o3 = x32_sum(fmaf(wa, bf_hi(d0.z), wb * bf_hi(d1.z)));
        if (lane < 32) *(LAS v2u*)(L + L_OS + w * 288 + lane * 8) = v2u{pack_bf(o0, o1), pack_bf(o2, o3)};
      }
      BAR();
      {
        const lds_p pa = L + L_OS + (lane & 7) * 288 + g * 16;
        f32x4 acc = {0.f, 0.f, 0.f, 0.f};
#pragma unroll
        for (int t = 0; t < 4; ++t) acc = mfma(lds_ld128(pa + t * 64), wf[t], acc);
        publish_tile(acc, rs, WS_RP + ((8 * ns + w) * 32 + h) * 128, 32 * 32 * 128, tag, lane);
      }
      STAMP(9);
      if (w == 0) {
        const int rho = 16 * j + r, bq = rho >> 4, cp = rho & 15;
        const unsigned xv = *(const unsigned*)(x + bq * D + 32 * cp + 2 * n16);
        const int off = WS_RP + ((bq * 32 + 2 * cp + ((lane >> 3) & 1)) * 32 + g) * 128 + (lane & 7) * 16;
        float a0, a1;
        for (unsigned spins = 0;; ++spins) {
          asm volatile("" ::: "memory");
          v4u v[8];
#pragma unroll
          for (int i = 0; i < 8; ++i) v[i] = __builtin_amdgcn_raw_buffer_load_b128(rs, off + i * 4 * 128, 0, AUX_SC1);
          bool ok = true;
          a0 = 0.f; a1 = 0.f;
#pragma unroll
          for (int i = 0; i < 8; ++i) { ok &= (v[i].y == tag) & (v[i].w == tag); a0 += asf(v[i].x); a1 += asf(v[i].z); }
          if (all_ok(ok) || spins > SPIN_CAP) break;
        }
        STAMP(10);
        a0 = x32_sum(x16_sum(a0)); a1 = x32_sum(x16_sum(a1));
        if (lane < 16) {
          const unsigned hv = pack_bf(bf_lo(xv) + rbf(a0), bf_hi(xv) + rbf(a1));
          __builtin_amdgcn_raw_buffer_store_b64(v2u{hv, tag}, rs, WS_GH + (bq * 256 + 16 * cp + lane) * 8, 0, AUX_SC1);
        }
        STAMP(11);
      } else if (w == 1) {
        final_cell(rs, tag, c, lane, out, (unsigned*)(ws + WS_EPOCH));
        STAMP(15);
      }
    } else if (is_mlp) {
      const int fg = 8 * j + (r - 16);
      const int kq = w >> 1, cth = w & 1;
      BAR();
      STAMP(8);
      v4u gf[4], uf[4], df[4];
      {
        const lds_p p = L + L_GT + (128 * kq + 8 * g + (n16 >> 2)) * 64 + (16 * cth + 4 * (lane & 3)) * 2;
#pragma unroll
        for (int t = 0; t < 4; ++t) { gf[t] = tr_frag(p + t * 32 * 64, 64); uf[t] = tr_frag(p + 32768 + t * 32 * 64, 64); }
        const lds_p pd = L + L_DN + (8 * g + (n16 >> 2)) * 1024 + (64 * w + 4 * (lane & 3)) * 2;
#pragma unroll
        for (int t = 0; t < 4; ++t) df[t] = tr_frag(pd + t * 32, 1024);
      }
      const int goff = WS_GH + (w * 256 + 2 * lane) * 8;
      v4u h0, h1;
      for (unsigned spins = 0;; ++spins) {
        asm volatile("" ::: "memory");
        h0 = __builtin_amdgcn_raw_buffer_load_b128(rs, goff, 0, AUX_SC1);
        h1 = __builtin_amdgcn_raw_buffer_load_b128(rs, goff + 1024, 0, AUX_SC1);
        const bool ok = (h0.y == tag) & (h0.w == tag) & (h1.y == tag) & (h1.w == tag);
        if (all_ok(ok) || spins > SPIN_CAP) break;
      }
      STAMP(9);
      {
        float f[8] = {bf_lo(h0.x), bf_hi(h0.x), bf_lo(h0.z), bf_hi(h0.z), bf_lo(h1.x), bf_hi(h1.x), bf_lo(h1.z), bf_hi(h1.z)};
        layernorm8(f);
        *(LAS v2u*)(L + L_HN + w * 1056 + lane * 8) = v2u{pack_bf(f[0], f[1]), pack_bf(f[2], f[3])};
        *(LAS v2u*)(L + L_HN + w * 1056 + 512 + lane * 8) = v2u{pack_bf(f[4], f[5]), pack_bf(f[6], f[7])};
      }
      BAR();
      {
        const lds_p pa = L + L_HN + (lane & 7) * 1056 + (128 * kq + 8 * g) * 2;
        f32x4 ag = {0.f, 0.f, 0.f, 0.f}, au = {0.f, 0.f, 0.f, 0.f};
#pragma unroll
        for (int t = 0; t < 4; ++t) {
          const v4u a = lds_ld128(pa + t * 64);
          ag = mfma(a, gf[t], ag);
          au = mfma(a, uf[t], au);
        }
        LAS float* r3 = (LAS float*)(L + L_R3) + (((kq * 2 + (g >> 1)) * 2 + cth) * 8 + 4 * (g & 1)) * 16 + n16;
#pragma unroll
        for (int i = 0; i < 4; ++i) r3[i * 16] = g >= 2 ? au[i] : ag[i];
      }
      BAR();
      if (w < 4) {
        const int bb = st_id >> 5, n32 = st_id & 31;
        const LAS float* r3 = (const LAS float*)(L + L_R3) + ((n32 >> 4) * 8 + bb) * 16 + (n32 & 15);
        float gg = 0.f, uu = 0.f;
#pragma unroll
        for (int k = 0; k < 4; ++k) { gg += r3[k * 512]; uu += r3[k * 512 + 256]; }
        gg = rbf(gg); uu = rbf(uu);
        const float sg = rbf(__builtin_amdgcn_rcpf(1.0f + __builtin_amdgcn_exp2f(-1.4426950408889634f * gg)));
        *(LAS unsigned short*)(L + L_HH + bb * 96 + n32 * 2) = (unsigned short)pack_bf(sg * uu, 0.f);
      }
      BAR();
      {
        const v4u a = lds_ld128(L + L_HH + (lane & 7) * 96 + g * 16);
#pragma unroll
        for (int t = 0; t < 4; ++t) {
          const f32x4 acc = mfma(a, df[t], f32x4{0.f, 0.f, 0.f, 0.f});
          publish_tile(acc, rs, WS_FP + ((4 * w + t) * 56 + fg) * 128, 32 * 56 * 128, tag, lane);
        }
      }
      STAMP(10);
      if (w == 7) {
        final_cell(rs, tag, c, lane, out, (unsigned*)(ws + WS_EPOCH));
        STAMP(15);
      }
    } else {
      if (w == 1) {
        final_cell(rs, tag, c, lane, out, (unsigned*)(ws + WS_EPOCH));
        STAMP(15);
      }
    }
  } else {
    const int hw = wv;
    __builtin_amdgcn_s_setprio(HPRIO);
    const int kq = r >> 3, ct8 = r & 7;
    v4u xa, xb; unsigned ep;
    {
      const unsigned xo = (2 * hw) * (D * 2) + lane * 16;
      asm volatile("global_load_dwordx4 %0, %1, %2" : "=v"(xa) : "v"(xo), "s"(x));
      asm volatile("global_load_dwordx4 %0, %1, %2 offset:1024" : "=v"(xb) : "v"(xo), "s"(x));
      const unsigned z = 0;
      asm volatile("global_load_dword %0, %1, %2 sc1" : "=v"(ep) : "v"(z), "s"(ws));
      const unsigned char* src = (const unsigned char*)wq + ((size_t)(128 * kq + 32 * hw + (lane >> 3)) * (HQ * DH) + 512 * j + 64 * ct8) * 2 + (lane & 7) * 16;
#pragma unroll
      for (int i = 0; i < 4; ++i) glds16(src + (size_t)i * 8 * (HQ * DH) * 2, lds0 + L_WQ + (32 * hw + 8 * i) * 128);
    }
    if (hw == 0 && lane < 4) flags[lane] = 0;
    BAR();
    asm volatile("s_waitcnt vmcnt(4)" : "+v"(xa), "+v"(xb), "+v"(ep));
    STAMP(1);
    const unsigned tag = ep + 1;
    {
      float fa[8] = {bf_lo(xa.x), bf_hi(xa.x), bf_lo(xa.y), bf_hi(xa.y), bf_lo(xa.z), bf_hi(xa.z), bf_lo(xa.w), bf_hi(xa.w)};
      float fb[8] = {bf_lo(xb.x), bf_hi(xb.x), bf_lo(xb.y), bf_hi(xb.y), bf_lo(xb.z), bf_hi(xb.z), bf_lo(xb.w), bf_hi(xb.w)};
      layernorm8(fa);
      layernorm8(fb);
      if (g == kq) {
        *(LAS v4u*)(L + L_XN + (2 * hw) * 288 + n16 * 16) = v4u{pack_bf(fa[0], fa[1]), pack_bf(fa[2], fa[3]), pack_bf(fa[4], fa[5]), pack_bf(fa[6], fa[7])};
        *(LAS v4u*)(L + L_XN + (2 * hw + 1) * 288 + n16 * 16) = v4u{pack_bf(fb[0], fb[1]), pack_bf(fb[2], fb[3]), pack_bf(fb[4], fb[5]), pack_bf(fb[6], fb[7])};
      }
    }
    asm volatile("s_waitcnt vmcnt(0) lgkmcnt(0)" ::: "memory");
    STAMP(2);
    if (lane == 0) __hip_atomic_fetch_add((LAS unsigned*)&flags[0], 1u, __ATOMIC_RELAXED, __HIP_MEMORY_SCOPE_WORKGROUP);
    while (flags[0] < 4u) __builtin_amdgcn_s_sleep(1);
    asm volatile("" ::: "memory");
    {
      const lds_p pa = L + L_XN + (lane & 7) * 288 + g * 16;
      const lds_p pb = L + L_WQ + (8 * g + (n16 >> 2)) * 128 + (16 * hw + 4 * (lane & 3)) * 2;
      f32x4 acc = {0.f, 0.f, 0.f, 0.f};
#pragma unroll
      for (int t = 0; t < 4; ++t) acc = mfma(lds_ld128(pa + t * 64), tr_frag(pb + t * 32 * 128, 128), acc);
      publish_tile(acc, rs, WS_QP + (kq * 8 * 4096 + 512 * j + 64 * ct8 + 16 * hw) * 8, 4096 * 8, tag, lane);
    }
    STAMP(3);
    __builtin_amdgcn_s_sleep(QSLEEP);
    {
      const int off = WS_QP + (b * 4096 + 512 * j + 128 * hw + 2 * lane) * 8;
      float q0, q1;
      for (unsigned spins = 0;; ++spins) {
        asm volatile("" ::: "memory");
        v4u v[4];
#pragma unroll
        for (int i = 0; i < 4; ++i) v[i] = __builtin_amdgcn_raw_buffer_load_b128(rs, off + i * 8 * 4096 * 8, 0, AUX_SC1);
        bool ok = true;
        q0 = 0.f; q1 = 0.f;
#pragma unroll
        for (int i = 0; i < 4; ++i) { ok &= (v[i].y == tag) & (v[i].w == tag); q0 += asf(v[i].x); q1 += asf(v[i].z); }
        if (all_ok(ok) || spins > SPIN_CAP) break;
      }
      *(LAS unsigned*)(L + L_Q + hw * 320 + lane * 4) = pack_bf(q0, q1);
      if (hw == 0 && lane == 0) flags[1] = tag;
    }
    STAMP(4);
    BAR();
    __builtin_amdgcn_s_setprio(0);
    if (is_wo) {
      const int h = 4 * j + (r >> 2), ns = r & 3;
      const unsigned char* src = (const unsigned char*)wo + ((size_t)(128 * h + 32 * hw + g) * D + 128 * ns) * 2 + n16 * 16;
#pragma unroll
      for (int i = 0; i < 8; ++i) glds16(src + (size_t)i * 4 * D * 2, lds0 + L_W + (32 * hw + 4 * i) * 256);
      STAMP(5);
      asm volatile("s_waitcnt vmcnt(0)" ::: "memory");
      STAMP(6);
      BAR();
    } else if (is_mlp) {
      const int fg = 8 * j + (r - 16);
      const size_t go = ((size_t)(128 * hw + (lane >> 2)) * FF + 32 * fg) * 2 + (lane & 3) * 16;
#pragma unroll
      for (int i = 0; i < 8; ++i) {
        if (i >= 2) asm volatile("s_waitcnt vmcnt(3)" ::: "memory");
        glds16((const unsigned char*)wg + go + (size_t)i * 16 * FF * 2, lds0 + L_GT + (128 * hw + 16 * i) * 64);
        if (i >= 2) asm volatile("s_waitcnt vmcnt(3)" ::: "memory");
        glds16((const unsigned char*)wu + go + (size_t)i * 16 * FF * 2, lds0 + L_UP + (128 * hw + 16 * i) * 64);
      }
      const unsigned char* sd = (const unsigned char*)wd + (size_t)(32 * fg + 8 * hw) * D * 2 + lane * 16;
#pragma unroll
      for (int i = 0; i < 8; ++i) {
        asm volatile("s_waitcnt vmcnt(5)" ::: "memory");
        glds16(sd + (size_t)i * D * 2, lds0 + L_DN + (8 * hw + i) * 1024);
      }
      STAMP(5);
      asm volatile("s_waitcnt vmcnt(0)" ::: "memory");
      STAMP(6);
      BAR();
    }
  }
}

}

extern "C" void kernel_launch(void* const* d_in, const int* in_sizes, int n_in, void* d_out, int out_size, void* d_ws,
                              size_t ws_size, hipStream_t stream) {
  (void)in_sizes; (void)n_in; (void)out_size; (void)ws_size;
  (void)hipFuncSetAttribute((const void*)fused_decode, hipFuncAttributeMaxDynamicSharedMemorySize, LDS_BYTES);
  fused_decode<<<256, NTHR, LDS_BYTES, stream>>>(
      (const unsigned short*)d_in[0], (const unsigned short*)d_in[1], (const unsigned short*)d_in[2], (const unsigned short*)d_in[3],
      (const unsigned short*)d_in[4], (const unsigned short*)d_in[6], (const unsigned short*)d_in[7], (const unsigned short*)d_in[8],
      (unsigned short*)d_out, (unsigned char*)d_ws);
}
```

```cpp
#include <hip/hip_runtime.h>
#include <hip/hip_bf16.h>
#include <stdint.h>

namespace {

typedef unsigned v4u __attribute__((ext_vector_type(4)));
typedef unsigned v2u __attribute__((ext_vector_type(2)));
typedef float f32x4 __attribute__((ext_vector_type(4)));
typedef __bf16 bf16x8 __attribute__((ext_vector_type(8)));
typedef short s16x4 __attribute__((ext_vector_type(4)));
typedef unsigned long long u64;
#define LAS __attribute__((address_space(3)))
typedef LAS unsigned char* lds_p;

constexpr int NB = 8, D = 512, HQ = 32, HKV = 8, SK = 4096, DH = 128, WIN = 1024, FF = 1792;
constexpr int NTHR = 768;
constexpr int AUX_SC1 = 16;
constexpr unsigned SPIN_CAP = 1u << 16;
#define PD 4
#define CODE_LINES 0
#define QSLEEP 16
#define HPRIO 2

constexpr int WS_EPOCH = 0;
constexpr int WS_QP = 1 << 16;
constexpr int WS_OP = WS_QP + (1 << 20);
constexpr int WS_LS = WS_OP + (1 << 19);
constexpr int WS_RP = WS_LS + (1 << 15);
constexpr int WS_GH = WS_RP + (1 << 20);
constexpr int WS_FP = WS_GH + (1 << 14);
constexpr int WS_END = WS_FP + 256 * 56 * 128;
constexpr int WS_STAMP = 64 << 20;

constexpr int L_WQ = 0;
constexpr int L_RED = 0;
constexpr int L_XN = 16384;
constexpr int L_Q = L_XN + 8 * 288;
constexpr int L_MAX = L_Q + 4 * 320;
constexpr int L_SUM = L_MAX + 128;
constexpr int L_FLAG = L_SUM + 128;
constexpr int L_OS = L_FLAG + 64;
constexpr int L_HN = L_OS + 8 * 288;
constexpr int L_R3 = L_HN + 8 * 1056;
constexpr int L_HH = L_R3 + 8192;
constexpr int L_W = ((L_HH + 8 * 96 + 255) / 256) * 256;
constexpr int L_GT = L_W, L_UP = L_W + 32768, L_DN = L_W + 65536;
constexpr int LDS_BYTES = L_W + 98304;
static_assert(LDS_BYTES <= 160 * 1024 && LDS_BYTES > 80 * 1024, "one workgroup per CU");

__device__ __forceinline__ float bf_lo(unsigned u) { return __builtin_bit_cast(float, u << 16); }
__device__ __forceinline__ float bf_hi(unsigned u) { return __builtin_bit_cast(float, u & 0xffff0000u); }
__device__ __forceinline__ unsigned pack_bf(float lo, float hi) {
  typedef __bf16 bf2 __attribute__((ext_vector_type(2)));
  bf2 r = {(__bf16)lo, (__bf16)hi};
  return __builtin_bit_cast(unsigned, r);
}
__device__ __forceinline__ float rbf(float f) { return bf_lo(pack_bf(f, f)); }
__device__ __forceinline__ float asf(unsigned u) { return __builtin_bit_cast(float, u); }
__device__ __forceinline__ unsigned asu(float f) { return __builtin_bit_cast(unsigned, f); }
template <int CTRL> __device__ __forceinline__ float dpp(float x) {
  return __builtin_bit_cast(float, __builtin_amdgcn_mov_dpp(__builtin_bit_cast(int, x), CTRL, 0xf, 0xf, true));
}
__device__ __forceinline__ float x16_sum(float x) {
  auto s = __builtin_amdgcn_permlane16_swap(asu(x), asu(x), false, false);
  return asf(s[0]) + asf(s[1]);
}
__device__ __forceinline__ float x32_sum(float x) {
  auto s = __builtin_amdgcn_permlane32_swap(asu(x), asu(x), false, false);
  return asf(s[0]) + asf(s[1]);
}
__device__ __forceinline__ float x16_max(float x) {
  auto s = __builtin_amdgcn_permlane16_swap(asu(x), asu(x), false, false);
  return fmaxf(asf(s[0]), asf(s[1]));
}
__device__ __forceinline__ float x32_max(float x) {
  auto s = __builtin_amdgcn_permlane32_swap(asu(x), asu(x), false, false);
  return fmaxf(asf(s[0]), asf(s[1]));
}
__device__ __forceinline__ float wave_sum(float v) {
  v += dpp<0xB1>(v);
  v += dpp<0x4E>(v);
  v += dpp<0x141>(v);
  v += dpp<0x140>(v);
  return x32_sum(x16_sum(v));
}
__device__ __forceinline__ f32x4 mfma(v4u a, v4u b, f32x4 c) {
  return __builtin_amdgcn_mfma_f32_16x16x32_bf16(__builtin_bit_cast(bf16x8, a), __builtin_bit_cast(bf16x8, b), c, 0, 0, 0);
}
__device__ __forceinline__ v2u tr_read(lds_p p) {
  typedef short v4s __attribute__((ext_vector_type(4)));
  return __builtin_bit_cast(v2u, __builtin_amdgcn_ds_read_tr16_b64_v4i16((LAS v4s*)p));
}
__device__ __forceinline__ v4u tr_frag(lds_p p, int row_stride) {
  const v2u a = tr_read(p), b = tr_read(p + 4 * row_stride);
  return v4u{a.x, a.y, b.x, b.y};
}
__device__ __forceinline__ v4u lds_ld128(lds_p p) { return *(const LAS v4u*)p; }
__device__ __forceinline__ void glds16(const void* gsrc, unsigned lds_dst) {
  unsigned keep;
  asm volatile("s_mov_b32 %0, m0\n\ts_mov_b32 m0, %2\n\ts_nop 0\n\tglobal_load_lds_dwordx4 %1, off nt\n\ts_mov_b32 m0, %0"
               : "=&s"(keep) : "v"(gsrc), "s"(lds_dst) : "memory");
}
#define BAR() do { asm volatile("s_waitcnt lgkmcnt(0)" ::: "memory"); __builtin_amdgcn_s_barrier(); asm volatile("" ::: "memory"); } while (0)
__device__ __forceinline__ void lds_arrive(volatile LAS unsigned* p, int lane) {
  asm volatile("" ::: "memory");
  if (lane == 0) __hip_atomic_fetch_add((LAS unsigned*)p, 1u, __ATOMIC_RELAXED, __HIP_MEMORY_SCOPE_WORKGROUP);
  asm volatile("" ::: "memory");
}
__device__ __forceinline__ void lds_wait(volatile LAS unsigned* p, unsigned n) {
  while (*p < n) { }
  asm volatile("" ::: "memory");
}
__device__ __forceinline__ bool all_ok(bool ok) { return __builtin_amdgcn_ballot_w64(ok) == ~0ull; }

__device__ __forceinline__ void layernorm8(float (&f)[8]) {
  float s = ((f[0] + f[1]) + (f[2] + f[3])) + ((f[4] + f[5]) + (f[6] + f[7]));
  const float mean = wave_sum(s) * (1.0f / D);
  float ss = 0.f;
#pragma unroll
  for (int i = 0; i < 8; ++i) { const float d = f[i] - mean; ss = fmaf(d, d, ss); }
  const float var = rbf(wave_sum(ss) * (1.0f / D));
  const float den = rbf(__builtin_sqrtf(rbf(var + 1e-5f)));
  const float inv = __builtin_amdgcn_rcpf(den), mb = rbf(mean);
#pragma unroll
  for (int i = 0; i < 8; ++i) f[i] = rbf(f[i] - mb) * inv;
}

__device__ __forceinline__ void publish_tile(f32x4 d, __amdgpu_buffer_rsrc_t rs, int base, int stride_b, unsigned tag, int lane) {
  const int n = lane & 15, gq = lane >> 4;
  const bool hi = gq >= 2, e = n & 1;
  const float a = hi ? d[2] : d[0], c = hi ? d[3] : d[1];
  const float recv = dpp<0xB1>(e ? a : c);
  const int bb = 4 * (gq & 1) + (hi ? 2 : 0) + (e ? 1 : 0);
  const v4u v = {asu(e ? recv : a), tag, asu(e ? c : recv), tag};
  __builtin_amdgcn_raw_buffer_store_b128(v, rs, base + bb * stride_b + (n >> 1) * 16, 0, AUX_SC1);
}

#define STAMP(i) do { } while (0)

__device__ __forceinline__ void final_cell(__amdgpu_buffer_rsrc_t rs, unsigned tag, int c, int lane, unsigned short* out, unsigned* ws_epoch) {
  const int bq = c >> 5, ct = c & 31, p = lane & 7;
  v2u hv;
  for (unsigned spins = 0;; ++spins) {
    asm volatile("" ::: "memory");
    hv = __builtin_amdgcn_raw_buffer_load_b64(rs, WS_GH + (bq * 256 + 8 * ct + p) * 8, 0, AUX_SC1);
    if (all_ok(hv.y == tag) || spins > SPIN_CAP) break;
    __builtin_amdgcn_s_sleep(2);
  }
  const int off = WS_FP + ((bq * 32 + ct) * 56 + (lane >> 3)) * 128 + p * 16;
  float a0, a1;
  for (unsigned spins = 0;; ++spins) {
    asm volatile("" ::: "memory");
    v4u v[7];
#pragma unroll
    for (int i = 0; i < 7; ++i) v[i] = __builtin_amdgcn_raw_buffer_load_b128(rs, off + i * 8 * 128, 0, AUX_SC1);
    bool ok = true;
    a0 = 0.f; a1 = 0.f;
#pragma unroll
    for (int i = 0; i < 7; ++i) { ok &= (v[i].y == tag) & (v[i].w == tag); a0 += asf(v[i].x); a1 += asf(v[i].z); }
    if (all_ok(ok) || spins > SPIN_CAP) break;
  }
  a0 += dpp<0x128>(a0); a1 += dpp<0x128>(a1);
  a0 = x32_sum(x16_sum(a0)); a1 = x32_sum(x16_sum(a1));
  if (lane < 8) {
    const unsigned o = pack_bf(bf_lo(hv.x) + rbf(a0), bf_hi(hv.x) + rbf(a1));
    *(unsigned*)(out + bq * D + 16 * ct + 2 * p) = o;
  }
  if (c == 0 && lane == 0) __hip_atomic_store(ws_epoch, tag, __ATOMIC_RELAXED, __HIP_MEMORY_SCOPE_AGENT);
}

__global__ void __launch_bounds__(NTHR, 3)
fused_decode(const unsigned short* __restrict__ x, const unsigned short* __restrict__ kc, const unsigned short* __restrict__ vc,
             const unsigned short* __restrict__ wq, const unsigned short* __restrict__ wo, const unsigned short* __restrict__ wg,
             const unsigned short* __restrict__ wu, const unsigned short* __restrict__ wd, unsigned short* __restrict__ out,
             unsigned char* __restrict__ ws) {
  unsigned long long code_base;
  asm volatile("s_getpc_b64 %0" : "=s"(code_base));
  code_base &= ~0xffull;
  extern __shared__ __attribute__((aligned(16))) unsigned char smem[];
  const lds_p L = (lds_p)smem;
  const unsigned lds0 = (unsigned)(size_t)L;
  const int tid = threadIdx.x, lane = tid & 63;
  const int wv = __builtin_amdgcn_readfirstlane(tid >> 6);
  const int w = wv - 4, st_id = tid - 256;
  const int c = blockIdx.x, j = c & 7, r = c >> 3;
  const int b = r >> 2, s = r & 3;
  const int g = lane >> 4, n16 = lane & 15;
  const bool is_wo = r < 16, is_mlp = (r >= 16) && (r < 24) && (j < 7);
  const __amdgpu_buffer_rsrc_t rs = __builtin_amdgcn_make_buffer_rsrc((void*)ws, 0, WS_END, 0x00020000);
  volatile LAS unsigned* flags = (volatile LAS unsigned*)(L + L_FLAG);
  STAMP(0);

  if (wv >= 4) {
    const size_t tile = ((size_t)(b * HKV + j) * SK + (SK - WIN) + 256 * s + 32 * w) * (DH * 2);
    const unsigned char* kb = (const unsigned char*)kc + tile + 4096;
    const unsigned char* vb = (const unsigned char*)vc + tile;
    const unsigned koff = (lane & 7) * 256 + (2 * g + ((lane >> 3) & 1)) * 16;
    const unsigned voff = (16 * (g >> 1) + 4 * (g & 1)) * 256 + n16 * 16;
    v4u K[8], V[8];
#define LDK(i, imm) asm volatile("global_load_dwordx4 %0, %1, %2 offset:" #imm " nt" : "=v"(K[i]) : "v"(koff), "s"(kb))
#define LDV(i, imm) asm volatile("global_load_dwordx4 %0, %1, %2 offset:" #imm " nt" : "=v"(V[i]) : "v"(voff), "s"(vb))
#define STR2(x) #x
#define STR(x) STR2(x)
#define THR(n) do { if (n >= PD) asm volatile("s_waitcnt vmcnt(%0)" :: "i"(PD - 1)); } while (0)
    LDK(0, -4096); THR(1); LDK(1, -3968); THR(2); LDK(2, -2048); THR(3); LDK(3, -1920);
    __builtin_amdgcn_s_barrier();
    THR(4); LDK(4, 0); THR(5); LDK(5, 128); THR(6); LDK(6, 2048); THR(7); LDK(7, 2176);
    THR(8); LDV(0, 0); THR(9); LDV(1, 256); THR(10); LDV(2, 512); THR(11); LDV(3, 768);
    THR(12); LDV(4, 2048); THR(13); LDV(5, 2304); THR(14); LDV(6, 2560); THR(15); LDV(7, 2816);
    STAMP(1);
    BAR();
    STAMP(2);
    const unsigned tag = flags[1];
    const lds_p qa = L + L_Q + (n16 & 3) * 320 + (2 * g + ((n16 >> 2) & 1)) * 16;
    const v4u q0 = lds_ld128(qa), q1 = lds_ld128(qa + 128);
    asm volatile("s_waitcnt vmcnt(8)" : "+v"(K[0]), "+v"(K[1]), "+v"(K[2]), "+v"(K[3]), "+v"(K[4]), "+v"(K[5]), "+v"(K[6]), "+v"(K[7]));
    STAMP(3);
    f32x4 sa[4];
#pragma unroll
    for (int o = 0; o < 4; ++o) {
      sa[o] = mfma(K[2 * o], q0, f32x4{0.f, 0.f, 0.f, 0.f});
      sa[o] = mfma(K[2 * o + 1], q1, sa[o]);
    }
    float sc[8];
#pragma unroll
    for (int p = 0; p < 2; ++p)
#pragma unroll
      for (int i = 0; i < 4; ++i) {
        auto sw = __builtin_amdgcn_permlane32_swap(asu(sa[p][i]), asu(sa[p + 2][i]), false, false);
        const float v = asf(sw[0]) + dpp<0x104>(asf(sw[1]));
        sc[4 * p + i] = rbf(rbf(v) * 0.08838834764831845f);
      }
    float m = fmaxf(fmaxf(fmaxf(sc[0], sc[1]), fmaxf(sc[2], sc[3])), fmaxf(fmaxf(sc[4], sc[5]), fmaxf(sc[6], sc[7])));
    m = x32_max(x16_max(m));
    if (lane < 4) ((LAS float*)(L + L_MAX))[w * 4 + lane] = m;
    lds_arrive(flags + 2, lane);
    lds_wait(flags + 2, 8);
    {
      const LAS float* mx = (const LAS float*)(L + L_MAX) + (n16 & 3);
      m = mx[0];
#pragma unroll
      for (int i = 1; i < 8; ++i) m = fmaxf(m, mx[4 * i]);
    }
    float pe[8], ls = 0.f;
#pragma unroll
    for (int i = 0; i < 8; ++i) { pe[i] = __builtin_amdgcn_exp2f((sc[i] - m) * 1.4426950408889634f); ls += pe[i]; }
    const v4u P = {pack_bf(pe[0], pe[1]), pack_bf(pe[2], pe[3]), pack_bf(pe[4], pe[5]), pack_bf(pe[6], pe[7])};
    ls = x32_sum(x16_sum(ls));
    if (lane < 4) ((LAS float*)(L + L_SUM))[w * 4 + lane] = ls;
    STAMP(4);
    v4u T[8];
#define XPOSE(n_)                                                                                         \
  _Pragma("unroll") for (int mm = 0; mm < 4; ++mm) {                                                      \
    T[2 * mm][n_] = __builtin_amdgcn_perm(V[2 * n_ + 1][mm], V[2 * n_][mm], 0x05040100u);                 \
    T[2 * mm + 1][n_] = __builtin_amdgcn_perm(V[2 * n_ + 1][mm], V[2 * n_][mm], 0x07060302u);             \
  }
    asm volatile("s_waitcnt vmcnt(6)" : "+v"(V[0]), "+v"(V[1])); XPOSE(0) __builtin_amdgcn_sched_barrier(0);
    asm volatile("s_waitcnt vmcnt(4)" : "+v"(V[2]), "+v"(V[3])); XPOSE(1) __builtin_amdgcn_sched_barrier(0);
    asm volatile("s_waitcnt vmcnt(2)" : "+v"(V[4]), "+v"(V[5])); XPOSE(2) __builtin_amdgcn_sched_barrier(0);
    asm volatile("s_waitcnt vmcnt(0)" : "+v"(V[6]), "+v"(V[7]));
    STAMP(5);
    XPOSE(3)
    f32x4 oa[8];
#pragma unroll
    for (int i = 0; i < 8; ++i) oa[i] = mfma(P, T[i], f32x4{0.f, 0.f, 0.f, 0.f});
    if (lane < 16) {
      LAS f32x4* red = (LAS f32x4*)(L + L_RED + w * 2048) + lane;
#pragma unroll
      for (int hh = 0; hh < 4; ++hh) {
        red[(hh * 2 + 0) * 16] = f32x4{oa[0][hh], oa[1][hh], oa[2][hh], oa[3][hh]};
        red[(hh * 2 + 1) * 16] = f32x4{oa[4][hh], oa[5][hh], oa[6][hh], oa[7][hh]};
      }
    }
    lds_arrive(flags + 3, lane);
    STAMP(6);
    if (w < 3) {
      lds_wait(flags + 3, 8);
      const int hh = w < 2 ? (st_id >> 5) : (lane & 3);
      const LAS float* sm = (const LAS float*)(L + L_SUM) + hh;
      float l = sm[0];
#pragma unroll
      for (int i = 1; i < 8; ++i) l += sm[4 * i];
      if (w < 2) {
        const int u = st_id & 31;
        const LAS f32x4* red = (const LAS f32x4*)(L + L_RED) + hh * 32 + u;
        f32x4 o = red[0];
#pragma unroll
        for (int i = 1; i < 8; ++i) o += red[i * 128];
        const float inv = __builtin_amdgcn_rcpf(l);
        const v4u v = {pack_bf(o[0] * inv, o[1] * inv), tag, pack_bf(o[2] * inv, o[3] * inv), tag};
        const int off = WS_OP + (((b * HQ + 4 * j + hh) * 4 + s) * 64 + 4 * (u & 15) + 2 * (u >> 4)) * 8;
        __builtin_amdgcn_raw_buffer_store_b128(v, rs, off, 0, AUX_SC1);
      } else if (lane < 16) {
        const LAS float* mx = (const LAS float*)(L + L_MAX) + hh;
        float mm = mx[0];
#pragma unroll
        for (int i = 1; i < 8; ++i) mm = fmaxf(mm, mx[4 * i]);
        const float lse = mm * 1.4426950408889634f + __builtin_amdgcn_logf(l);
        __builtin_amdgcn_raw_buffer_store_b64(v2u{asu(lse), tag}, rs, WS_LS + ((b * HKV + j) * 4 + s) * 128 + lane * 8, 0, AUX_SC1);
      }
    }
    STAMP(7);

    if (is_wo) {
      const int h = 4 * j + (r >> 2), ns = r & 3;
      BAR();
      v4u wf[4];
      {
        const lds_p p = L + L_W + (8 * g + (n16 >> 2)) * 256 + (16 * w + 4 * (lane & 3)) * 2;
#pragma unroll
        for (int t = 0; t < 4; ++t) wf[t] = tr_frag(p + t * 32 * 256, 256);
      }
      const int doff = WS_OP + ((w * HQ + h) * 4 + (lane >> 5)) * 512 + (lane & 31) * 16;
      const int loff = WS_LS + ((w * HKV + j) * 4 + (lane & 3)) * 128 + (h & 3) * 8;
      v4u d0, d1; v2u lv;
      for (unsigned spins = 0;; ++spins) {
        asm volatile("" ::: "memory");
        d0 = __builtin_amdgcn_raw_buffer_load_b128(rs, doff, 0, AUX_SC1);
        d1 = __builtin_amdgcn_raw_buffer_load_b128(rs, doff + 1024, 0, AUX_SC1);
        lv = __builtin_amdgcn_raw_buffer_load_b64(rs, loff, 0, AUX_SC1);
        const bool ok = (d0.y == tag) & (d0.w == tag) & (d1.y == tag) & (d1.w == tag) & (lv.y == tag);
        if (all_ok(ok) || spins > SPIN_CAP) break;
      }
      STAMP(8);
      {
        const float lse = asf(lv.x);
        float mx = fmaxf(lse, dpp<0xB1>(lse));
        mx = fmaxf(mx, dpp<0x4E>(mx));
        const float e = __builtin_amdgcn_exp2f(lse - mx);
        float es = e + dpp<0xB1>(e);
        es += dpp<0x4E>(es);
        const float wt = e * __builtin_amdgcn_rcpf(es);
        const float w0 = dpp<0x00>(wt), w1 = dpp<0x55>(wt), w2 = dpp<0xAA>(wt), w3 = dpp<0xFF>(wt);
        const float wa = lane < 32 ? w0 : w1, wb = lane < 32 ? w2 : w3;
        float o0 = x32_sum(fmaf(wa, bf_lo(d0.x), wb * bf_lo(d1.x)));
        float o1 = x32_sum(fmaf(wa, bf_hi(d0.x), wb * bf_hi(d1.x)));
        float o2 = x32_sum(fmaf(wa, bf_lo(d0.z), wb * bf_lo(d1.z)));
        float o3 = x32_sum(fmaf(wa, bf_hi(d0.z), wb * bf_hi(d1.z)));
        if (lane < 32) *(LAS v2u*)(L + L_OS + w * 288 + lane * 8) = v2u{pack_bf(o0, o1), pack_bf(o2, o3)};
      }
      BAR();
      {
        const lds_p pa = L + L_OS + (lane & 7) * 288 + g * 16;
        f32x4 acc = {0.f, 0.f, 0.f, 0.f};
#pragma unroll
        for (int t = 0; t < 4; ++t) acc = mfma(lds_ld128(pa + t * 64), wf[t], acc);
        publish_tile(acc, rs, WS_RP + ((8 * ns + w) * 32 + h) * 128, 32 * 32 * 128, tag, lane);
      }
      STAMP(9);
      if (w == 0) {
        const int rho = 16 * j + r, bq = rho >> 4, cp = rho & 15;
        const unsigned xv = *(const unsigned*)(x + bq * D + 32 * cp + 2 * n16);
        const int off = WS_RP + ((bq * 32 + 2 * cp + ((lane >> 3) & 1)) * 32 + g) * 128 + (lane & 7) * 16;
        float a0, a1;
        for (unsigned spins = 0;; ++spins) {
          asm volatile("" ::: "memory");
          v4u v[8];
#pragma unroll
          for (int i = 0; i < 8; ++i) v[i] = __builtin_amdgcn_raw_buffer_load_b128(rs, off + i * 4 * 128, 0, AUX_SC1);
          bool ok = true;
          a0 = 0.f; a1 = 0.f;
#pragma unroll
          for (int i = 0; i < 8; ++i) { ok &= (v[i].y == tag) & (v[i].w == tag); a0 += asf(v[i].x); a1 += asf(v[i].z); }
          if (all_ok(ok) || spins > SPIN_CAP) break;
        }
        STAMP(10);
        a0 = x32_sum(x16_sum(a0)); a1 = x32_sum(x16_sum(a1));
        if (lane < 16) {
          const unsigned hv = pack_bf(bf_lo(xv) + rbf(a0), bf_hi(xv) + rbf(a1));
          __builtin_amdgcn_raw_buffer_store_b64(v2u{hv, tag}, rs, WS_GH + (bq * 256 + 16 * cp + lane) * 8, 0, AUX_SC1);
        }
        STAMP(11);
      } else if (w == 1) {
        final_cell(rs, tag, c, lane, out, (unsigned*)(ws + WS_EPOCH));
        STAMP(15);
      }
    } else if (is_mlp) {
      const int fg = 8 * j + (r - 16);
      const int kq = w >> 1, cth = w & 1;
      BAR();
      STAMP(8);
      v4u gf[4], uf[4], df[4];
      {
        const lds_p p = L + L_GT + (128 * kq + 8 * g + (n16 >> 2)) * 64 + (16 * cth + 4 * (lane & 3)) * 2;
#pragma unroll
        for (int t = 0; t < 4; ++t) { gf[t] = tr_frag(p + t * 32 * 64, 64); uf[t] = tr_frag(p + 32768 + t * 32 * 64, 64); }
        const lds_p pd = L + L_DN + (8 * g + (n16 >> 2)) * 1024 + (64 * w + 4 * (lane & 3)) * 2;
#pragma unroll
        for (int t = 0; t < 4; ++t) df[t] = tr_frag(pd + t * 32, 1024);
      }
      const int goff = WS_GH + (w * 256 + 2 * lane) * 8;
      v4u h0, h1;
      for (unsigned spins = 0;; ++spins) {
        asm volatile("" ::: "memory");
        h0 = __builtin_amdgcn_raw_buffer_load_b128(rs, goff, 0, AUX_SC1);
        h1 = __builtin_amdgcn_raw_buffer_load_b128(rs, goff + 1024, 0, AUX_SC1);
        const bool ok = (h0.y == tag) & (h0.w == tag) & (h1.y == tag) & (h1.w == tag);
        if (all_ok(ok) || spins > SPIN_CAP) break;
      }
      STAMP(9);
      {
        float f[8] = {bf_lo(h0.x), bf_hi(h0.x), bf_lo(h0.z), bf_hi(h0.z), bf_lo(h1.x), bf_hi(h1.x), bf_lo(h1.z), bf_hi(h1.z)};
        layernorm8(f);
        *(LAS v2u*)(L + L_HN + w * 1056 + lane * 8) = v2u{pack_bf(f[0], f[1]), pack_bf(f[2], f[3])};
        *(LAS v2u*)(L + L_HN + w * 1056 + 512 + lane * 8) = v2u{pack_bf(f[4], f[5]), pack_bf(f[6], f[7])};
      }
      BAR();
      {
        const lds_p pa = L + L_HN + (lane & 7) * 1056 + (128 * kq + 8 * g) * 2;
        f32x4 ag = {0.f, 0.f, 0.f, 0.f}, au = {0.f, 0.f, 0.f, 0.f};
#pragma unroll
        for (int t = 0; t < 4; ++t) {
          const v4u a = lds_ld128(pa + t * 64);
          ag = mfma(a, gf[t], ag);
          au = mfma(a, uf[t], au);
        }
        LAS float* r3 = (LAS float*)(L + L_R3) + (((kq * 2 + (g >> 1)) * 2 + cth) * 8 + 4 * (g & 1)) * 16 + n16;
#pragma unroll
        for (int i = 0; i < 4; ++i) r3[i * 16] = g >= 2 ? au[i] : ag[i];
      }
      BAR();
      if (w < 4) {
        const int bb = st_id >> 5, n32 = st_id & 31;
        const LAS float* r3 = (const LAS float*)(L + L_R3) + ((n32 >> 4) * 8 + bb) * 16 + (n32 & 15);
        float gg = 0.f, uu = 0.f;
#pragma unroll
        for (int k = 0; k < 4; ++k) { gg += r3[k * 512]; uu += r3[k * 512 + 256]; }
        gg = rbf(gg); uu = rbf(uu);
        const float sg = rbf(__builtin_amdgcn_rcpf(1.0f + __builtin_amdgcn_exp2f(-1.4426950408889634f * gg)));
        *(LAS unsigned short*)(L + L_HH + bb * 96 + n32 * 2) = (unsigned short)pack_bf(sg * uu, 0.f);
      }
      BAR();
      {
        const v4u a = lds_ld128(L + L_HH + (lane & 7) * 96 + g * 16);
#pragma unroll
        for (int t = 0; t < 4; ++t) {
          const f32x4 acc = mfma(a, df[t], f32x4{0.f, 0.f, 0.f, 0.f});
          publish_tile(acc, rs, WS_FP + ((4 * w + t) * 56 + fg) * 128, 32 * 56 * 128, tag, lane);
        }
      }
      STAMP(10);
      if (w == 7) {
        final_cell(rs, tag, c, lane, out, (unsigned*)(ws + WS_EPOCH));
        STAMP(15);
      }
    } else {
      if (w == 1) {
        final_cell(rs, tag, c, lane, out, (unsigned*)(ws + WS_EPOCH));
        STAMP(15);
      }
    }
  } else {
    const int hw = wv;
    __builtin_amdgcn_s_setprio(HPRIO);
    const int kq = r >> 3, ct8 = r & 7;
    v4u xa, xb; unsigned ep, pf;
    {
      pf = 0;
      const unsigned xo = (2 * hw) * (D * 2) + lane * 16;
      asm volatile("global_load_dwordx4 %0, %1, %2" : "=v"(xa) : "v"(xo), "s"(x));
      asm volatile("global_load_dwordx4 %0, %1, %2 offset:1024" : "=v"(xb) : "v"(xo), "s"(x));
      const unsigned z = 0;
      asm volatile("global_load_dword %0, %1, %2 sc1" : "=v"(ep) : "v"(z), "s"(ws));
      const unsigned char* src = (const unsigned char*)wq + ((size_t)(128 * kq + 32 * hw + (lane >> 3)) * (HQ * DH) + 512 * j + 64 * ct8) * 2 + (lane & 7) * 16;
#pragma unroll
      for (int i = 0; i < 4; ++i) glds16(src + (size_t)i * 8 * (HQ * DH) * 2, lds0 + L_WQ + (32 * hw + 8 * i) * 128);
    }
    if (hw == 0 && lane < 4) flags[lane] = 0;
    BAR();
    asm volatile("s_waitcnt vmcnt(4)" : "+v"(xa), "+v"(xb), "+v"(ep), "+v"(pf));
    STAMP(1);
    const unsigned tag = ep + 1;
    {
      float fa[8] = {bf_lo(xa.x), bf_hi(xa.x), bf_lo(xa.y), bf_hi(xa.y), bf_lo(xa.z), bf_hi(xa.z), bf_lo(xa.w), bf_hi(xa.w)};
      float fb[8] = {bf_lo(xb.x), bf_hi(xb.x), bf_lo(xb.y), bf_hi(xb.y), bf_lo(xb.z), bf_hi(xb.z), bf_lo(xb.w), bf_hi(xb.w)};
      layernorm8(fa);
      layernorm8(fb);
      if (g == kq) {
        *(LAS v4u*)(L + L_XN + (2 * hw) * 288 + n16 * 16) = v4u{pack_bf(fa[0], fa[1]), pack_bf(fa[2], fa[3]), pack_bf(fa[4], fa[5]), pack_bf(fa[6], fa[7])};
        *(LAS v4u*)(L + L_XN + (2 * hw + 1) * 288 + n16 * 16) = v4u{pack_bf(fb[0], fb[1]), pack_bf(fb[2], fb[3]), pack_bf(fb[4], fb[5]), pack_bf(fb[6], fb[7])};
      }
    }
    asm volatile("s_waitcnt vmcnt(0) lgkmcnt(0)" ::: "memory");
    STAMP(2);
    if (lane == 0) __hip_atomic_fetch_add((LAS unsigned*)&flags[0], 1u, __ATOMIC_RELAXED, __HIP_MEMORY_SCOPE_WORKGROUP);
    while (flags[0] < 4u) __builtin_amdgcn_s_sleep(1);
    asm volatile("" ::: "memory");
    {
      const lds_p pa = L + L_XN + (lane & 7) * 288 + g * 16;
      const lds_p pb = L + L_WQ + (8 * g + (n16 >> 2)) * 128 + (16 * hw + 4 * (lane & 3)) * 2;
      f32x4 acc = {0.f, 0.f, 0.f, 0.f};
#pragma unroll
      for (int t = 0; t < 4; ++t) acc = mfma(lds_ld128(pa + t * 64), tr_frag(pb + t * 32 * 128, 128), acc);
      publish_tile(acc, rs, WS_QP + (kq * 8 * 4096 + 512 * j + 64 * ct8 + 16 * hw) * 8, 4096 * 8, tag, lane);
    }
    STAMP(3);
    __builtin_amdgcn_s_sleep(QSLEEP);
    {
      const int off = WS_QP + (b * 4096 + 512 * j + 128 * hw + 2 * lane) * 8;
      float q0, q1;
      for (unsigned spins = 0;; ++spins) {
        asm volatile("" ::: "memory");
        v4u v[4];
#pragma unroll
        for (int i = 0; i < 4; ++i) v[i] = __builtin_amdgcn_raw_buffer_load_b128(rs, off + i * 8 * 4096 * 8, 0, AUX_SC1);
        bool ok = true;
        q0 = 0.f; q1 = 0.f;
#pragma unroll
        for (int i = 0; i < 4; ++i) { ok &= (v[i].y == tag) & (v[i].w == tag); q0 += asf(v[i].x); q1 += asf(v[i].z); }
        if (all_ok(ok) || spins > SPIN_CAP) break;
      }
      *(LAS unsigned*)(L + L_Q + hw * 320 + lane * 4) = pack_bf(q0, q1);
      if (hw == 0 && lane == 0) flags[1] = tag;
    }
    STAMP(4);
    BAR();
    __builtin_amdgcn_s_setprio(0);
    if (is_wo) {
      const int h = 4 * j + (r >> 2), ns = r & 3;
      const unsigned char* src = (const unsigned char*)wo + ((size_t)(128 * h + 32 * hw + g) * D + 128 * ns) * 2 + n16 * 16;
#pragma unroll
      for (int i = 0; i < 8; ++i) glds16(src + (size_t)i * 4 * D * 2, lds0 + L_W + (32 * hw + 4 * i) * 256);
      STAMP(5);
      asm volatile("s_waitcnt vmcnt(0)" ::: "memory");
      STAMP(6);
      BAR();
    } else if (is_mlp) {
      const int fg = 8 * j + (r - 16);
      const size_t go = ((size_t)(128 * hw + (lane >> 2)) * FF + 32 * fg) * 2 + (lane & 3) * 16;
#pragma unroll
      for (int i = 0; i < 8; ++i) {
        if (i >= 2) asm volatile("s_waitcnt vmcnt(3)" ::: "memory");
        glds16((const unsigned char*)wg + go + (size_t)i * 16 * FF * 2, lds0 + L_GT + (128 * hw + 16 * i) * 64);
        if (i >= 2) asm volatile("s_waitcnt vmcnt(3)" ::: "memory");
        glds16((const unsigned char*)wu + go + (size_t)i * 16 * FF * 2, lds0 + L_UP + (128 * hw + 16 * i) * 64);
      }
      const unsigned char* sd = (const unsigned char*)wd + (size_t)(32 * fg + 8 * hw) * D * 2 + lane * 16;
#pragma unroll
      for (int i = 0; i < 8; ++i) {
        asm volatile("s_waitcnt vmcnt(5)" ::: "memory");
        glds16(sd + (size_t)i * D * 2, lds0 + L_DN + (8 * hw + i) * 1024);
      }
      STAMP(5);
      asm volatile("s_waitcnt vmcnt(0)" ::: "memory");
      STAMP(6);
      BAR();
    }
  }
}

}

extern "C" void kernel_launch(void* const* d_in, const int* in_sizes, int n_in, void* d_out, int out_size, void* d_ws,
                              size_t ws_size, hipStream_t stream) {
  (void)in_sizes; (void)n_in; (void)out_size; (void)ws_size;
  (void)hipFuncSetAttribute((const void*)fused_decode, hipFuncAttributeMaxDynamicSharedMemorySize, LDS_BYTES);
  fused_decode<<<256, NTHR, LDS_BYTES, stream>>>(
      (const unsigned short*)d_in[0], (const unsigned short*)d_in[1], (const unsigned short*)d_in[2], (const unsigned short*)d_in[3],
      (const unsigned short*)d_in[4], (const unsigned short*)d_in[6], (const unsigned short*)d_in[7], (const unsigned short*)d_in[8],
      (unsigned short*)d_out, (unsigned char*)d_ws);
}
```

```cpp
#include <hip/hip_runtime.h>
#include <hip/hip_bf16.h>
#include <stdint.h>

namespace {

typedef unsigned v4u __attribute__((ext_vector_type(4)));
typedef unsigned v2u __attribute__((ext_vector_type(2)));
typedef float f32x4 __attribute__((ext_vector_type(4)));
typedef __bf16 bf16x8 __attribute__((ext_vector_type(8)));
typedef short s16x4 __attribute__((ext_vector_type(4)));
typedef unsigned long long u64;
#define LAS __attribute__((address_space(3)))
typedef LAS unsigned char* lds_p;

constexpr int NB = 8, D = 512, HQ = 32, HKV = 8, SK = 4096, DH = 128, WIN = 1024, FF = 1792;
constexpr int NTHR = 768;
constexpr int AUX_SC1 = 16;
constexpr unsigned SPIN_CAP = 1u << 16;
#define PD 4
#define CODE_LINES 126
#define PRETOUCH 1
#define QSLEEP 16
#define HPRIO 2

constexpr int WS_EPOCH = 0;
constexpr int WS_QP = 1 << 16;
constexpr int WS_OP = WS_QP + (1 << 20);
constexpr int WS_LS = WS_OP + (1 << 19);
constexpr int WS_RP = WS_LS + (1 << 15);
constexpr int WS_GH = WS_RP + (1 << 20);
constexpr int WS_FP = WS_GH + (1 << 14);
constexpr int WS_END = WS_FP + 256 * 56 * 128;
constexpr int WS_STAMP = 64 << 20;

constexpr int L_WQ = 0;
constexpr int L_RED = 0;
constexpr int L_XN = 16384;
constexpr int L_Q = L_XN + 8 * 288;
constexpr int L_MAX = L_Q + 4 * 320;
constexpr int L_SUM = L_MAX + 128;
constexpr int L_FLAG = L_SUM + 128;
constexpr int L_OS = L_FLAG + 64;
constexpr int L_HN = L_OS + 8 * 288;
constexpr int L_R3 = L_HN + 8 * 1056;
constexpr int L_HH = L_R3 + 8192;
constexpr int L_W = ((L_HH + 8 * 96 + 255) / 256) * 256;
constexpr int L_GT = L_W, L_UP = L_W + 32768, L_DN = L_W + 65536;
constexpr int LDS_BYTES = L_W + 98304;
static_assert(LDS_BYTES <= 160 * 1024 && LDS_BYTES > 80 * 1024, "one workgroup per CU");

__device__ __forceinline__ float bf_lo(unsigned u) { return __builtin_bit_cast(float, u << 16); }
__device__ __forceinline__ float bf_hi(unsigned u) { return __builtin_bit_cast(float, u & 0xffff0000u); }
__device__ __forceinline__ unsigned pack_bf(float lo, float hi) {
  typedef __bf16 bf2 __attribute__((ext_vector_type(2)));
  bf2 r = {(__bf16)lo, (__bf16)hi};
  return __builtin_bit_cast(unsigned, r);
}
__device__ __forceinline__ float rbf(float f) { return bf_lo(pack_bf(f, f)); }
__device__ __forceinline__ float asf(unsigned u) { return __builtin_bit_cast(float, u); }
__device__ __forceinline__ unsigned asu(float f) { return __builtin_bit_cast(unsigned, f); }
template <int CTRL> __device__ __forceinline__ float dpp(float x) {
  return __builtin_bit_cast(float, __builtin_amdgcn_mov_dpp(__builtin_bit_cast(int, x), CTRL, 0xf, 0xf, true));
}
__device__ __forceinline__ float x16_sum(float x) {
  auto s = __builtin_amdgcn_permlane16_swap(asu(x), asu(x), false, false);
  return asf(s[0]) + asf(s[1]);
}
__device__ __forceinline__ float x32_sum(float x) {
  auto s = __builtin_amdgcn_permlane32_swap(asu(x), asu(x), false, false);
  return asf(s[0]) + asf(s[1]);
}
__device__ __forceinline__ float x16_max(float x) {
  auto s = __builtin_amdgcn_permlane16_swap(asu(x), asu(x), false, false);
  return fmaxf(asf(s[0]), asf(s[1]));
}
__device__ __forceinline__ float x32_max(float x) {
  auto s = __builtin_amdgcn_permlane32_swap(asu(x), asu(x), false, false);
  return fmaxf(asf(s[0]), asf(s[1]));
}
__device__ __forceinline__ float wave_sum(float v) {
  v += dpp<0xB1>(v);
  v += dpp<0x4E>(v);
  v += dpp<0x141>(v);
  v += dpp<0x140>(v);
  return x32_sum(x16_sum(v));
}
__device__ __forceinline__ f32x4 mfma(v4u a, v4u b, f32x4 c) {
  return __builtin_amdgcn_mfma_f32_16x16x32_bf16(__builtin_bit_cast(bf16x8, a), __builtin_bit_cast(bf16x8, b), c, 0, 0, 0);
}
__device__ __forceinline__ v2u tr_read(lds_p p) {
  typedef short v4s __attribute__((ext_vector_type(4)));
  return __builtin_bit_cast(v2u, __builtin_amdgcn_ds_read_tr16_b64_v4i16((LAS v4s*)p));
}
__device__ __forceinline__ v4u tr_frag(lds_p p, int row_stride) {
  const v2u a = tr_read(p), b = tr_read(p + 4 * row_stride);
  return v4u{a.x, a.y, b.x, b.y};
}
__device__ __forceinline__ v4u lds_ld128(lds_p p) { return *(const LAS v4u*)p; }
__device__ __forceinline__ void glds16(const void* gsrc, unsigned lds_dst) {
  unsigned keep;
  asm volatile("s_mov_b32 %0, m0\n\ts_mov_b32 m0, %2\n\ts_nop 0\n\tglobal_load_lds_dwordx4 %1, off nt\n\ts_mov_b32 m0, %0"
               : "=&s"(keep) : "v"(gsrc), "s"(lds_dst) : "memory");
}
#define BAR() do { asm volatile("s_waitcnt lgkmcnt(0)" ::: "memory"); __builtin_amdgcn_s_barrier(); asm volatile("" ::: "memory"); } while (0)
__device__ __forceinline__ void lds_arrive(volatile LAS unsigned* p, int lane) {
  asm volatile("" ::: "memory");
  if (lane == 0) __hip_atomic_fetch_add((LAS unsigned*)p, 1u, __ATOMIC_RELAXED, __HIP_MEMORY_SCOPE_WORKGROUP);
  asm volatile("" ::: "memory");
}
__device__ __forceinline__ void lds_wait(volatile LAS unsigned* p, unsigned n) {
  while (*p < n) { }
  asm volatile("" ::: "memory");
}
__device__ __forceinline__ bool all_ok(bool ok) { return __builtin_amdgcn_ballot_w64(ok) == ~0ull; }

__device__ __forceinline__ void layernorm8(float (&f)[8]) {
  float s = ((f[0] + f[1]) + (f[2] + f[3])) + ((f[4] + f[5]) + (f[6] + f[7]));
  const float mean = wave_sum(s) * (1.0f / D);
  float ss = 0.f;
#pragma unroll
  for (int i = 0; i < 8; ++i) { const float d = f[i] - mean; ss = fmaf(d, d, ss); }
  const float var = rbf(wave_sum(ss) * (1.0f / D));
  const float den = rbf(__builtin_sqrtf(rbf(var + 1e-5f)));
  const float inv = __builtin_amdgcn_rcpf(den), mb = rbf(mean);
#pragma unroll
  for (int i = 0; i < 8; ++i) f[i] = rbf(f[i] - mb) * inv;
}

__device__ __forceinline__ void publish_tile(f32x4 d, __amdgpu_buffer_rsrc_t rs, int base, int stride_b, unsigned tag, int lane) {
  const int n = lane & 15, gq = lane >> 4;
  const bool hi = gq >= 2, e = n & 1;
  const float a = hi ? d[2] : d[0], c = hi ? d[3] : d[1];
  const float recv = dpp<0xB1>(e ? a : c);
  const int bb = 4 * (gq & 1) + (hi ? 2 : 0) + (e ? 1 : 0);
  const v4u v = {asu(e ? recv : a), tag, asu(e ? c : recv), tag};
  __builtin_amdgcn_raw_buffer_store_b128(v, rs, base + bb * stride_b + (n >> 1) * 16, 0, AUX_SC1);
}

#define STAMP(i) do { } while (0)

__device__ __forceinline__ void final_cell(__amdgpu_buffer_rsrc_t rs, unsigned tag, int c, int lane, unsigned short* out, unsigned* ws_epoch) {
  const int bq = c >> 5, ct = c & 31, p = lane & 7;
  v2u hv;
  for (unsigned spins = 0;; ++spins) {
    asm volatile("" ::: "memory");
    hv = __builtin_amdgcn_raw_buffer_load_b64(rs, WS_GH + (bq * 256 + 8 * ct + p) * 8, 0, AUX_SC1);
    if (all_ok(hv.y == tag) || spins > SPIN_CAP) break;
    __builtin_amdgcn_s_sleep(2);
  }
  const int off = WS_FP + ((bq * 32 + ct) * 56 + (lane >> 3)) * 128 + p * 16;
  float a0, a1;
  for (unsigned spins = 0;; ++spins) {
    asm volatile("" ::: "memory");
    v4u v[7];
#pragma unroll
    for (int i = 0; i < 7; ++i) v[i] = __builtin_amdgcn_raw_buffer_load_b128(rs, off + i * 8 * 128, 0, AUX_SC1);
    bool ok = true;
    a0 = 0.f; a1 = 0.f;
#pragma unroll
    for (int i = 0; i < 7; ++i) { ok &= (v[i].y == tag) & (v[i].w == tag); a0 += asf(v[i].x); a1 += asf(v[i].z); }
    if (all_ok(ok) || spins > SPIN_CAP) break;
  }
  a0 += dpp<0x128>(a0); a1 += dpp<0x128>(a1);
  a0 = x32_sum(x16_sum(a0)); a1 = x32_sum(x16_sum(a1));
  if (lane < 8) {
    const unsigned o = pack_bf(bf_lo(hv.x) + rbf(a0), bf_hi(hv.x) + rbf(a1));
    *(unsigned*)(out + bq * D + 16 * ct + 2 * p) = o;
  }
  if (c == 0 && lane == 0) __hip_atomic_store(ws_epoch, tag, __ATOMIC_RELAXED, __HIP_MEMORY_SCOPE_AGENT);
}

__global__ void __launch_bounds__(NTHR, 3)
fused_decode(const unsigned short* __restrict__ x, const unsigned short* __restrict__ kc, const unsigned short* __restrict__ vc,
             const unsigned short* __restrict__ wq, const unsigned short* __restrict__ wo, const unsigned short* __restrict__ wg,
             const unsigned short* __restrict__ wu, const unsigned short* __restrict__ wd, unsigned short* __restrict__ out,
             unsigned char* __restrict__ ws) {
  unsigned long long code_base;
  asm volatile("s_getpc_b64 %0" : "=s"(code_base));
  code_base &= ~0xffull;
  extern __shared__ __attribute__((aligned(16))) unsigned char smem[];
  const lds_p L = (lds_p)smem;
  const unsigned lds0 = (unsigned)(size_t)L;
  const int tid = threadIdx.x, lane = tid & 63;
  const int wv = __builtin_amdgcn_readfirstlane(tid >> 6);
  const int w = wv - 4, st_id = tid - 256;
  const int c = blockIdx.x, j = c & 7, r = c >> 3;
  const int b = r >> 2, s = r & 3;
  const int g = lane >> 4, n16 = lane & 15;
  const bool is_wo = r < 16, is_mlp = (r >= 16) && (r < 24) && (j < 7);
  const __amdgpu_buffer_rsrc_t rs = __builtin_amdgcn_make_buffer_rsrc((void*)ws, 0, WS_END, 0x00020000);
  volatile LAS unsigned* flags = (volatile LAS unsigned*)(L + L_FLAG);
  STAMP(0);

  if (wv >= 4) {
    const size_t tile = ((size_t)(b * HKV + j) * SK + (SK - WIN) + 256 * s + 32 * w) * (DH * 2);
    const unsigned char* kb = (const unsigned char*)kc + tile + 4096;
    const unsigned char* vb = (const unsigned char*)vc + tile;
    const unsigned koff = (lane & 7) * 256 + (2 * g + ((lane >> 3) & 1)) * 16;
    const unsigned voff = (16 * (g >> 1) + 4 * (g & 1)) * 256 + n16 * 16;
    v4u K[8], V[8];
#define LDK(i, imm) asm volatile("global_load_dwordx4 %0, %1, %2 offset:" #imm " nt" : "=v"(K[i]) : "v"(koff), "s"(kb))
#define LDV(i, imm) asm volatile("global_load_dwordx4 %0, %1, %2 offset:" #imm " nt" : "=v"(V[i]) : "v"(voff), "s"(vb))
#define STR2(x) #x
#define STR(x) STR2(x)
#define THR(n) do { if (n >= PD) asm volatile("s_waitcnt vmcnt(%0)" :: "i"(PD - 1)); } while (0)
    LDK(0, -4096); THR(1); LDK(1, -3968); THR(2); LDK(2, -2048); THR(3); LDK(3, -1920);
    __builtin_amdgcn_s_barrier();
    THR(4); LDK(4, 0); THR(5); LDK(5, 128); THR(6); LDK(6, 2048); THR(7); LDK(7, 2176);
    THR(8); LDV(0, 0); THR(9); LDV(1, 256); THR(10); LDV(2, 512); THR(11); LDV(3, 768);
    THR(12); LDV(4, 2048); THR(13); LDV(5, 2304); THR(14); LDV(6, 2560); THR(15); LDV(7, 2816);
    STAMP(1);
    BAR();
    STAMP(2);
    const unsigned tag = flags[1];
    const lds_p qa = L + L_Q + (n16 & 3) * 320 + (2 * g + ((n16 >> 2) & 1)) * 16;
    const v4u q0 = lds_ld128(qa), q1 = lds_ld128(qa + 128);
    asm volatile("s_waitcnt vmcnt(8)" : "+v"(K[0]), "+v"(K[1]), "+v"(K[2]), "+v"(K[3]), "+v"(K[4]), "+v"(K[5]), "+v"(K[6]), "+v"(K[7]));
    STAMP(3);
    f32x4 sa[4];
#pragma unroll
    for (int o = 0; o < 4; ++o) {
      sa[o] = mfma(K[2 * o], q0, f32x4{0.f, 0.f, 0.f, 0.f});
      sa[o] = mfma(K[2 * o + 1], q1, sa[o]);
    }
    float sc[8];
#pragma unroll
    for (int p = 0; p < 2; ++p)
#pragma unroll
      for (int i = 0; i < 4; ++i) {
        auto sw = __builtin_amdgcn_permlane32_swap(asu(sa[p][i]), asu(sa[p + 2][i]), false, false);
        const float v = asf(sw[0]) + dpp<0x104>(asf(sw[1]));
        sc[4 * p + i] = rbf(rbf(v) * 0.08838834764831845f);
      }
    float m = fmaxf(fmaxf(fmaxf(sc[0], sc[1]), fmaxf(sc[2], sc[3])), fmaxf(fmaxf(sc[4], sc[5]), fmaxf(sc[6], sc[7])));
    m = x32_max(x16_max(m));
    if (lane < 4) ((LAS float*)(L + L_MAX))[w * 4 + lane] = m;
    lds_arrive(flags + 2, lane);
    lds_wait(flags + 2, 8);
    {
      const LAS float* mx = (const LAS float*)(L + L_MAX) + (n16 & 3);
      m = mx[0];
#pragma unroll
      for (int i = 1; i < 8; ++i) m = fmaxf(m, mx[4 * i]);
    }
    float pe[8], ls = 0.f;
#pragma unroll
    for (int i = 0; i < 8; ++i) { pe[i] = __builtin_amdgcn_exp2f((sc[i] - m) * 1.4426950408889634f); ls += pe[i]; }
    const v4u P = {pack_bf(pe[0], pe[1]), pack_bf(pe[2], pe[3]), pack_bf(pe[4], pe[5]), pack_bf(pe[6], pe[7])};
    ls = x32_sum(x16_sum(ls));
    if (lane < 4) ((LAS float*)(L + L_SUM))[w * 4 + lane] = ls;
    STAMP(4);
    v4u T[8];
#define XPOSE(n_)                                                                                         \
  _Pragma("unroll") for (int mm = 0; mm < 4; ++mm) {                                                      \
    T[2 * mm][n_] = __builtin_amdgcn_perm(V[2 * n_ + 1][mm], V[2 * n_][mm], 0x05040100u);                 \
    T[2 * mm + 1][n_] = __builtin_amdgcn_perm(V[2 * n_ + 1][mm], V[2 * n_][mm], 0x07060302u);             \
  }
    asm volatile("s_waitcnt vmcnt(6)" : "+v"(V[0]), "+v"(V[1])); XPOSE(0) __builtin_amdgcn_sched_barrier(0);
    asm volatile("s_waitcnt vmcnt(4)" : "+v"(V[2]), "+v"(V[3])); XPOSE(1) __builtin_amdgcn_sched_barrier(0);
    asm volatile("s_waitcnt vmcnt(2)" : "+v"(V[4]), "+v"(V[5])); XPOSE(2) __builtin_amdgcn_sched_barrier(0);
    asm volatile("s_waitcnt vmcnt(0)" : "+v"(V[6]), "+v"(V[7]));
    STAMP(5);
    XPOSE(3)
    f32x4 oa[8];
#pragma unroll
    for (int i = 0; i < 8; ++i) oa[i] = mfma(P, T[i], f32x4{0.f, 0.f, 0.f, 0.f});
    if (lane < 16) {
      LAS f32x4* red = (LAS f32x4*)(L + L_RED + w * 2048) + lane;
#pragma unroll
      for (int hh = 0; hh < 4; ++hh) {
        red[(hh * 2 + 0) * 16] = f32x4{oa[0][hh], oa[1][hh], oa[2][hh], oa[3][hh]};
        red[(hh * 2 + 1) * 16] = f32x4{oa[4][hh], oa[5][hh], oa[6][hh], oa[7][hh]};
      }
    }
    lds_arrive(flags + 3, lane);
    STAMP(6);
    if (w < 3) {
      lds_wait(flags + 3, 8);
      const int hh = w < 2 ? (st_id >> 5) : (lane & 3);
      const LAS float* sm = (const LAS float*)(L + L_SUM) + hh;
      float l = sm[0];
#pragma unroll
      for (int i = 1; i < 8; ++i) l += sm[4 * i];
      if (w < 2) {
        const int u = st_id & 31;
        const LAS f32x4* red = (const LAS f32x4*)(L + L_RED) + hh * 32 + u;
        f32x4 o = red[0];
#pragma unroll
        for (int i = 1; i < 8; ++i) o += red[i * 128];
        const float inv = __builtin_amdgcn_rcpf(l);
        const v4u v = {pack_bf(o[0] * inv, o[1] * inv), tag, pack_bf(o[2] * inv, o[3] * inv), tag};
        const int off = WS_OP + (((b * HQ + 4 * j + hh) * 4 + s) * 64 + 4 * (u & 15) + 2 * (u >> 4)) * 8;
        __builtin_amdgcn_raw_buffer_store_b128(v, rs, off, 0, AUX_SC1);
      } else if (lane < 16) {
        const LAS float* mx = (const LAS float*)(L + L_MAX) + hh;
        float mm = mx[0];
#pragma unroll
        for (int i = 1; i < 8; ++i) mm = fmaxf(mm, mx[4 * i]);
        const float lse = mm * 1.4426950408889634f + __builtin_amdgcn_logf(l);
        __builtin_amdgcn_raw_buffer_store_b64(v2u{asu(lse), tag}, rs, WS_LS + ((b * HKV + j) * 4 + s) * 128 + lane * 8, 0, AUX_SC1);
      }
    }
    STAMP(7);

    if (is_wo) {
      const int h = 4 * j + (r >> 2), ns = r & 3;
      BAR();
      v4u wf[4];
      {
        const lds_p p = L + L_W + (8 * g + (n16 >> 2)) * 256 + (16 * w + 4 * (lane & 3)) * 2;
#pragma unroll
        for (int t = 0; t < 4; ++t) wf[t] = tr_frag(p + t * 32 * 256, 256);
      }
      const int doff = WS_OP + ((w * HQ + h) * 4 + (lane >> 5)) * 512 + (lane & 31) * 16;
      const int loff = WS_LS + ((w * HKV + j) * 4 + (lane & 3)) * 128 + (h & 3) * 8;
      v4u d0, d1; v2u lv;
      for (unsigned spins = 0;; ++spins) {
        asm volatile("" ::: "memory");
        d0 = __builtin_amdgcn_raw_buffer_load_b128(rs, doff, 0, AUX_SC1);
        d1 = __builtin_amdgcn_raw_buffer_load_b128(rs, doff + 1024, 0, AUX_SC1);
        lv = __builtin_amdgcn_raw_buffer_load_b64(rs, loff, 0, AUX_SC1);
        const bool ok = (d0.y == tag) & (d0.w == tag) & (d1.y == tag) & (d1.w == tag) & (lv.y == tag);
        if (all_ok(ok) || spins > SPIN_CAP) break;
      }
      STAMP(8);
      {
        const float lse = asf(lv.x);
        float mx = fmaxf(lse, dpp<0xB1>(lse));
        mx = fmaxf(mx, dpp<0x4E>(mx));
        const float e = __builtin_amdgcn_exp2f(lse - mx);
        float es = e + dpp<0xB1>(e);
        es += dpp<0x4E>(es);
        const float wt = e * __builtin_amdgcn_rcpf(es);
        const float w0 = dpp<0x00>(wt), w1 = dpp<0x55>(wt), w2 = dpp<0xAA>(wt), w3 = dpp<0xFF>(wt);
        const float wa = lane < 32 ? w0 : w1, wb = lane < 32 ? w2 : w3;
        float o0 = x32_sum(fmaf(wa, bf_lo(d0.x), wb * bf_lo(d1.x)));
        float o1 = x32_sum(fmaf(wa, bf_hi(d0.x), wb * bf_hi(d1.x)));
        float o2 = x32_sum(fmaf(wa, bf_lo(d0.z), wb * bf_lo(d1.z)));
        float o3 = x32_sum(fmaf(wa, bf_hi(d0.z), wb * bf_hi(d1.z)));
        if (lane < 32) *(LAS v2u*)(L + L_OS + w * 288 + lane * 8) = v2u{pack_bf(o0, o1), pack_bf(o2, o3)};
      }
      BAR();
      {
        const lds_p pa = L + L_OS + (lane & 7) * 288 + g * 16;
        f32x4 acc = {0.f, 0.f, 0.f, 0.f};
#pragma unroll
        for (int t = 0; t < 4; ++t) acc = mfma(lds_ld128(pa + t * 64), wf[t], acc);
        publish_tile(acc, rs, WS_RP + ((8 * ns + w) * 32 + h) * 128, 32 * 32 * 128, tag, lane);
      }
      STAMP(9);
      if (w == 0) {
        const int rho = 16 * j + r, bq = rho >> 4, cp = rho & 15;
        const unsigned xv = *(const unsigned*)(x + bq * D + 32 * cp + 2 * n16);
        const int off = WS_RP + ((bq * 32 + 2 * cp + ((lane >> 3) & 1)) * 32 + g) * 128 + (lane & 7) * 16;
        float a0, a1;
        for (unsigned spins = 0;; ++spins) {
          asm volatile("" ::: "memory");
          v4u v[8];
#pragma unroll
          for (int i = 0; i < 8; ++i) v[i] = __builtin_amdgcn_raw_buffer_load_b128(rs, off + i * 4 * 128, 0, AUX_SC1);
          bool ok = true;
          a0 = 0.f; a1 = 0.f;
#pragma unroll
          for (int i = 0; i < 8; ++i) { ok &= (v[i].y == tag) & (v[i].w == tag); a0 += asf(v[i].x); a1 += asf(v[i].z); }
          if (all_ok(ok) || spins > SPIN_CAP) break;
        }
        STAMP(10);
        a0 = x32_sum(x16_sum(a0)); a1 = x32_sum(x16_sum(a1));
        if (lane < 16) {
          const unsigned hv = pack_bf(bf_lo(xv) + rbf(a0), bf_hi(xv) + rbf(a1));
          __builtin_amdgcn_raw_buffer_store_b64(v2u{hv, tag}, rs, WS_GH + (bq * 256 + 16 * cp + lane) * 8, 0, AUX_SC1);
        }
        STAMP(11);
      } else if (w == 1) {
        final_cell(rs, tag, c, lane, out, (unsigned*)(ws + WS_EPOCH));
        STAMP(15);
      }
    } else if (is_mlp) {
      const int fg = 8 * j + (r - 16);
      const int kq = w >> 1, cth = w & 1;
      BAR();
      STAMP(8);
      v4u gf[4], uf[4], df[4];
      {
        const lds_p p = L + L_GT + (128 * kq + 8 * g + (n16 >> 2)) * 64 + (16 * cth + 4 * (lane & 3)) * 2;
#pragma unroll
        for (int t = 0; t < 4; ++t) { gf[t] = tr_frag(p + t * 32 * 64, 64); uf[t] = tr_frag(p + 32768 + t * 32 * 64, 64); }
        const lds_p pd = L + L_DN + (8 * g + (n16 >> 2)) * 1024 + (64 * w + 4 * (lane & 3)) * 2;
#pragma unroll
        for (int t = 0; t < 4; ++t) df[t] = tr_frag(pd + t * 32, 1024);
      }
      const int goff = WS_GH + (w * 256 + 2 * lane) * 8;
      v4u h0, h1;
      for (unsigned spins = 0;; ++spins) {
        asm volatile("" ::: "memory");
        h0 = __builtin_amdgcn_raw_buffer_load_b128(rs, goff, 0, AUX_SC1);
        h1 = __builtin_amdgcn_raw_buffer_load_b128(rs, goff + 1024, 0, AUX_SC1);
        const bool ok = (h0.y == tag) & (h0.w == tag) & (h1.y == tag) & (h1.w == tag);
        if (all_ok(ok) || spins > SPIN_CAP) break;
      }
      STAMP(9);
      {
        float f[8] = {bf_lo(h0.x), bf_hi(h0.x), bf_lo(h0.z), bf_hi(h0.z), bf_lo(h1.x), bf_hi(h1.x), bf_lo(h1.z), bf_hi(h1.z)};
        layernorm8(f);
        *(LAS v2u*)(L + L_HN + w * 1056 + lane * 8) = v2u{pack_bf(f[0], f[1]), pack_bf(f[2], f[3])};
        *(LAS v2u*)(L + L_HN + w * 1056 + 512 + lane * 8) = v2u{pack_bf(f[4], f[5]), pack_bf(f[6], f[7])};
      }
      BAR();
      {
        const lds_p pa = L + L_HN + (lane & 7) * 1056 + (128 * kq + 8 * g) * 2;
        f32x4 ag = {0.f, 0.f, 0.f, 0.f}, au = {0.f, 0.f, 0.f, 0.f};
#pragma unroll
        for (int t = 0; t < 4; ++t) {
          const v4u a = lds_ld128(pa + t * 64);
          ag = mfma(a, gf[t], ag);
          au = mfma(a, uf[t], au);
        }
        LAS float* r3 = (LAS float*)(L + L_R3) + (((kq * 2 + (g >> 1)) * 2 + cth) * 8 + 4 * (g & 1)) * 16 + n16;
#pragma unroll
        for (int i = 0; i < 4; ++i) r3[i * 16] = g >= 2 ? au[i] : ag[i];
      }
      BAR();
      if (w < 4) {
        const int bb = st_id >> 5, n32 = st_id & 31;
        const LAS float* r3 = (const LAS float*)(L + L_R3) + ((n32 >> 4) * 8 + bb) * 16 + (n32 & 15);
        float gg = 0.f, uu = 0.f;
#pragma unroll
        for (int k = 0; k < 4; ++k) { gg += r3[k * 512]; uu += r3[k * 512 + 256]; }
        gg = rbf(gg); uu = rbf(uu);
        const float sg = rbf(__builtin_amdgcn_rcpf(1.0f + __builtin_amdgcn_exp2f(-1.4426950408889634f * gg)));
        *(LAS unsigned short*)(L + L_HH + bb * 96 + n32 * 2) = (unsigned short)pack_bf(sg * uu, 0.f);
      }
      BAR();
      {
        const v4u a = lds_ld128(L + L_HH + (lane & 7) * 96 + g * 16);
#pragma unroll
        for (int t = 0; t < 4; ++t) {
          const f32x4 acc = mfma(a, df[t], f32x4{0.f, 0.f, 0.f, 0.f});
          publish_tile(acc, rs, WS_FP + ((4 * w + t) * 56 + fg) * 128, 32 * 56 * 128, tag, lane);
        }
      }
      STAMP(10);
      if (w == 7) {
        final_cell(rs, tag, c, lane, out, (unsigned*)(ws + WS_EPOCH));
        STAMP(15);
      }
    } else {
      if (w == 1) {
        final_cell(rs, tag, c, lane, out, (unsigned*)(ws + WS_EPOCH));
        STAMP(15);
      }
    }
  } else {
    const int hw = wv;
    __builtin_amdgcn_s_setprio(HPRIO);
    const int kq = r >> 3, ct8 = r & 7;
    v4u xa, xb; unsigned ep, pf, pt;
    {
      const unsigned xo = (2 * hw) * (D * 2) + lane * 16;
      asm volatile("global_load_dwordx4 %0, %1, %2" : "=v"(xa) : "v"(xo), "s"(x));
      asm volatile("global_load_dwordx4 %0, %1, %2 offset:1024" : "=v"(xb) : "v"(xo), "s"(x));
      const unsigned z = 0;
      asm volatile("global_load_dword %0, %1, %2 sc1" : "=v"(ep) : "v"(z), "s"(ws));
      const unsigned char* src = (const unsigned char*)wq + ((size_t)(128 * kq + 32 * hw + (lane >> 3)) * (HQ * DH) + 512 * j + 64 * ct8) * 2 + (lane & 7) * 16;
#pragma unroll
      for (int i = 0; i < 4; ++i) glds16(src + (size_t)i * 8 * (HQ * DH) * 2, lds0 + L_WQ + (32 * hw + 8 * i) * 128);
      {
        const int li = min((r * 4 + hw) * 2 + (lane & 1), CODE_LINES - 1);
        const unsigned po = li * 128;
        asm volatile("global_load_dword %0, %1, %2" : "=v"(pf) : "v"(po), "s"(code_base));
      }
      {
        const unsigned char* p;
        const int k = lane & 15;
        if (hw == 0) p = ws + min((r * 5 + min(k, 4)) * 32768, WS_END - 128);
        else if (hw == 1) p = (const unsigned char*)wo + (r * 4 + (k & 3)) * 32768;
        else if (hw == 2) p = (k < 2 ? (const unsigned char*)wg : k < 4 ? (const unsigned char*)wu : (const unsigned char*)wd) + min(r * 2 + (k & 1), 55) * 32768;
        else p = k == 0 ? (const unsigned char*)out : k < 4 ? (const unsigned char*)ws + (k - 1) * (2 << 20) : (const unsigned char*)vc + ((size_t)(b * HKV + j) * SK + (SK - WIN) + 256 * s) * (DH * 2);
        asm volatile("global_load_dword %0, %1, off sc1" : "=v"(pt) : "v"(p));
      }
    }
    if (hw == 0 && lane < 4) flags[lane] = 0;
    BAR();
    asm volatile("s_waitcnt vmcnt(%3)" : "+v"(xa), "+v"(xb), "+v"(ep) : "i"(4 + (CODE_LINES > 0) + (PRETOUCH > 0)));
    STAMP(1);
    const unsigned tag = ep + 1;
    {
      float fa[8] = {bf_lo(xa.x), bf_hi(xa.x), bf_lo(xa.y), bf_hi(xa.y), bf_lo(xa.z), bf_hi(xa.z), bf_lo(xa.w), bf_hi(xa.w)};
      float fb[8] = {bf_lo(xb.x), bf_hi(xb.x), bf_lo(xb.y), bf_hi(xb.y), bf_lo(xb.z), bf_hi(xb.z), bf_lo(xb.w), bf_hi(xb.w)};
      layernorm8(fa);
      layernorm8(fb);
      if (g == kq) {
        *(LAS v4u*)(L + L_XN + (2 * hw) * 288 + n16 * 16) = v4u{pack_bf(fa[0], fa[1]), pack_bf(fa[2], fa[3]), pack_bf(fa[4], fa[5]), pack_bf(fa[6], fa[7])};
        *(LAS v4u*)(L + L_XN + (2 * hw + 1) * 288 + n16 * 16) = v4u{pack_bf(fb[0], fb[1]), pack_bf(fb[2], fb[3]), pack_bf(fb[4], fb[5]), pack_bf(fb[6], fb[7])};
      }
    }
    asm volatile("s_waitcnt vmcnt(%0) lgkmcnt(0)" :: "i"((CODE_LINES > 0) + (PRETOUCH > 0)) : "memory");
    STAMP(2);
    if (lane == 0) __hip_atomic_fetch_add((LAS unsigned*)&flags[0], 1u, __ATOMIC_RELAXED, __HIP_MEMORY_SCOPE_WORKGROUP);
    while (flags[0] < 4u) __builtin_amdgcn_s_sleep(1);
    asm volatile("" ::: "memory");
    {
      const lds_p pa = L + L_XN + (lane & 7) * 288 + g * 16;
      const lds_p pb = L + L_WQ + (8 * g + (n16 >> 2)) * 128 + (16 * hw + 4 * (lane & 3)) * 2;
      f32x4 acc = {0.f, 0.f, 0.f, 0.f};
#pragma unroll
      for (int t = 0; t < 4; ++t) acc = mfma(lds_ld128(pa + t * 64), tr_frag(pb + t * 32 * 128, 128), acc);
      publish_tile(acc, rs, WS_QP + (kq * 8 * 4096 + 512 * j + 64 * ct8 + 16 * hw) * 8, 4096 * 8, tag, lane);
    }
    STAMP(3);
    __builtin_amdgcn_s_sleep(QSLEEP);
    {
      const int off = WS_QP + (b * 4096 + 512 * j + 128 * hw + 2 * lane) * 8;
      float q0, q1;
      for (unsigned spins = 0;; ++spins) {
        asm volatile("" ::: "memory");
        v4u v[4];
#pragma unroll
        for (int i = 0; i < 4; ++i) v[i] = __builtin_amdgcn_raw_buffer_load_b128(rs, off + i * 8 * 4096 * 8, 0, AUX_SC1);
        bool ok = true;
        q0 = 0.f; q1 = 0.f;
#pragma unroll
        for (int i = 0; i < 4; ++i) { ok &= (v[i].y == tag) & (v[i].w == tag); q0 += asf(v[i].x); q1 += asf(v[i].z); }
        if (all_ok(ok) || spins > SPIN_CAP) break;
      }
      asm volatile("" : "+v"(pf), "+v"(pt));
      *(LAS unsigned*)(L + L_Q + hw * 320 + lane * 4) = pack_bf(q0, q1);
      if (hw == 0 && lane == 0) flags[1] = tag;
    }
    STAMP(4);
    BAR();
    __builtin_amdgcn_s_setprio(0);
    if (is_wo) {
      const int h = 4 * j + (r >> 2), ns = r & 3;
      const unsigned char* src = (const unsigned char*)wo + ((size_t)(128 * h + 32 * hw + g) * D + 128 * ns) * 2 + n16 * 16;
#pragma unroll
      for (int i = 0; i < 8; ++i) glds16(src + (size_t)i * 4 * D * 2, lds0 + L_W + (32 * hw + 4 * i) * 256);
      STAMP(5);
      asm volatile("s_waitcnt vmcnt(0)" ::: "memory");
      STAMP(6);
      BAR();
    } else if (is_mlp) {
      const int fg = 8 * j + (r - 16);
      const size_t go = ((size_t)(128 * hw + (lane >> 2)) * FF + 32 * fg) * 2 + (lane & 3) * 16;
#pragma unroll
      for (int i = 0; i < 8; ++i) {
        if (i >= 2) asm volatile("s_waitcnt vmcnt(3)" ::: "memory");
        glds16((const unsigned char*)wg + go + (size_t)i * 16 * FF * 2, lds0 + L_GT + (128 * hw + 16 * i) * 64);
        if (i >= 2) asm volatile("s_waitcnt vmcnt(3)" ::: "memory");
        glds16((const unsigned char*)wu + go + (size_t)i * 16 * FF * 2, lds0 + L_UP + (128 * hw + 16 * i) * 64);
      }
      const unsigned char* sd = (const unsigned char*)wd + (size_t)(32 * fg + 8 * hw) * D * 2 + lane * 16;
#pragma unroll
      for (int i = 0; i < 8; ++i) {
        asm volatile("s_waitcnt vmcnt(5)" ::: "memory");
        glds16(sd + (size_t)i * D * 2, lds0 + L_DN + (8 * hw + i) * 1024);
      }
      STAMP(5);
      asm volatile("s_waitcnt vmcnt(0)" ::: "memory");
      STAMP(6);
      BAR();
    }
  }
}

}

extern "C" void kernel_launch(void* const* d_in, const int* in_sizes, int n_in, void* d_out, int out_size, void* d_ws,
                              size_t ws_size, hipStream_t stream) {
  (void)in_sizes; (void)n_in; (void)out_size; (void)ws_size;
  (void)hipFuncSetAttribute((const void*)fused_decode, hipFuncAttributeMaxDynamicSharedMemorySize, LDS_BYTES);
  fused_decode<<<256, NTHR, LDS_BYTES, stream>>>(
      (const unsigned short*)d_in[0], (const unsigned short*)d_in[1], (const unsigned short*)d_in[2], (const unsigned short*)d_in[3],
      (const unsigned short*)d_in[4], (const unsigned short*)d_in[6], (const unsigned short*)d_in[7], (const unsigned short*)d_in[8],
      (unsigned short*)d_out, (unsigned char*)d_ws);
}
```

```cpp
#define DIAG 2
#include <hip/hip_runtime.h>
#include <hip/hip_bf16.h>
#include <stdint.h>

namespace {

typedef unsigned v4u __attribute__((ext_vector_type(4)));
typedef unsigned v2u __attribute__((ext_vector_type(2)));
typedef float f32x4 __attribute__((ext_vector_type(4)));
typedef __bf16 bf16x8 __attribute__((ext_vector_type(8)));
typedef short s16x4 __attribute__((ext_vector_type(4)));
typedef unsigned long long u64;
#define LAS __attribute__((address_space(3)))
typedef LAS unsigned char* lds_p;

constexpr int NB = 8, D = 512, HQ = 32, HKV = 8, SK = 4096, DH = 128, WIN = 1024, FF = 1792;
constexpr int NTHR = 768;
constexpr int AUX_SC1 = 16;
constexpr unsigned SPIN_CAP = 1u << 16;
#define PD 4
#define CODE_LINES 126
#define PRETOUCH 1
#define DIAGC 0
#define QSLEEP 16
#define HPRIO 2

constexpr int WS_EPOCH = 0;
constexpr int WS_QP = 1 << 16;
constexpr int WS_OP = WS_QP + (1 << 20);
constexpr int WS_LS = WS_OP + (1 << 19);
constexpr int WS_RP = WS_LS + (1 << 15);
constexpr int WS_GH = WS_RP + (1 << 20);
constexpr int WS_FP = WS_GH + (1 << 14);
constexpr int WS_END = WS_FP + 256 * 56 * 128;
constexpr int WS_STAMP = 64 << 20;

constexpr int L_WQ = 0;
constexpr int L_RED = 0;
constexpr int L_XN = 16384;
constexpr int L_Q = L_XN + 8 * 288;
constexpr int L_MAX = L_Q + 4 * 320;
constexpr int L_SUM = L_MAX + 128;
constexpr int L_FLAG = L_SUM + 128;
constexpr int L_OS = L_FLAG + 64;
constexpr int L_HN = L_OS + 8 * 288;
constexpr int L_R3 = L_HN + 8 * 1056;
constexpr int L_HH = L_R3 + 8192;
constexpr int L_W = ((L_HH + 8 * 96 + 255) / 256) * 256;
constexpr int L_GT = L_W, L_UP = L_W + 32768, L_DN = L_W + 65536;
constexpr int LDS_BYTES = L_W + 98304;
static_assert(LDS_BYTES <= 160 * 1024 && LDS_BYTES > 80 * 1024, "one workgroup per CU");

__device__ __forceinline__ float bf_lo(unsigned u) { return __builtin_bit_cast(float, u << 16); }
__device__ __forceinline__ float bf_hi(unsigned u) { return __builtin_bit_cast(float, u & 0xffff0000u); }
__device__ __forceinline__ unsigned pack_bf(float lo, float hi) {
  typedef __bf16 bf2 __attribute__((ext_vector_type(2)));
  bf2 r = {(__bf16)lo, (__bf16)hi};
  return __builtin_bit_cast(unsigned, r);
}
__device__ __forceinline__ float rbf(float f) { return bf_lo(pack_bf(f, f)); }
__device__ __forceinline__ float asf(unsigned u) { return __builtin_bit_cast(float, u); }
__device__ __forceinline__ unsigned asu(float f) { return __builtin_bit_cast(unsigned, f); }
template <int CTRL> __device__ __forceinline__ float dpp(float x) {
  return __builtin_bit_cast(float, __builtin_amdgcn_mov_dpp(__builtin_bit_cast(int, x), CTRL, 0xf, 0xf, true));
}
__device__ __forceinline__ float x16_sum(float x) {
  auto s = __builtin_amdgcn_permlane16_swap(asu(x), asu(x), false, false);
  return asf(s[0]) + asf(s[1]);
}
__device__ __forceinline__ float x32_sum(float x) {
  auto s = __builtin_amdgcn_permlane32_swap(asu(x), asu(x), false, false);
  return asf(s[0]) + asf(s[1]);
}
__device__ __forceinline__ float x16_max(float x) {
  auto s = __builtin_amdgcn_permlane16_swap(asu(x), asu(x), false, false);
  return fmaxf(asf(s[0]), asf(s[1]));
}
__device__ __forceinline__ float x32_max(float x) {
  auto s = __builtin_amdgcn_permlane32_swap(asu(x), asu(x), false, false);
  return fmaxf(asf(s[0]), asf(s[1]));
}
__device__ __forceinline__ float wave_sum(float v) {
  v += dpp<0xB1>(v);
  v += dpp<0x4E>(v);
  v += dpp<0x141>(v);
  v += dpp<0x140>(v);
  return x32_sum(x16_sum(v));
}
__device__ __forceinline__ f32x4 mfma(v4u a, v4u b, f32x4 c) {
  return __builtin_amdgcn_mfma_f32_16x16x32_bf16(__builtin_bit_cast(bf16x8, a), __builtin_bit_cast(bf16x8, b), c, 0, 0, 0);
}
__device__ __forceinline__ v2u tr_read(lds_p p) {
  typedef short v4s __attribute__((ext_vector_type(4)));
  return __builtin_bit_cast(v2u, __builtin_amdgcn_ds_read_tr16_b64_v4i16((LAS v4s*)p));
}
__device__ __forceinline__ v4u tr_frag(lds_p p, int row_stride) {
  const v2u a = tr_read(p), b = tr_read(p + 4 * row_stride);
  return v4u{a.x, a.y, b.x, b.y};
}
__device__ __forceinline__ v4u lds_ld128(lds_p p) { return *(const LAS v4u*)p; }
__device__ __forceinline__ void glds16(const void* gsrc, unsigned lds_dst) {
  unsigned keep;
  asm volatile("s_mov_b32 %0, m0\n\ts_mov_b32 m0, %2\n\ts_nop 0\n\tglobal_load_lds_dwordx4 %1, off nt\n\ts_mov_b32 m0, %0"
               : "=&s"(keep) : "v"(gsrc), "s"(lds_dst) : "memory");
}
#define BAR() do { asm volatile("s_waitcnt lgkmcnt(0)" ::: "memory"); __builtin_amdgcn_s_barrier(); asm volatile("" ::: "memory"); } while (0)
__device__ __forceinline__ void lds_arrive(volatile LAS unsigned* p, int lane) {
  asm volatile("" ::: "memory");
  if (lane == 0) __hip_atomic_fetch_add((LAS unsigned*)p, 1u, __ATOMIC_RELAXED, __HIP_MEMORY_SCOPE_WORKGROUP);
  asm volatile("" ::: "memory");
}
__device__ __forceinline__ void lds_wait(volatile LAS unsigned* p, unsigned n) {
  while (*p < n) { }
  asm volatile("" ::: "memory");
}
__device__ __forceinline__ bool all_ok(bool ok) { return __builtin_amdgcn_ballot_w64(ok) == ~0ull; }

__device__ __forceinline__ void layernorm8(float (&f)[8]) {
  float s = ((f[0] + f[1]) + (f[2] + f[3])) + ((f[4] + f[5]) + (f[6] + f[7]));
  const float mean = wave_sum(s) * (1.0f / D);
  float ss = 0.f;
#pragma unroll
  for (int i = 0; i < 8; ++i) { const float d = f[i] - mean; ss = fmaf(d, d, ss); }
  const float var = rbf(wave_sum(ss) * (1.0f / D));
  const float den = rbf(__builtin_sqrtf(rbf(var + 1e-5f)));
  const float inv = __builtin_amdgcn_rcpf(den), mb = rbf(mean);
#pragma unroll
  for (int i = 0; i < 8; ++i) f[i] = rbf(f[i] - mb) * inv;
}

__device__ __forceinline__ void publish_tile(f32x4 d, __amdgpu_buffer_rsrc_t rs, int base, int stride_b, unsigned tag, int lane) {
  const int n = lane & 15, gq = lane >> 4;
  const bool hi = gq >= 2, e = n & 1;
  const float a = hi ? d[2] : d[0], c = hi ? d[3] : d[1];
  const float recv = dpp<0xB1>(e ? a : c);
  const int bb = 4 * (gq & 1) + (hi ? 2 : 0) + (e ? 1 : 0);
  const v4u v = {asu(e ? recv : a), tag, asu(e ? c : recv), tag};
  __builtin_amdgcn_raw_buffer_store_b128(v, rs, base + bb * stride_b + (n >> 1) * 16, 0, AUX_SC1);
}

#define STAMP(i) do { if (i == 0) dg0 = __builtin_amdgcn_s_memrealtime(); if (i == DIAG) dg1 = __builtin_amdgcn_s_memrealtime(); } while (0)

__device__ __forceinline__ void final_cell(__amdgpu_buffer_rsrc_t rs, unsigned tag, int c, int lane, unsigned short* out, unsigned* ws_epoch) {
  const int bq = c >> 5, ct = c & 31, p = lane & 7;
  v2u hv;
  for (unsigned spins = 0;; ++spins) {
    asm volatile("" ::: "memory");
    hv = __builtin_amdgcn_raw_buffer_load_b64(rs, WS_GH + (bq * 256 + 8 * ct + p) * 8, 0, AUX_SC1);
    if (all_ok(hv.y == tag) || spins > SPIN_CAP) break;
    __builtin_amdgcn_s_sleep(2);
  }
  const int off = WS_FP + ((bq * 32 + ct) * 56 + (lane >> 3)) * 128 + p * 16;
  float a0, a1;
  for (unsigned spins = 0;; ++spins) {
    asm volatile("" ::: "memory");
    v4u v[7];
#pragma unroll
    for (int i = 0; i < 7; ++i) v[i] = __builtin_amdgcn_raw_buffer_load_b128(rs, off + i * 8 * 128, 0, AUX_SC1);
    bool ok = true;
    a0 = 0.f; a1 = 0.f;
#pragma unroll
    for (int i = 0; i < 7; ++i) { ok &= (v[i].y == tag) & (v[i].w == tag); a0 += asf(v[i].x); a1 += asf(v[i].z); }
    if (all_ok(ok) || spins > SPIN_CAP) break;
  }
  a0 += dpp<0x128>(a0); a1 += dpp<0x128>(a1);
  a0 = x32_sum(x16_sum(a0)); a1 = x32_sum(x16_sum(a1));
  if (lane < 8) {
    const unsigned o = pack_bf(bf_lo(hv.x) + rbf(a0), bf_hi(hv.x) + rbf(a1));
    *(unsigned*)(out + bq * D + 16 * ct + 2 * p) = o;
  }
  if (c == 0 && lane == 0) __hip_atomic_store(ws_epoch, tag, __ATOMIC_RELAXED, __HIP_MEMORY_SCOPE_AGENT);
}

__global__ void __launch_bounds__(NTHR, 3)
fused_decode(const unsigned short* __restrict__ x, const unsigned short* __restrict__ kc, const unsigned short* __restrict__ vc,
             const unsigned short* __restrict__ wq, const unsigned short* __restrict__ wo, const unsigned short* __restrict__ wg,
             const unsigned short* __restrict__ wu, const unsigned short* __restrict__ wd, unsigned short* __restrict__ out,
             unsigned char* __restrict__ ws) {
  unsigned long long code_base;
  asm volatile("s_getpc_b64 %0" : "=s"(code_base));
  code_base &= ~0xffull;
  extern __shared__ __attribute__((aligned(16))) unsigned char smem[];
  const lds_p L = (lds_p)smem;
  const unsigned lds0 = (unsigned)(size_t)L;
  const int tid = threadIdx.x, lane = tid & 63;
  const int wv = __builtin_amdgcn_readfirstlane(tid >> 6);
  const int w = wv - 4, st_id = tid - 256;
  const int c = blockIdx.x, j = c & 7, r = c >> 3;
  const int b = r >> 2, s = r & 3;
  const int g = lane >> 4, n16 = lane & 15;
  const bool is_wo = r < 16, is_mlp = (r >= 16) && (r < 24) && (j < 7);
  const __amdgpu_buffer_rsrc_t rs = __builtin_amdgcn_make_buffer_rsrc((void*)ws, 0, WS_END, 0x00020000);
  volatile LAS unsigned* flags = (volatile LAS unsigned*)(L + L_FLAG);
  u64 dg0 = 0, dg1 = 0;
  STAMP(0);

  if (wv >= 4) {
    const size_t tile = ((size_t)(b * HKV + j) * SK + (SK - WIN) + 256 * s + 32 * w) * (DH * 2);
    const unsigned char* kb = (const unsigned char*)kc + tile + 4096;
    const unsigned char* vb = (const unsigned char*)vc + tile;
    const unsigned koff = (lane & 7) * 256 + (2 * g + ((lane >> 3) & 1)) * 16;
    const unsigned voff = (16 * (g >> 1) + 4 * (g & 1)) * 256 + n16 * 16;
    v4u K[8], V[8];
#define LDK(i, imm) asm volatile("global_load_dwordx4 %0, %1, %2 offset:" #imm " nt" : "=v"(K[i]) : "v"(koff), "s"(kb))
#define LDV(i, imm) asm volatile("global_load_dwordx4 %0, %1, %2 offset:" #imm " nt" : "=v"(V[i]) : "v"(voff), "s"(vb))
#define STR2(x) #x
#define STR(x) STR2(x)
#define THR(n) do { if (n >= PD) asm volatile("s_waitcnt vmcnt(%0)" :: "i"(PD - 1)); } while (0)
    LDK(0, -4096); THR(1); LDK(1, -3968); THR(2); LDK(2, -2048); THR(3); LDK(3, -1920);
    __builtin_amdgcn_s_barrier();
    THR(4); LDK(4, 0); THR(5); LDK(5, 128); THR(6); LDK(6, 2048); THR(7); LDK(7, 2176);
    THR(8); LDV(0, 0); THR(9); LDV(1, 256); THR(10); LDV(2, 512); THR(11); LDV(3, 768);
    THR(12); LDV(4, 2048); THR(13); LDV(5, 2304); THR(14); LDV(6, 2560); THR(15); LDV(7, 2816);
    STAMP(1);
    BAR();
    STAMP(2);
    const unsigned tag = flags[1];
    const lds_p qa = L + L_Q + (n16 & 3) * 320 + (2 * g + ((n16 >> 2) & 1)) * 16;
    const v4u q0 = lds_ld128(qa), q1 = lds_ld128(qa + 128);
    asm volatile("s_waitcnt vmcnt(8)" : "+v"(K[0]), "+v"(K[1]), "+v"(K[2]), "+v"(K[3]), "+v"(K[4]), "+v"(K[5]), "+v"(K[6]), "+v"(K[7]));
    STAMP(3);
    f32x4 sa[4];
#pragma unroll
    for (int o = 0; o < 4; ++o) {
      sa[o] = mfma(K[2 * o], q0, f32x4{0.f, 0.f, 0.f, 0.f});
      sa[o] = mfma(K[2 * o + 1], q1, sa[o]);
    }
    float sc[8];
#pragma unroll
    for (int p = 0; p < 2; ++p)
#pragma unroll
      for (int i = 0; i < 4; ++i) {
        auto sw = __builtin_amdgcn_permlane32_swap(asu(sa[p][i]), asu(sa[p + 2][i]), false, false);
        const float v = asf(sw[0]) + dpp<0x104>(asf(sw[1]));
        sc[4 * p + i] = rbf(rbf(v) * 0.08838834764831845f);
      }
    float m = fmaxf(fmaxf(fmaxf(sc[0], sc[1]), fmaxf(sc[2], sc[3])), fmaxf(fmaxf(sc[4], sc[5]), fmaxf(sc[6], sc[7])));
    m = x32_max(x16_max(m));
    if (lane < 4) ((LAS float*)(L + L_MAX))[w * 4 + lane] = m;
    lds_arrive(flags + 2, lane);
    lds_wait(flags + 2, 8);
    {
      const LAS float* mx = (const LAS float*)(L + L_MAX) + (n16 & 3);
      m = mx[0];
#pragma unroll
      for (int i = 1; i < 8; ++i) m = fmaxf(m, mx[4 * i]);
    }
    float pe[8], ls = 0.f;
#pragma unroll
    for (int i = 0; i < 8; ++i) { pe[i] = __builtin_amdgcn_exp2f((sc[i] - m) * 1.4426950408889634f); ls += pe[i]; }
    const v4u P = {pack_bf(pe[0], pe[1]), pack_bf(pe[2], pe[3]), pack_bf(pe[4], pe[5]), pack_bf(pe[6], pe[7])};
    ls = x32_sum(x16_sum(ls));
    if (lane < 4) ((LAS float*)(L + L_SUM))[w * 4 + lane] = ls;
    STAMP(4);
    v4u T[8];
#define XPOSE(n_)                                                                                         \
  _Pragma("unroll") for (int mm = 0; mm < 4; ++mm) {                                                      \
    T[2 * mm][n_] = __builtin_amdgcn_perm(V[2 * n_ + 1][mm], V[2 * n_][mm], 0x05040100u);                 \
    T[2 * mm + 1][n_] = __builtin_amdgcn_perm(V[2 * n_ + 1][mm], V[2 * n_][mm], 0x07060302u);             \
  }
    asm volatile("s_waitcnt vmcnt(6)" : "+v"(V[0]), "+v"(V[1])); XPOSE(0) __builtin_amdgcn_sched_barrier(0);
    asm volatile("s_waitcnt vmcnt(4)" : "+v"(V[2]), "+v"(V[3])); XPOSE(1) __builtin_amdgcn_sched_barrier(0);
    asm volatile("s_waitcnt vmcnt(2)" : "+v"(V[4]), "+v"(V[5])); XPOSE(2) __builtin_amdgcn_sched_barrier(0);
    asm volatile("s_waitcnt vmcnt(0)" : "+v"(V[6]), "+v"(V[7]));
    STAMP(5);
    XPOSE(3)
    f32x4 oa[8];
#pragma unroll
    for (int i = 0; i < 8; ++i) oa[i] = mfma(P, T[i], f32x4{0.f, 0.f, 0.f, 0.f});
    if (lane < 16) {
      LAS f32x4* red = (LAS f32x4*)(L + L_RED + w * 2048) + lane;
#pragma unroll
      for (int hh = 0; hh < 4; ++hh) {
        red[(hh * 2 + 0) * 16] = f32x4{oa[0][hh], oa[1][hh], oa[2][hh], oa[3][hh]};
        red[(hh * 2 + 1) * 16] = f32x4{oa[4][hh], oa[5][hh], oa[6][hh], oa[7][hh]};
      }
    }
    lds_arrive(flags + 3, lane);
    STAMP(6);
    if (w < 3) {
      lds_wait(flags + 3, 8);
      const int hh = w < 2 ? (st_id >> 5) : (lane & 3);
      const LAS float* sm = (const LAS float*)(L + L_SUM) + hh;
      float l = sm[0];
#pragma unroll
      for (int i = 1; i < 8; ++i) l += sm[4 * i];
      if (w < 2) {
        const int u = st_id & 31;
        const LAS f32x4* red = (const LAS f32x4*)(L + L_RED) + hh * 32 + u;
        f32x4 o = red[0];
#pragma unroll
        for (int i = 1; i < 8; ++i) o += red[i * 128];
        const float inv = __builtin_amdgcn_rcpf(l);
        const v4u v = {pack_bf(o[0] * inv, o[1] * inv), tag, pack_bf(o[2] * inv, o[3] * inv), tag};
        const int off = WS_OP + (((b * HQ + 4 * j + hh) * 4 + s) * 64 + 4 * (u & 15) + 2 * (u >> 4)) * 8;
        __builtin_amdgcn_raw_buffer_store_b128(v, rs, off, 0, AUX_SC1);
      } else if (lane < 16) {
        const LAS float* mx = (const LAS float*)(L + L_MAX) + hh;
        float mm = mx[0];
#pragma unroll
        for (int i = 1; i < 8; ++i) mm = fmaxf(mm, mx[4 * i]);
        const float lse = mm * 1.4426950408889634f + __builtin_amdgcn_logf(l);
        __builtin_amdgcn_raw_buffer_store_b64(v2u{asu(lse), tag}, rs, WS_LS + ((b * HKV + j) * 4 + s) * 128 + lane * 8, 0, AUX_SC1);
      }
    }
    STAMP(7);

    if (is_wo) {
      const int h = 4 * j + (r >> 2), ns = r & 3;
      BAR();
      v4u wf[4];
      {
        const lds_p p = L + L_W + (8 * g + (n16 >> 2)) * 256 + (16 * w + 4 * (lane & 3)) * 2;
#pragma unroll
        for (int t = 0; t < 4; ++t) wf[t] = tr_frag(p + t * 32 * 256, 256);
      }
      const int doff = WS_OP + ((w * HQ + h) * 4 + (lane >> 5)) * 512 + (lane & 31) * 16;
      const int loff = WS_LS + ((w * HKV + j) * 4 + (lane & 3)) * 128 + (h & 3) * 8;
      v4u d0, d1; v2u lv;
      for (unsigned spins = 0;; ++spins) {
        asm volatile("" ::: "memory");
        d0 = __builtin_amdgcn_raw_buffer_load_b128(rs, doff, 0, AUX_SC1);
        d1 = __builtin_amdgcn_raw_buffer_load_b128(rs, doff + 1024, 0, AUX_SC1);
        lv = __builtin_amdgcn_raw_buffer_load_b64(rs, loff, 0, AUX_SC1);
        const bool ok = (d0.y == tag) & (d0.w == tag) & (d1.y == tag) & (d1.w == tag) & (lv.y == tag);
        if (all_ok(ok) || spins > SPIN_CAP) break;
      }
      STAMP(8);
      {
        const float lse = asf(lv.x);
        float mx = fmaxf(lse, dpp<0xB1>(lse));
        mx = fmaxf(mx, dpp<0x4E>(mx));
        const float e = __builtin_amdgcn_exp2f(lse - mx);
        float es = e + dpp<0xB1>(e);
        es += dpp<0x4E>(es);
        const float wt = e * __builtin_amdgcn_rcpf(es);
        const float w0 = dpp<0x00>(wt), w1 = dpp<0x55>(wt), w2 = dpp<0xAA>(wt), w3 = dpp<0xFF>(wt);
        const float wa = lane < 32 ? w0 : w1, wb = lane < 32 ? w2 : w3;
        float o0 = x32_sum(fmaf(wa, bf_lo(d0.x), wb * bf_lo(d1.x)));
        float o1 = x32_sum(fmaf(wa, bf_hi(d0.x), wb * bf_hi(d1.x)));
        float o2 = x32_sum(fmaf(wa, bf_lo(d0.z), wb * bf_lo(d1.z)));
        float o3 = x32_sum(fmaf(wa, bf_hi(d0.z), wb * bf_hi(d1.z)));
        if (lane < 32) *(LAS v2u*)(L + L_OS + w * 288 + lane * 8) = v2u{pack_bf(o0, o1), pack_bf(o2, o3)};
      }
      BAR();
      {
        const lds_p pa = L + L_OS + (lane & 7) * 288 + g * 16;
        f32x4 acc = {0.f, 0.f, 0.f, 0.f};
#pragma unroll
        for (int t = 0; t < 4; ++t) acc = mfma(lds_ld128(pa + t * 64), wf[t], acc);
        publish_tile(acc, rs, WS_RP + ((8 * ns + w) * 32 + h) * 128, 32 * 32 * 128, tag, lane);
      }
      STAMP(9);
      if (w == 0) {
        const int rho = 16 * j + r, bq = rho >> 4, cp = rho & 15;
        const unsigned xv = *(const unsigned*)(x + bq * D + 32 * cp + 2 * n16);
        const int off = WS_RP + ((bq * 32 + 2 * cp + ((lane >> 3) & 1)) * 32 + g) * 128 + (lane & 7) * 16;
        float a0, a1;
        for (unsigned spins = 0;; ++spins) {
          asm volatile("" ::: "memory");
          v4u v[8];
#pragma unroll
          for (int i = 0; i < 8; ++i) v[i] = __builtin_amdgcn_raw_buffer_load_b128(rs, off + i * 4 * 128, 0, AUX_SC1);
          bool ok = true;
          a0 = 0.f; a1 = 0.f;
#pragma unroll
          for (int i = 0; i < 8; ++i) { ok &= (v[i].y == tag) & (v[i].w == tag); a0 += asf(v[i].x); a1 += asf(v[i].z); }
          if (all_ok(ok) || spins > SPIN_CAP) break;
        }
        STAMP(10);
        a0 = x32_sum(x16_sum(a0)); a1 = x32_sum(x16_sum(a1));
        if (lane < 16) {
          const unsigned hv = pack_bf(bf_lo(xv) + rbf(a0), bf_hi(xv) + rbf(a1));
          __builtin_amdgcn_raw_buffer_store_b64(v2u{hv, tag}, rs, WS_GH + (bq * 256 + 16 * cp + lane) * 8, 0, AUX_SC1);
        }
        STAMP(11);
      } else if (w == 1) {
        final_cell(rs, tag, c, lane, out, (unsigned*)(ws + WS_EPOCH));
        STAMP(15);
        if (c == DIAGC) {
          const u64 tend = __builtin_amdgcn_s_memrealtime();
          const u64 until = tend + 4 * (dg1 - dg0);
          for (int sp = 0; sp < 100000 && __builtin_amdgcn_s_memrealtime() < until; ++sp) __builtin_amdgcn_s_sleep(1);
        }
      }
    } else if (is_mlp) {
      const int fg = 8 * j + (r - 16);
      const int kq = w >> 1, cth = w & 1;
      BAR();
      STAMP(8);
      v4u gf[4], uf[4], df[4];
      {
        const lds_p p = L + L_GT + (128 * kq + 8 * g + (n16 >> 2)) * 64 + (16 * cth + 4 * (lane & 3)) * 2;
#pragma unroll
        for (int t = 0; t < 4; ++t) { gf[t] = tr_frag(p + t * 32 * 64, 64); uf[t] = tr_frag(p + 32768 + t * 32 * 64, 64); }
        const lds_p pd = L + L_DN + (8 * g + (n16 >> 2)) * 1024 + (64 * w + 4 * (lane & 3)) * 2;
#pragma unroll
        for (int t = 0; t < 4; ++t) df[t] = tr_frag(pd + t * 32, 1024);
      }
      const int goff = WS_GH + (w * 256 + 2 * lane) * 8;
      v4u h0, h1;
      for (unsigned spins = 0;; ++spins) {
        asm volatile("" ::: "memory");
        h0 = __builtin_amdgcn_raw_buffer_load_b128(rs, goff, 0, AUX_SC1);
        h1 = __builtin_amdgcn_raw_buffer_load_b128(rs, goff + 1024, 0, AUX_SC1);
        const bool ok = (h0.y == tag) & (h0.w == tag) & (h1.y == tag) & (h1.w == tag);
        if (all_ok(ok) || spins > SPIN_CAP) break;
      }
      STAMP(9);
      {
        float f[8] = {bf_lo(h0.x), bf_hi(h0.x), bf_lo(h0.z), bf_hi(h0.z), bf_lo(h1.x), bf_hi(h1.x), bf_lo(h1.z), bf_hi(h1.z)};
        layernorm8(f);
        *(LAS v2u*)(L + L_HN + w * 1056 + lane * 8) = v2u{pack_bf(f[0], f[1]), pack_bf(f[2], f[3])};
        *(LAS v2u*)(L + L_HN + w * 1056 + 512 + lane * 8) = v2u{pack_bf(f[4], f[5]), pack_bf(f[6], f[7])};
      }
      BAR();
      {
        const lds_p pa = L + L_HN + (lane & 7) * 1056 + (128 * kq + 8 * g) * 2;
        f32x4 ag = {0.f, 0.f, 0.f, 0.f}, au = {0.f, 0.f, 0.f, 0.f};
#pragma unroll
        for (int t = 0; t < 4; ++t) {
          const v4u a = lds_ld128(pa + t * 64);
          ag = mfma(a, gf[t], ag);
          au = mfma(a, uf[t], au);
        }
        LAS float* r3 = (LAS float*)(L + L_R3) + (((kq * 2 + (g >> 1)) * 2 + cth) * 8 + 4 * (g & 1)) * 16 + n16;
#pragma unroll
        for (int i = 0; i < 4; ++i) r3[i * 16] = g >= 2 ? au[i] : ag[i];
      }
      BAR();
      if (w < 4) {
        const int bb = st_id >> 5, n32 = st_id & 31;
        const LAS float* r3 = (const LAS float*)(L + L_R3) + ((n32 >> 4) * 8 + bb) * 16 + (n32 & 15);
        float gg = 0.f, uu = 0.f;
#pragma unroll
        for (int k = 0; k < 4; ++k) { gg += r3[k * 512]; uu += r3[k * 512 + 256]; }
        gg = rbf(gg); uu = rbf(uu);
        const float sg = rbf(__builtin_amdgcn_rcpf(1.0f + __builtin_amdgcn_exp2f(-1.4426950408889634f * gg)));
        *(LAS unsigned short*)(L + L_HH + bb * 96 + n32 * 2) = (unsigned short)pack_bf(sg * uu, 0.f);
      }
      BAR();
      {
        const v4u a = lds_ld128(L + L_HH + (lane & 7) * 96 + g * 16);
#pragma unroll
        for (int t = 0; t < 4; ++t) {
          const f32x4 acc = mfma(a, df[t], f32x4{0.f, 0.f, 0.f, 0.f});
          publish_tile(acc, rs, WS_FP + ((4 * w + t) * 56 + fg) * 128, 32 * 56 * 128, tag, lane);
        }
      }
      STAMP(10);
      if (w == 7) {
        final_cell(rs, tag, c, lane, out, (unsigned*)(ws + WS_EPOCH));
        STAMP(15);
      }
    } else {
      if (w == 1) {
        final_cell(rs, tag, c, lane, out, (unsigned*)(ws + WS_EPOCH));
        STAMP(15);
      }
    }
  } else {
    const int hw = wv;
    __builtin_amdgcn_s_setprio(HPRIO);
    const int kq = r >> 3, ct8 = r & 7;
    v4u xa, xb; unsigned ep, pf, pt;
    {
      const unsigned xo = (2 * hw) * (D * 2) + lane * 16;
      asm volatile("global_load_dwordx4 %0, %1, %2" : "=v"(xa) : "v"(xo), "s"(x));
      asm volatile("global_load_dwordx4 %0, %1, %2 offset:1024" : "=v"(xb) : "v"(xo), "s"(x));
      const unsigned z = 0;
      asm volatile("global_load_dword %0, %1, %2 sc1" : "=v"(ep) : "v"(z), "s"(ws));
      const unsigned char* src = (const unsigned char*)wq + ((size_t)(128 * kq + 32 * hw + (lane >> 3)) * (HQ * DH) + 512 * j + 64 * ct8) * 2 + (lane & 7) * 16;
#pragma unroll
      for (int i = 0; i < 4; ++i) glds16(src + (size_t)i * 8 * (HQ * DH) * 2, lds0 + L_WQ + (32 * hw + 8 * i) * 128);
      {
        const int li = min((r * 4 + hw) * 2 + (lane & 1), CODE_LINES - 1);
        const unsigned po = li * 128;
        asm volatile("global_load_dword %0, %1, %2" : "=v"(pf) : "v"(po), "s"(code_base));
      }
      {
        const unsigned char* p;
        const int k = lane & 15;
        if (hw == 0) p = ws + min((r * 5 + min(k, 4)) * 32768, WS_END - 128);
        else if (hw == 1) p = (const unsigned char*)wo + (r * 4 + (k & 3)) * 32768;
        else if (hw == 2) p = (k < 2 ? (const unsigned char*)wg : k < 4 ? (const unsigned char*)wu : (const unsigned char*)wd) + min(r * 2 + (k & 1), 55) * 32768;
        else p = k == 0 ? (const unsigned char*)out : k < 4 ? (const unsigned char*)ws + (k - 1) * (2 << 20) : (const unsigned char*)vc + ((size_t)(b * HKV + j) * SK + (SK - WIN) + 256 * s) * (DH * 2);
        asm volatile("global_load_dword %0, %1, off sc1" : "=v"(pt) : "v"(p));
      }
    }
    if (hw == 0 && lane < 4) flags[lane] = 0;
    BAR();
    asm volatile("s_waitcnt vmcnt(%3)" : "+v"(xa), "+v"(xb), "+v"(ep) : "i"(4 + (CODE_LINES > 0) + (PRETOUCH > 0)));
    STAMP(1);
    const unsigned tag = ep + 1;
    {
      float fa[8] = {bf_lo(xa.x), bf_hi(xa.x), bf_lo(xa.y), bf_hi(xa.y), bf_lo(xa.z), bf_hi(xa.z), bf_lo(xa.w), bf_hi(xa.w)};
      float fb[8] = {bf_lo(xb.x), bf_hi(xb.x), bf_lo(xb.y), bf_hi(xb.y), bf_lo(xb.z), bf_hi(xb.z), bf_lo(xb.w), bf_hi(xb.w)};
      layernorm8(fa);
      layernorm8(fb);
      if (g == kq) {
        *(LAS v4u*)(L + L_XN + (2 * hw) * 288 + n16 * 16) = v4u{pack_bf(fa[0], fa[1]), pack_bf(fa[2], fa[3]), pack_bf(fa[4], fa[5]), pack_bf(fa[6], fa[7])};
        *(LAS v4u*)(L + L_XN + (2 * hw + 1) * 288 + n16 * 16) = v4u{pack_bf(fb[0], fb[1]), pack_bf(fb[2], fb[3]), pack_bf(fb[4], fb[5]), pack_bf(fb[6], fb[7])};
      }
    }
    asm volatile("s_waitcnt vmcnt(%0) lgkmcnt(0)" :: "i"((CODE_LINES > 0) + (PRETOUCH > 0)) : "memory");
    STAMP(2);
    if (lane == 0) __hip_atomic_fetch_add((LAS unsigned*)&flags[0], 1u, __ATOMIC_RELAXED, __HIP_MEMORY_SCOPE_WORKGROUP);
    while (flags[0] < 4u) __builtin_amdgcn_s_sleep(1);
    asm volatile("" ::: "memory");
    {
      const lds_p pa = L + L_XN + (lane & 7) * 288 + g * 16;
      const lds_p pb = L + L_WQ + (8 * g + (n16 >> 2)) * 128 + (16 * hw + 4 * (lane & 3)) * 2;
      f32x4 acc = {0.f, 0.f, 0.f, 0.f};
#pragma unroll
      for (int t = 0; t < 4; ++t) acc = mfma(lds_ld128(pa + t * 64), tr_frag(pb + t * 32 * 128, 128), acc);
      publish_tile(acc, rs, WS_QP + (kq * 8 * 4096 + 512 * j + 64 * ct8 + 16 * hw) * 8, 4096 * 8, tag, lane);
    }
    STAMP(3);
    __builtin_amdgcn_s_sleep(QSLEEP);
    {
      const int off = WS_QP + (b * 4096 + 512 * j + 128 * hw + 2 * lane) * 8;
      float q0, q1;
      for (unsigned spins = 0;; ++spins) {
        asm volatile("" ::: "memory");
        v4u v[4];
#pragma unroll
        for (int i = 0; i < 4; ++i) v[i] = __builtin_amdgcn_raw_buffer_load_b128(rs, off + i * 8 * 4096 * 8, 0, AUX_SC1);
        bool ok = true;
        q0 = 0.f; q1 = 0.f;
#pragma unroll
        for (int i = 0; i < 4; ++i) { ok &= (v[i].y == tag) & (v[i].w == tag); q0 += asf(v[i].x); q1 += asf(v[i].z); }
        if (all_ok(ok) || spins > SPIN_CAP) break;
      }
      asm volatile("" : "+v"(pf), "+v"(pt));
      *(LAS unsigned*)(L + L_Q + hw * 320 + lane * 4) = pack_bf(q0, q1);
      if (hw == 0 && lane == 0) flags[1] = tag;
    }
    STAMP(4);
    BAR();
    __builtin_amdgcn_s_setprio(0);
    if (is_wo) {
      const int h = 4 * j + (r >> 2), ns = r & 3;
      const unsigned char* src = (const unsigned char*)wo + ((size_t)(128 * h + 32 * hw + g) * D + 128 * ns) * 2 + n16 * 16;
#pragma unroll
      for (int i = 0; i < 8; ++i) glds16(src + (size_t)i * 4 * D * 2, lds0 + L_W + (32 * hw + 4 * i) * 256);
      STAMP(5);
      asm volatile("s_waitcnt vmcnt(0)" ::: "memory");
      STAMP(6);
      BAR();
    } else if (is_mlp) {
      const int fg = 8 * j + (r - 16);
      const size_t go = ((size_t)(128 * hw + (lane >> 2)) * FF + 32 * fg) * 2 + (lane & 3) * 16;
#pragma unroll
      for (int i = 0; i < 8; ++i) {
        if (i >= 2) asm volatile("s_waitcnt vmcnt(3)" ::: "memory");
        glds16((const unsigned char*)wg + go + (size_t)i * 16 * FF * 2, lds0 + L_GT + (128 * hw + 16 * i) * 64);
        if (i >= 2) asm volatile("s_waitcnt vmcnt(3)" ::: "memory");
        glds16((const unsigned char*)wu + go + (size_t)i * 16 * FF * 2, lds0 + L_UP + (128 * hw + 16 * i) * 64);
      }
      const unsigned char* sd = (const unsigned char*)wd + (size_t)(32 * fg + 8 * hw) * D * 2 + lane * 16;
#pragma unroll
      for (int i = 0; i < 8; ++i) {
        asm volatile("s_waitcnt vmcnt(5)" ::: "memory");
        glds16(sd + (size_t)i * D * 2, lds0 + L_DN + (8 * hw + i) * 1024);
      }
      STAMP(5);
      asm volatile("s_waitcnt vmcnt(0)" ::: "memory");
      STAMP(6);
      BAR();
    }
  }
}

}

extern "C" void kernel_launch(void* const* d_in, const int* in_sizes, int n_in, void* d_out, int out_size, void* d_ws,
                              size_t ws_size, hipStream_t stream) {
  (void)in_sizes; (void)n_in; (void)out_size; (void)ws_size;
  (void)hipFuncSetAttribute((const void*)fused_decode, hipFuncAttributeMaxDynamicSharedMemorySize, LDS_BYTES);
  fused_decode<<<256, NTHR, LDS_BYTES, stream>>>(
      (const unsigned short*)d_in[0], (const unsigned short*)d_in[1], (const unsigned short*)d_in[2], (const unsigned short*)d_in[3],
      (const unsigned short*)d_in[4], (const unsigned short*)d_in[6], (const unsigned short*)d_in[7], (const unsigned short*)d_in[8],
      (unsigned short*)d_out, (unsigned char*)d_ws);
}
```

```cpp
#include <hip/hip_runtime.h>
#include <hip/hip_bf16.h>
#include <stdint.h>

namespace {

typedef unsigned v4u __attribute__((ext_vector_type(4)));
typedef unsigned v2u __attribute__((ext_vector_type(2)));
typedef float f32x4 __attribute__((ext_vector_type(4)));
typedef __bf16 bf16x8 __attribute__((ext_vector_type(8)));
typedef unsigned long long u64;
#define LAS __attribute__((address_space(3)))
typedef LAS unsigned char* lds_p;

constexpr int D = 512, HQ = 32, HKV = 8, SK = 4096, DH = 128, WIN = 1024, FF = 1792;
constexpr int NTHR = 768;
constexpr int AUX_SC1 = 16;
constexpr unsigned SPIN_CAP = 1u << 16;
#define PD 3
#define THRN 12
#define DMAFLAG 1
#define QSLEEP 28
#define HPRIO 2
#define FBF 1

constexpr int WS_EPOCH = 0;
constexpr int WS_QP = 1 << 16;
constexpr int WS_OP = WS_QP + (1 << 20);
constexpr int WS_LS = WS_OP + (1 << 19);
constexpr int WS_RP = WS_LS + (1 << 15);
constexpr int WS_GH = WS_RP + (1 << 20);
constexpr int WS_FP = WS_GH + (1 << 14);
constexpr int WS_END = WS_FP + 256 * 56 * 128;
constexpr int WS_STAMP = 64 << 20;

constexpr int L_RED = 0;
constexpr int L_WQ = 0;
constexpr int L_Q = 16384;
constexpr int L_MAX = L_Q + 4 * 320;
constexpr int L_SUM = L_MAX + 128;
constexpr int L_FLAG = L_SUM + 128;
constexpr int L_OS = L_FLAG + 64;
constexpr int L_HN = L_OS + 8 * 288;
constexpr int L_R3 = L_HN + 8 * 1056;
constexpr int L_HH = L_R3 + 8192;
constexpr int L_W = ((L_HH + 8 * 96 + 255) / 256) * 256;
constexpr int L_GT = L_W, L_UP = L_W + 32768, L_DN = L_W + 65536;
constexpr int LDS_BYTES = L_W + 98304;
static_assert(LDS_BYTES <= 160 * 1024 && LDS_BYTES > 80 * 1024, "one workgroup per CU");

__device__ __forceinline__ float bf_lo(unsigned u) { return __builtin_bit_cast(float, u << 16); }
__device__ __forceinline__ float bf_hi(unsigned u) { return __builtin_bit_cast(float, u & 0xffff0000u); }
__device__ __forceinline__ unsigned pack_bf(float lo, float hi) {
  typedef __bf16 bf2 __attribute__((ext_vector_type(2)));
  bf2 r = {(__bf16)lo, (__bf16)hi};
  return __builtin_bit_cast(unsigned, r);
}
__device__ __forceinline__ float rbf(float f) { return bf_lo(pack_bf(f, f)); }
__device__ __forceinline__ float asf(unsigned u) { return __builtin_bit_cast(float, u); }
__device__ __forceinline__ unsigned asu(float f) { return __builtin_bit_cast(unsigned, f); }
template <int CTRL> __device__ __forceinline__ float dpp(float x) {
  return __builtin_bit_cast(float, __builtin_amdgcn_mov_dpp(__builtin_bit_cast(int, x), CTRL, 0xf, 0xf, true));
}
__device__ __forceinline__ float max3(float a, float b, float c) {
  float r;
  asm("v_max3_f32 %0, %1, %2, %3" : "=v"(r) : "v"(a), "v"(b), "v"(c));
  return r;
}
__device__ __forceinline__ float max2(float a, float b) {
  float r;
  asm("v_max_f32 %0, %1, %2" : "=v"(r) : "v"(a), "v"(b));
  return r;
}
__device__ __forceinline__ float x16_sum(float x) {
  auto s = __builtin_amdgcn_permlane16_swap(asu(x), asu(x), false, false);
  return asf(s[0]) + asf(s[1]);
}
__device__ __forceinline__ float x32_sum(float x) {
  auto s = __builtin_amdgcn_permlane32_swap(asu(x), asu(x), false, false);
  return asf(s[0]) + asf(s[1]);
}
__device__ __forceinline__ float x16_max(float x) {
  auto s = __builtin_amdgcn_permlane16_swap(asu(x), asu(x), false, false);
  return max2(asf(s[0]), asf(s[1]));
}
__device__ __forceinline__ float x32_max(float x) {
  auto s = __builtin_amdgcn_permlane32_swap(asu(x), asu(x), false, false);
  return max2(asf(s[0]), asf(s[1]));
}
__device__ __forceinline__ f32x4 mfma(v4u a, v4u b, f32x4 c) {
  return __builtin_amdgcn_mfma_f32_16x16x32_bf16(__builtin_bit_cast(bf16x8, a), __builtin_bit_cast(bf16x8, b), c, 0, 0, 0);
}
__device__ __forceinline__ v2u tr_read(lds_p p) {
  typedef short v4s __attribute__((ext_vector_type(4)));
  return __builtin_bit_cast(v2u, __builtin_amdgcn_ds_read_tr16_b64_v4i16((LAS v4s*)p));
}
__device__ __forceinline__ v4u tr_frag(lds_p p, int row_stride) {
  const v2u a = tr_read(p), b = tr_read(p + 4 * row_stride);
  return v4u{a.x, a.y, b.x, b.y};
}
__device__ __forceinline__ v4u lds_ld128(lds_p p) { return *(const LAS v4u*)p; }
__device__ __forceinline__ void glds16(const void* gsrc, unsigned lds_dst) {
  unsigned keep;
  asm volatile("s_mov_b32 %0, m0\n\ts_mov_b32 m0, %2\n\ts_nop 0\n\tglobal_load_lds_dwordx4 %1, off nt\n\ts_mov_b32 m0, %0"
               : "=&s"(keep) : "v"(gsrc), "s"(lds_dst) : "memory");
}
#define BAR() do { asm volatile("s_waitcnt lgkmcnt(0)" ::: "memory"); __builtin_amdgcn_s_barrier(); asm volatile("" ::: "memory"); } while (0)
__device__ __forceinline__ void lds_arrive(volatile LAS unsigned* p, int lane) {
  asm volatile("" ::: "memory");
  if (lane == 0) asm volatile("ds_add_u32 %0, %1" :: "v"((unsigned)(size_t)p), "v"(1u) : "memory");
  asm volatile("" ::: "memory");
}
__device__ __forceinline__ void lds_wait(volatile LAS unsigned* p, unsigned n) {
  while (*p < n) { }
  asm volatile("" ::: "memory");
}
__device__ __forceinline__ bool all_ok(bool ok) { return __builtin_amdgcn_ballot_w64(ok) == ~0ull; }

__device__ __forceinline__ void layernorm8(float (&f)[8]) {
  const float c0 = asf(__builtin_amdgcn_readfirstlane(asu(f[0])));
  float s1 = 0.f, s2 = 0.f;
#pragma unroll
  for (int i = 0; i < 8; ++i) { const float d = f[i] - c0; s1 += d; s2 = fmaf(d, d, s2); }
  s1 += dpp<0xB1>(s1); s2 += dpp<0xB1>(s2);
  s1 += dpp<0x4E>(s1); s2 += dpp<0x4E>(s2);
  s1 += dpp<0x141>(s1); s2 += dpp<0x141>(s2);
  s1 += dpp<0x140>(s1); s2 += dpp<0x140>(s2);
  s1 = x16_sum(s1); s2 = x16_sum(s2);
  s1 = x32_sum(s1) * (1.0f / D); s2 = x32_sum(s2) * (1.0f / D);
  const float var = rbf(fmaxf(fmaf(-s1, s1, s2), 0.f));
  const float den = rbf(__builtin_amdgcn_sqrtf(rbf(var + 1e-5f)));
  const float inv = __builtin_amdgcn_rcpf(den), mb = rbf(c0 + s1);
#pragma unroll
  for (int i = 0; i < 8; ++i) f[i] = rbf(f[i] - mb) * inv;
}

__device__ __forceinline__ void publish_tile(f32x4 d, __amdgpu_buffer_rsrc_t rs, int base, int stride_b, unsigned tag, int lane) {
  const bool t = lane & 8;
  const v4u v = {asu(t ? d[2] : d[0]), tag, asu(t ? d[3] : d[1]), tag};
  __builtin_amdgcn_raw_buffer_store_b128(v, rs, base + (lane & 7) * stride_b + (lane >> 4) * 32 + (t ? 16 : 0), 0, AUX_SC1);
}

#define STAMP(i) do { } while (0)

__device__ __forceinline__ void final_cell(__amdgpu_buffer_rsrc_t rs, unsigned tag, int cell, int lane, unsigned short* out, unsigned* ws_epoch) {
  const int bq = cell >> 4, ct = cell & 15, p = lane & 7;
  v4u hv;
  for (unsigned spins = 0;; ++spins) {
    asm volatile("" ::: "memory");
    hv = __builtin_amdgcn_raw_buffer_load_b128(rs, WS_GH + (bq * 256 + 16 * ct + 2 * p) * 8, 0, AUX_SC1);
    if (all_ok((hv.y == tag) & (hv.w == tag)) || spins > SPIN_CAP) break;
    __builtin_amdgcn_s_sleep(2);
  }
  const int off = WS_FP + ((bq * 16 + ct) * 56 + (lane >> 3)) * 128 + p * 16;
  float a0, a1, a2, a3;
  for (unsigned spins = 0;; ++spins) {
    asm volatile("" ::: "memory");
    v4u v[7];
#pragma unroll
    for (int i = 0; i < 7; ++i) v[i] = __builtin_amdgcn_raw_buffer_load_b128(rs, off + i * 8 * 128, 0, AUX_SC1);
    bool ok = true;
    a0 = a1 = a2 = a3 = 0.f;
#pragma unroll
    for (int i = 0; i < 7; ++i) {
      ok &= (v[i].y == tag) & (v[i].w == tag);
      a0 += bf_lo(v[i].x); a1 += bf_hi(v[i].x); a2 += bf_lo(v[i].z); a3 += bf_hi(v[i].z);
    }
    if (all_ok(ok) || spins > SPIN_CAP) break;
  }
  a0 += dpp<0x128>(a0); a1 += dpp<0x128>(a1); a2 += dpp<0x128>(a2); a3 += dpp<0x128>(a3);
  a0 = x16_sum(a0); a1 = x16_sum(a1); a2 = x16_sum(a2); a3 = x16_sum(a3);
  a0 = x32_sum(a0); a1 = x32_sum(a1); a2 = x32_sum(a2); a3 = x32_sum(a3);
  if (lane < 8) {
    const v2u o = {pack_bf(bf_lo(hv.x) + rbf(a0), bf_hi(hv.x) + rbf(a1)), pack_bf(bf_lo(hv.z) + rbf(a2), bf_hi(hv.z) + rbf(a3))};
    *(v2u*)(out + bq * D + 32 * ct + 4 * p) = o;
  }
  if (cell == 0 && lane == 0) __hip_atomic_store(ws_epoch, tag, __ATOMIC_RELAXED, __HIP_MEMORY_SCOPE_AGENT);
}

__global__ void __launch_bounds__(NTHR, 3)
fused_decode(const unsigned short* __restrict__ x, const unsigned short* __restrict__ kc, const unsigned short* __restrict__ vc,
             const unsigned short* __restrict__ wq, const unsigned short* __restrict__ wo, const unsigned short* __restrict__ wg,
             const unsigned short* __restrict__ wu, const unsigned short* __restrict__ wd, unsigned short* __restrict__ out,
             unsigned char* __restrict__ ws) {
  extern __shared__ __attribute__((aligned(16))) unsigned char smem[];
  const lds_p L = (lds_p)smem;
  const unsigned lds0 = (unsigned)(size_t)L;
  const int tid = threadIdx.x, lane = tid & 63;
  const int wv = __builtin_amdgcn_readfirstlane(tid >> 6);
  const int w = wv - 4;
  const int c = blockIdx.x, j = c & 7, r = c >> 3;
  const int b = r >> 2, s = r & 3;
  const int g = lane >> 4, n16 = lane & 15;
  const bool is_wo = r < 16, is_mlp = (r >= 16) && (r < 24) && (j < 7);
  const __amdgpu_buffer_rsrc_t rs = __builtin_amdgcn_make_buffer_rsrc((void*)ws, 0, WS_END, 0x00020000);
  volatile LAS unsigned* flags = (volatile LAS unsigned*)(L + L_FLAG);
  const int fcell = is_wo ? 16 * j + r : -1;
  STAMP(0);

  if (wv >= 4) {
    const size_t tile = ((size_t)(b * HKV + j) * SK + (SK - WIN) + 256 * s + 32 * w) * (DH * 2);
    const unsigned char* kb = (const unsigned char*)kc + tile + 4096;
    const unsigned char* vb = (const unsigned char*)vc + tile;
    const unsigned koff = (lane & 7) * 256 + (2 * g + ((lane >> 3) & 1)) * 16;
    const unsigned voff = (16 * (g >> 1) + 4 * (g & 1)) * 256 + n16 * 16;
    v4u K[8], V[8];
#define LDK(i, imm) asm volatile("global_load_dwordx4 %0, %1, %2 offset:" #imm " nt" : "=v"(K[i]) : "v"(koff), "s"(kb))
#define LDV(i, imm) asm volatile("global_load_dwordx4 %0, %1, %2 offset:" #imm " nt" : "=v"(V[i]) : "v"(voff), "s"(vb))
#define THR(n) do { if (n >= PD && n < THRN) asm volatile("s_waitcnt vmcnt(%0)" :: "i"(PD - 1)); } while (0)
    __builtin_amdgcn_s_barrier();
    LDK(0, -4096); THR(1); LDK(1, -3968); THR(2); LDK(2, -2048); THR(3); LDK(3, -1920);
    THR(4); LDK(4, 0); THR(5); LDK(5, 128); THR(6); LDK(6, 2048); THR(7); LDK(7, 2176);
    THR(8); LDV(0, 0); THR(9); LDV(1, 256); THR(10); LDV(2, 512); THR(11); LDV(3, 768);
    THR(12); LDV(4, 2048); THR(13); LDV(5, 2304); THR(14); LDV(6, 2560); THR(15); LDV(7, 2816);
    STAMP(1);
    BAR();
    STAMP(2);
    const unsigned tag = flags[1];
    const lds_p qa = L + L_Q + (n16 & 3) * 320 + (2 * g + ((n16 >> 2) & 1)) * 16;
    const v4u q0 = lds_ld128(qa), q1 = lds_ld128(qa + 128);
    asm volatile("s_waitcnt vmcnt(8)" : "+v"(K[0]), "+v"(K[1]), "+v"(K[2]), "+v"(K[3]), "+v"(K[4]), "+v"(K[5]), "+v"(K[6]), "+v"(K[7]));
    f32x4 sa[4];
#pragma unroll
    for (int o = 0; o < 4; ++o) {
      sa[o] = mfma(K[2 * o], q0, f32x4{0.f, 0.f, 0.f, 0.f});
      sa[o] = mfma(K[2 * o + 1], q1, sa[o]);
    }
    float sc[8];
    {
      const bool par = lane & 4;
      float raw[8];
#pragma unroll
      for (int p = 0; p < 2; ++p)
#pragma unroll
        for (int i = 0; i < 4; ++i) {
          auto sw = __builtin_amdgcn_permlane32_swap(asu(sa[p][i]), asu(sa[p + 2][i]), false, false);
          const float a = par ? asf(sw[1]) : asf(sw[0]);
          raw[4 * p + i] = a + dpp<0x124>(a);
        }
#pragma unroll
      for (int i = 0; i < 8; i += 2) {
        const unsigned u = pack_bf(raw[i], raw[i + 1]);
        const unsigned t = pack_bf(bf_lo(u) * 0.08838834764831845f, bf_hi(u) * 0.08838834764831845f);
        sc[i] = bf_lo(t); sc[i + 1] = bf_hi(t);
      }
    }
    float m = max3(max3(sc[0], sc[1], sc[2]), max3(sc[3], sc[4], sc[5]), max2(sc[6], sc[7]));
    m = x32_max(x16_max(m));
    float pe[8], ls;
    {
      const float mc = m * 1.4426950408889634f;
#pragma unroll
      for (int i = 0; i < 8; ++i) pe[i] = __builtin_amdgcn_exp2f(fmaf(sc[i], 1.4426950408889634f, -mc));
      ls = ((pe[0] + pe[1]) + (pe[2] + pe[3])) + ((pe[4] + pe[5]) + (pe[6] + pe[7]));
    }
    v4u PA[4];
    {
      const v4u P = {pack_bf(pe[0], pe[1]), pack_bf(pe[2], pe[3]), pack_bf(pe[4], pe[5]), pack_bf(pe[6], pe[7])};
#pragma unroll
      for (int mq = 0; mq < 4; ++mq) {
        const bool on = (n16 >> 2) == mq;
        PA[mq] = v4u{on ? P.x : 0u, on ? P.y : 0u, on ? P.z : 0u, on ? P.w : 0u};
      }
    }
    ls = x32_sum(x16_sum(ls));
    if (lane < 4) {
      ((LAS float*)(L + L_MAX))[w * 4 + lane] = m;
      ((LAS float*)(L + L_SUM))[w * 4 + lane] = ls;
    }
    lds_arrive(flags + 2, lane);
    lds_wait(flags + 2, 8);
    float msc[8];
    {
      const float mw = ((const LAS float*)(L + L_MAX))[lane & 31], lw = ((const LAS float*)(L + L_SUM))[lane & 31];
      float mx = max2(mw, dpp<0x124>(mw));
      mx = max2(mx, dpp<0x128>(mx));
      mx = x16_max(mx);
      const float f = __builtin_amdgcn_exp2f((mw - mx) * 1.4426950408889634f);
      float l = f * lw;
      l += dpp<0x124>(l);
      l += dpp<0x128>(l);
      l = x16_sum(l);
      if (w == 0 && lane < 16) {
        const float lse = mx * 1.4426950408889634f + __builtin_amdgcn_logf(l);
        __builtin_amdgcn_raw_buffer_store_b64(v2u{asu(lse), tag}, rs, WS_LS + ((b * HKV + j) * 4 + s) * 128 + lane * 8, 0, AUX_SC1);
      }
      const float sc_ = f * __builtin_amdgcn_rcpf(l);
#pragma unroll
      for (int i = 0; i < 8; ++i) msc[i] = asf(__builtin_amdgcn_readlane(asu(sc_), 4 * i + (w >> 1)));
    }
    STAMP(4);
    unsigned T[8][4];
#define XPOSE(n_)                                                                                         \
  _Pragma("unroll") for (int mm = 0; mm < 4; ++mm) {                                                      \
    T[2 * mm][n_] = __builtin_amdgcn_perm(V[2 * n_ + 1][mm], V[2 * n_][mm], 0x05040100u);                 \
    T[2 * mm + 1][n_] = __builtin_amdgcn_perm(V[2 * n_ + 1][mm], V[2 * n_][mm], 0x07060302u);             \
  }                                                                                                       \
  asm volatile("" : "+v"(T[0][n_]), "+v"(T[1][n_]), "+v"(T[2][n_]), "+v"(T[3][n_]), "+v"(T[4][n_]), "+v"(T[5][n_]), "+v"(T[6][n_]), "+v"(T[7][n_]));
    int ooff = WS_OP + ((b * HQ + 4 * j + (w >> 1)) * 4 + s) * 512 + (w & 1) * 256 + (lane & 7) * 32 + (lane >> 5) * 16;
    unsigned redr_a = lds0 + L_RED + ((w >> 1) * 128 + (lane >> 3) * 16 + 8 * (w & 1) + (lane & 7)) * 4;
    unsigned redw_a = lds0 + L_RED + w * 2048 + lane * 4;
    asm volatile("" : "+v"(PA[0]), "+v"(PA[1]), "+v"(PA[2]), "+v"(PA[3]), "+v"(ooff), "+v"(redr_a), "+v"(redw_a));
    asm volatile("s_waitcnt vmcnt(6)" : "+v"(V[0]), "+v"(V[1])); XPOSE(0)
    asm volatile("s_waitcnt vmcnt(4)" : "+v"(V[2]), "+v"(V[3])); XPOSE(1)
    asm volatile("s_waitcnt vmcnt(2)" : "+v"(V[4]), "+v"(V[5])); XPOSE(2)
    if (w == 0) asm volatile("s_waitcnt vmcnt(1)" ::: "memory"); else asm volatile("s_waitcnt vmcnt(0)" ::: "memory");
    asm volatile("" : "+v"(V[6]), "+v"(V[7]));
    STAMP(5);
    XPOSE(3)
    {
      f32x4 oa = {0.f, 0.f, 0.f, 0.f}, ob = {0.f, 0.f, 0.f, 0.f};
#pragma unroll
      for (int i = 0; i < 4; ++i) {
        oa = mfma(PA[i], v4u{T[i][0], T[i][1], T[i][2], T[i][3]}, oa);
        ob = mfma(PA[i], v4u{T[i + 4][0], T[i + 4][1], T[i + 4][2], T[i + 4][3]}, ob);
      }
      LAS float* red = (LAS float*)(size_t)redw_a;
#pragma unroll
      for (int hh = 0; hh < 4; ++hh) { red[hh * 128] = oa[hh]; red[hh * 128 + 64] = ob[hh]; }
    }
    lds_arrive(flags + 4, lane);
    STAMP(6);
    const LAS float* redr = (const LAS float*)(size_t)redr_a;
    lds_wait(flags + 4, 8);
    {
      float o = redr[0] * msc[0], o2 = redr[512] * msc[1];
#pragma unroll
      for (int i = 2; i < 8; i += 2) { o = fmaf(redr[i * 512], msc[i], o); o2 = fmaf(redr[(i + 1) * 512], msc[i + 1], o2); }
      o += o2;
      const unsigned pr = pack_bf(o, dpp<0x128>(o));
      auto sw = __builtin_amdgcn_permlane16_swap(pr, pr, false, false);
      if ((lane & 24) == 0) __builtin_amdgcn_raw_buffer_store_b128(v4u{sw[0], tag, sw[1], tag}, rs, ooff, 0, AUX_SC1);
    }
    STAMP(7);

    if (is_wo) {
      const int h = 4 * j + (r >> 2), ns = r & 3;
      float w0, w1, w2, w3;
      {
        const int loff = WS_LS + (w * HKV + j) * 4 * 128 + (h & 3) * 8;
        v2u l0, l1, l2, l3;
        for (unsigned spins = 0;; ++spins) {
          asm volatile("" ::: "memory");
          l0 = __builtin_amdgcn_raw_buffer_load_b64(rs, loff, 0, AUX_SC1);
          l1 = __builtin_amdgcn_raw_buffer_load_b64(rs, loff + 128, 0, AUX_SC1);
          l2 = __builtin_amdgcn_raw_buffer_load_b64(rs, loff + 256, 0, AUX_SC1);
          l3 = __builtin_amdgcn_raw_buffer_load_b64(rs, loff + 384, 0, AUX_SC1);
          if (all_ok((l0.y == tag) & (l1.y == tag) & (l2.y == tag) & (l3.y == tag)) || spins > SPIN_CAP) break;
        }
        const float mx = fmaxf(fmaxf(asf(l0.x), asf(l1.x)), fmaxf(asf(l2.x), asf(l3.x)));
        w0 = __builtin_amdgcn_exp2f(asf(l0.x) - mx); w1 = __builtin_amdgcn_exp2f(asf(l1.x) - mx);
        w2 = __builtin_amdgcn_exp2f(asf(l2.x) - mx); w3 = __builtin_amdgcn_exp2f(asf(l3.x) - mx);
        const float inv = __builtin_amdgcn_rcpf((w0 + w1) + (w2 + w3));
        w0 *= inv; w1 *= inv; w2 *= inv; w3 *= inv;
      }
      BAR();
      v4u wf[4];
      {
        const lds_p p = L + L_W + (8 * g + (n16 >> 2)) * 256 + (16 * w + 4 * (lane & 3)) * 2;
#pragma unroll
        for (int t = 0; t < 4; ++t) wf[t] = tr_frag(p + t * 32 * 256, 256);
      }
      const int doff = WS_OP + (w * HQ + h) * 4 * 512 + lane * 8;
      v2u d0, d1, d2, d3;
      for (unsigned spins = 0;; ++spins) {
        asm volatile("" ::: "memory");
        d0 = __builtin_amdgcn_raw_buffer_load_b64(rs, doff, 0, AUX_SC1);
        d1 = __builtin_amdgcn_raw_buffer_load_b64(rs, doff + 512, 0, AUX_SC1);
        d2 = __builtin_amdgcn_raw_buffer_load_b64(rs, doff + 1024, 0, AUX_SC1);
        d3 = __builtin_amdgcn_raw_buffer_load_b64(rs, doff + 1536, 0, AUX_SC1);
        if (all_ok((d0.y == tag) & (d1.y == tag) & (d2.y == tag) & (d3.y == tag)) || spins > SPIN_CAP) break;
      }
      STAMP(8);
      {
        const float o0 = fmaf(w0, bf_lo(d0.x), w1 * bf_lo(d1.x)) + fmaf(w2, bf_lo(d2.x), w3 * bf_lo(d3.x));
        const float o1 = fmaf(w0, bf_hi(d0.x), w1 * bf_hi(d1.x)) + fmaf(w2, bf_hi(d2.x), w3 * bf_hi(d3.x));
        *(LAS unsigned*)(L + L_OS + w * 288 + lane * 4) = pack_bf(o0, o1);
      }
      BAR();
      {
        const lds_p pa = L + L_OS + (lane & 7) * 288 + g * 16;
        f32x4 acc0 = mfma(wf[0], lds_ld128(pa), f32x4{0.f, 0.f, 0.f, 0.f});
        f32x4 acc1 = mfma(wf[1], lds_ld128(pa + 64), f32x4{0.f, 0.f, 0.f, 0.f});
        acc0 = mfma(wf[2], lds_ld128(pa + 128), acc0);
        acc1 = mfma(wf[3], lds_ld128(pa + 192), acc1);
        publish_tile(acc0 + acc1, rs, WS_RP + ((8 * ns + w) * 32 + h) * 128, 32 * 32 * 128, tag, lane);
      }
      STAMP(9);
      if (w == 0) {
        const int rho = 16 * j + r, bq = rho >> 4, cp = rho & 15;
        const unsigned xv = *(const unsigned*)(x + bq * D + 32 * cp + 2 * n16);
        const int off = WS_RP + ((bq * 32 + 2 * cp + ((lane >> 3) & 1)) * 32 + g) * 128 + (lane & 7) * 16;
        float a0, a1;
        for (unsigned spins = 0;; ++spins) {
          asm volatile("" ::: "memory");
          v4u v[8];
#pragma unroll
          for (int i = 0; i < 8; ++i) v[i] = __builtin_amdgcn_raw_buffer_load_b128(rs, off + i * 4 * 128, 0, AUX_SC1);
          bool ok = true;
          a0 = 0.f; a1 = 0.f;
#pragma unroll
          for (int i = 0; i < 8; ++i) { ok &= (v[i].y == tag) & (v[i].w == tag); a0 += asf(v[i].x); a1 += asf(v[i].z); }
          if (all_ok(ok) || spins > SPIN_CAP) break;
        }
        STAMP(10);
        a0 = x32_sum(x16_sum(a0)); a1 = x32_sum(x16_sum(a1));
        if (lane < 16) {
          const unsigned hv = pack_bf(bf_lo(xv) + rbf(a0), bf_hi(xv) + rbf(a1));
          __builtin_amdgcn_raw_buffer_store_b64(v2u{hv, tag}, rs, WS_GH + (bq * 256 + 16 * cp + lane) * 8, 0, AUX_SC1);
        }
        STAMP(11);
      } else if (w == 1) {
        final_cell(rs, tag, fcell, lane, out, (unsigned*)(ws + WS_EPOCH));
        STAMP(15);
      }
    } else if (is_mlp) {
      const int fg = 8 * j + (r - 16);
      const int kq = w >> 1, cth = w & 1;
      BAR();
      STAMP(8);
      v4u gf[4], uf[4], df[4];
      {
        const lds_p p = L + L_GT + (128 * kq + 8 * g + (n16 >> 2)) * 64 + (16 * cth + 4 * (lane & 3)) * 2;
#pragma unroll
        for (int t = 0; t < 4; ++t) { gf[t] = tr_frag(p + t * 32 * 64, 64); uf[t] = tr_frag(p + 32768 + t * 32 * 64, 64); }
        const lds_p pd = L + L_DN + (8 * g + (n16 >> 2)) * 1024 + (64 * w + 4 * (lane & 3)) * 2;
#pragma unroll
        for (int t = 0; t < 4; ++t) df[t] = tr_frag(pd + t * 32, 1024);
      }
      const int goff = WS_GH + (w * 256 + 2 * lane) * 8;
      v4u h0, h1;
      for (unsigned spins = 0;; ++spins) {
        asm volatile("" ::: "memory");
        h0 = __builtin_amdgcn_raw_buffer_load_b128(rs, goff, 0, AUX_SC1);
        h1 = __builtin_amdgcn_raw_buffer_load_b128(rs, goff + 1024, 0, AUX_SC1);
        const bool ok = (h0.y == tag) & (h0.w == tag) & (h1.y == tag) & (h1.w == tag);
        if (all_ok(ok) || spins > SPIN_CAP) break;
      }
      STAMP(9);
      {
        float f[8] = {bf_lo(h0.x), bf_hi(h0.x), bf_lo(h0.z), bf_hi(h0.z), bf_lo(h1.x), bf_hi(h1.x), bf_lo(h1.z), bf_hi(h1.z)};
        layernorm8(f);
        *(LAS v2u*)(L + L_HN + w * 1056 + lane * 8) = v2u{pack_bf(f[0], f[1]), pack_bf(f[2], f[3])};
        *(LAS v2u*)(L + L_HN + w * 1056 + 512 + lane * 8) = v2u{pack_bf(f[4], f[5]), pack_bf(f[6], f[7])};
      }
      BAR();
      {
        const lds_p pa = L + L_HN + (lane & 7) * 1056 + (128 * kq + 8 * g) * 2;
        f32x4 ag = {0.f, 0.f, 0.f, 0.f}, au = {0.f, 0.f, 0.f, 0.f};
#pragma unroll
        for (int t = 0; t < 4; ++t) {
          const v4u a = lds_ld128(pa + t * 64);
          ag = mfma(gf[t], a, ag);
          au = mfma(uf[t], a, au);
        }
        const bool up = lane & 8;
        const f32x4 v = {up ? au[0] : ag[0], up ? au[1] : ag[1], up ? au[2] : ag[2], up ? au[3] : ag[3]};
        *(LAS f32x4*)((LAS float*)(L + L_R3) + (((kq * 2 + (up ? 1 : 0)) * 2 + cth) * 8 + (lane & 7)) * 16 + 4 * g) = v;
      }
      BAR();
      if (w < 4) {
        const int bb = 2 * w + (lane >> 5), n32 = lane & 31;
        const LAS float* r3 = (const LAS float*)(L + L_R3) + ((n32 >> 4) * 8 + bb) * 16 + (n32 & 15);
        float gg = (r3[0] + r3[512]) + (r3[1024] + r3[1536]);
        float uu = (r3[256] + r3[768]) + (r3[1280] + r3[1792]);
        gg = rbf(gg); uu = rbf(uu);
        const float sg = rbf(__builtin_amdgcn_rcpf(1.0f + __builtin_amdgcn_exp2f(-1.4426950408889634f * gg)));
        *(LAS unsigned short*)(L + L_HH + bb * 96 + n32 * 2) = (unsigned short)pack_bf(sg * uu, 0.f);
      }
      BAR();
      {
        const v4u a = lds_ld128(L + L_HH + (lane & 7) * 96 + g * 16);
#pragma unroll
        for (int tp = 0; tp < 2; ++tp) {
          const f32x4 d0 = mfma(df[2 * tp], a, f32x4{0.f, 0.f, 0.f, 0.f});
          const f32x4 d1 = mfma(df[2 * tp + 1], a, f32x4{0.f, 0.f, 0.f, 0.f});
          const bool t = lane & 8;
          const v4u v = {pack_bf(t ? d1[0] : d0[0], t ? d1[1] : d0[1]), tag, pack_bf(t ? d1[2] : d0[2], t ? d1[3] : d0[3]), tag};
          const int off = WS_FP + (((lane & 7) * 16 + 2 * w + tp) * 56 + fg) * 128 + (t ? 64 : 0) + g * 16;
          __builtin_amdgcn_raw_buffer_store_b128(v, rs, off, 0, AUX_SC1);
        }
      }
      STAMP(10);
      if (fcell >= 0 && w == 7) {
        final_cell(rs, tag, fcell, lane, out, (unsigned*)(ws + WS_EPOCH));
        STAMP(15);
      }
    } else {
      if (fcell >= 0 && w == 1) {
        final_cell(rs, tag, fcell, lane, out, (unsigned*)(ws + WS_EPOCH));
        STAMP(15);
      }
    }
  } else {
    const int hw = wv;
    __builtin_amdgcn_s_setprio(HPRIO);
    const int kq = r >> 3, ct8 = r & 7;
    v4u xa, xb; unsigned ep;
    {
      const unsigned zero = 0;
      const unsigned xo = (2 * hw) * (D * 2) + lane * 16;
      asm volatile("global_load_dwordx4 %0, %1, %2" : "=v"(xa) : "v"(xo), "s"(x));
      asm volatile("global_load_dwordx4 %0, %1, %2 offset:1024" : "=v"(xb) : "v"(xo), "s"(x));
      const unsigned char* src = (const unsigned char*)wq + ((size_t)(128 * kq + 32 * hw + (lane >> 3)) * (HQ * DH) + 512 * j + 64 * ct8) * 2 + (lane & 7) * 16;
#pragma unroll
      for (int i = 0; i < 4; ++i) glds16(src + (size_t)i * 8 * (HQ * DH) * 2, lds0 + L_WQ + (32 * hw + 8 * i) * 128);
      asm volatile("global_load_dword %0, %1, %2" : "=v"(ep) : "v"(zero), "s"(ws));
    }
    if (hw == 0 && lane < 8) flags[lane] = 0;
    BAR();
    asm volatile("s_waitcnt vmcnt(5)" : "+v"(xa), "+v"(xb));
    STAMP(1);
    {
      float fa[8] = {bf_lo(xa.x), bf_hi(xa.x), bf_lo(xa.y), bf_hi(xa.y), bf_lo(xa.z), bf_hi(xa.z), bf_lo(xa.w), bf_hi(xa.w)};
      float fb[8] = {bf_lo(xb.x), bf_hi(xb.x), bf_lo(xb.y), bf_hi(xb.y), bf_lo(xb.z), bf_hi(xb.z), bf_lo(xb.w), bf_hi(xb.w)};
      layernorm8(fa);
      layernorm8(fb);
      if (g == kq) {
        *(LAS v4u*)(L + L_HN + (2 * hw) * 288 + n16 * 16) = v4u{pack_bf(fa[0], fa[1]), pack_bf(fa[2], fa[3]), pack_bf(fa[4], fa[5]), pack_bf(fa[6], fa[7])};
        *(LAS v4u*)(L + L_HN + (2 * hw + 1) * 288 + n16 * 16) = v4u{pack_bf(fb[0], fb[1]), pack_bf(fb[2], fb[3]), pack_bf(fb[4], fb[5]), pack_bf(fb[6], fb[7])};
      }
    }
    asm volatile("s_waitcnt vmcnt(0)" : "+v"(ep) :: "memory");
    STAMP(2);
    const unsigned tag = ep + 1;
    lds_arrive(flags + 0, lane);
    lds_wait(flags + 0, 4);
    {
      const lds_p pa = L + L_HN + (lane & 7) * 288 + g * 16;
      const lds_p pb = L + L_WQ + (8 * g + (n16 >> 2)) * 128 + (16 * hw + 4 * (lane & 3)) * 2;
      f32x4 acc0 = mfma(tr_frag(pb, 128), lds_ld128(pa), f32x4{0.f, 0.f, 0.f, 0.f});
      f32x4 acc1 = mfma(tr_frag(pb + 32 * 128, 128), lds_ld128(pa + 64), f32x4{0.f, 0.f, 0.f, 0.f});
      acc0 = mfma(tr_frag(pb + 64 * 128, 128), lds_ld128(pa + 128), acc0);
      acc1 = mfma(tr_frag(pb + 96 * 128, 128), lds_ld128(pa + 192), acc1);
      publish_tile(acc0 + acc1, rs, WS_QP + (kq * 8 * 4096 + 512 * j + 64 * ct8 + 16 * hw) * 8, 4096 * 8, tag, lane);
    }
    STAMP(3);
    __builtin_amdgcn_s_sleep(QSLEEP);
    {
      const int off = WS_QP + (b * 4096 + 512 * j + 128 * hw + 2 * lane) * 8;
      float q0, q1;
      for (unsigned spins = 0;; ++spins) {
        asm volatile("" ::: "memory");
        v4u v[4];
#pragma unroll
        for (int i = 0; i < 4; ++i) v[i] = __builtin_amdgcn_raw_buffer_load_b128(rs, off + i * 8 * 4096 * 8, 0, AUX_SC1);
        bool ok = true;
        q0 = 0.f; q1 = 0.f;
#pragma unroll
        for (int i = 0; i < 4; ++i) { ok &= (v[i].y == tag) & (v[i].w == tag); q0 += asf(v[i].x); q1 += asf(v[i].z); }
        if (all_ok(ok) || spins > SPIN_CAP) break;
      }
      *(LAS unsigned*)(L + L_Q + hw * 320 + lane * 4) = pack_bf(q0, q1);
      if (hw == 0 && lane == 0) flags[1] = tag;
    }
    STAMP(4);
    BAR();
    __builtin_amdgcn_s_setprio(0);
    if (is_wo || is_mlp) { while (flags[4] < DMAFLAG) __builtin_amdgcn_s_sleep(2); }
    if (is_wo) {
      const int h = 4 * j + (r >> 2), ns = r & 3;
      const unsigned char* src = (const unsigned char*)wo + ((size_t)(128 * h + 32 * hw + g) * D + 128 * ns) * 2 + n16 * 16;
#pragma unroll
      for (int i = 0; i < 8; ++i) glds16(src + (size_t)i * 4 * D * 2, lds0 + L_W + (32 * hw + 4 * i) * 256);
      STAMP(5);
      asm volatile("s_waitcnt vmcnt(0)" ::: "memory");
      STAMP(6);
      BAR();
    } else if (is_mlp) {
      const int fg = 8 * j + (r - 16);
      const size_t go = ((size_t)(128 * hw + (lane >> 2)) * FF + 32 * fg) * 2 + (lane & 3) * 16;
#pragma unroll
      for (int i = 0; i < 8; ++i) {
        if (i >= 2) asm volatile("s_waitcnt vmcnt(3)" ::: "memory");
        glds16((const unsigned char*)wg + go + (size_t)i * 16 * FF * 2, lds0 + L_GT + (128 * hw + 16 * i) * 64);
        if (i >= 2) asm volatile("s_waitcnt vmcnt(3)" ::: "memory");
        glds16((const unsigned char*)wu + go + (size_t)i * 16 * FF * 2, lds0 + L_UP + (128 * hw + 16 * i) * 64);
      }
      const unsigned char* sd = (const unsigned char*)wd + (size_t)(32 * fg + 8 * hw) * D * 2 + lane * 16;
#pragma unroll
      for (int i = 0; i < 8; ++i) {
        asm volatile("s_waitcnt vmcnt(5)" ::: "memory");
        glds16(sd + (size_t)i * D * 2, lds0 + L_DN + (8 * hw + i) * 1024);
      }
      STAMP(5);
      asm volatile("s_waitcnt vmcnt(0)" ::: "memory");
      STAMP(6);
      BAR();
    }
  }
}

}

extern "C" void kernel_launch(void* const* d_in, const int* in_sizes, int n_in, void* d_out, int out_size, void* d_ws,
                              size_t ws_size, hipStream_t stream) {
  (void)in_sizes; (void)n_in; (void)out_size; (void)ws_size;
  (void)hipFuncSetAttribute((const void*)fused_decode, hipFuncAttributeMaxDynamicSharedMemorySize, LDS_BYTES);
  fused_decode<<<256, NTHR, LDS_BYTES, stream>>>(
      (const unsigned short*)d_in[0], (const unsigned short*)d_in[1], (const unsigned short*)d_in[2], (const unsigned short*)d_in[3],
      (const unsigned short*)d_in[4], (const unsigned short*)d_in[6], (const unsigned short*)d_in[7], (const unsigned short*)d_in[8],
      (unsigned short*)d_out, (unsigned char*)d_ws);
}
```

```cpp
#include <hip/hip_runtime.h>
#include <hip/hip_bf16.h>
#include <stdint.h>

namespace {

typedef unsigned v4u __attribute__((ext_vector_type(4)));
typedef unsigned v2u __attribute__((ext_vector_type(2)));
typedef float f32x4 __attribute__((ext_vector_type(4)));
typedef __bf16 bf16x8 __attribute__((ext_vector_type(8)));
typedef unsigned long long u64;
#define LAS __attribute__((address_space(3)))
typedef LAS unsigned char* lds_p;

constexpr int D = 512, HQ = 32, HKV = 8, SK = 4096, DH = 128, WIN = 1024, FF = 1792;
constexpr int NTHR = 768;
constexpr int AUX_SC1 = 16;
constexpr unsigned SPIN_CAP = 1u << 16;
#define PD 3
#define THRN 12
#define DMAFLAG 2
#define ABL_NOQG 0
#define ABL_NOQP 0
#define ABL_NOW 0
#define WS_TOP 0
#define LSELATE 9
#define QBUF 0
#define ARR3 0
#define CODE_LINES 118
#define FDELAY 0
#define RDELAY 0
#define GSENT 0
#define TDELAY 0
#define UNTHR 0
#define WODELAY 0
#define RBF 1
#define QDELAY 310

#define HPRIO 2

constexpr int WS_EPOCH = 0;
#define PAD 1
constexpr int QP_B = 256 + PAD, QP_K = 8 * QP_B;
constexpr int RP_C = 32 + PAD;
constexpr int FP_C = 56 + PAD;
constexpr int WS_QP = 1 << 16;
constexpr int WS_OP = WS_QP + 4 * QP_K * 128;
constexpr int WS_LS = WS_OP + (1 << 19);
constexpr int WS_RP = WS_LS + (1 << 15);
constexpr int WS_GH = WS_RP + 128 * RP_C * 128;
constexpr int WS_FP = WS_GH + (1 << 14);
constexpr int WS_END = WS_FP + 128 * FP_C * 128;
constexpr long long WS_STAMP = 64ll << 20;

constexpr int L_RED = 0;
constexpr int L_WQ = 0;
constexpr int L_Q = 16384;
constexpr int L_MAX = L_Q + 4 * 320;
constexpr int L_SUM = L_MAX + 128;
constexpr int L_FLAG = L_SUM + 128;
constexpr int L_MSC = L_FLAG + 64;
constexpr int L_LW = L_MSC + 128;
constexpr int L_CS = L_LW + 128;
constexpr int L_ST = L_CS + 256;
constexpr int L_OS = L_ST + 64;
constexpr int L_HN = L_OS + 8 * 288;
constexpr int L_R3 = L_HN + 8 * 1056;
constexpr int L_HH = L_R3 + 8192;
constexpr int L_W = ((L_HH + 8 * 96 + 255) / 256) * 256;
constexpr int L_GT = L_W, L_UP = L_W + 32768, L_DN = L_W + 65536;
constexpr int LDS_BYTES = L_W + 98304;
static_assert(LDS_BYTES <= 160 * 1024 && LDS_BYTES > 80 * 1024, "one workgroup per CU");

__device__ __forceinline__ float bf_lo(unsigned u) { return __builtin_bit_cast(float, u << 16); }
__device__ __forceinline__ float bf_hi(unsigned u) { return __builtin_bit_cast(float, u & 0xffff0000u); }
__device__ __forceinline__ unsigned pack_bf(float lo, float hi) {
  typedef __bf16 bf2 __attribute__((ext_vector_type(2)));
  bf2 r = {(__bf16)lo, (__bf16)hi};
  return __builtin_bit_cast(unsigned, r);
}
__device__ __forceinline__ float rbf(float f) { return bf_lo(pack_bf(f, f)); }
__device__ __forceinline__ float asf(unsigned u) { return __builtin_bit_cast(float, u); }
__device__ __forceinline__ unsigned asu(float f) { return __builtin_bit_cast(unsigned, f); }
template <int CTRL> __device__ __forceinline__ float dpp(float x) {
  return __builtin_bit_cast(float, __builtin_amdgcn_mov_dpp(__builtin_bit_cast(int, x), CTRL, 0xf, 0xf, true));
}
__device__ __forceinline__ float max3(float a, float b, float c) {
  float r;
  asm("v_max3_f32 %0, %1, %2, %3" : "=v"(r) : "v"(a), "v"(b), "v"(c));
  return r;
}
__device__ __forceinline__ float max2(float a, float b) {
  float r;
  asm("v_max_f32 %0, %1, %2" : "=v"(r) : "v"(a), "v"(b));
  return r;
}
__device__ __forceinline__ float x16_sum(float x) {
  auto s = __builtin_amdgcn_permlane16_swap(asu(x), asu(x), false, false);
  return asf(s[0]) + asf(s[1]);
}
__device__ __forceinline__ float x32_sum(float x) {
  auto s = __builtin_amdgcn_permlane32_swap(asu(x), asu(x), false, false);
  return asf(s[0]) + asf(s[1]);
}
__device__ __forceinline__ float x16_max(float x) {
  auto s = __builtin_amdgcn_permlane16_swap(asu(x), asu(x), false, false);
  return max2(asf(s[0]), asf(s[1]));
}
__device__ __forceinline__ float x32_max(float x) {
  auto s = __builtin_amdgcn_permlane32_swap(asu(x), asu(x), false, false);
  return max2(asf(s[0]), asf(s[1]));
}
__device__ __forceinline__ f32x4 mfma(v4u a, v4u b, f32x4 c) {
  return __builtin_amdgcn_mfma_f32_16x16x32_bf16(__builtin_bit_cast(bf16x8, a), __builtin_bit_cast(bf16x8, b), c, 0, 0, 0);
}
__device__ __forceinline__ v2u tr_read(lds_p p) {
  typedef short v4s __attribute__((ext_vector_type(4)));
  return __builtin_bit_cast(v2u, __builtin_amdgcn_ds_read_tr16_b64_v4i16((LAS v4s*)p));
}
__device__ __forceinline__ v4u tr_frag(lds_p p, int row_stride) {
  const v2u a = tr_read(p), b = tr_read(p + 4 * row_stride);
  return v4u{a.x, a.y, b.x, b.y};
}
__device__ __forceinline__ v4u lds_ld128(lds_p p) { return *(const LAS v4u*)p; }
__device__ __forceinline__ void glds16(const void* gsrc, unsigned lds_dst) {
  unsigned keep;
  asm volatile("s_mov_b32 %0, m0\n\ts_mov_b32 m0, %2\n\ts_nop 0\n\tglobal_load_lds_dwordx4 %1, off nt\n\ts_mov_b32 m0, %0"
               : "=&s"(keep) : "v"(gsrc), "s"(lds_dst) : "memory");
}
#define BAR() do { asm volatile("s_waitcnt lgkmcnt(0)" ::: "memory"); __builtin_amdgcn_s_barrier(); asm volatile("" ::: "memory"); } while (0)
__device__ __forceinline__ void lds_arrive(volatile LAS unsigned* p, int lane) {
  asm volatile("" ::: "memory");
  if (lane == 0) asm volatile("ds_add_u32 %0, %1" :: "v"((unsigned)(size_t)p), "v"(1u) : "memory");
  asm volatile("" ::: "memory");
}
__device__ __forceinline__ void lds_wait(volatile LAS unsigned* p, unsigned n) {
  while (*p < n) { }
  asm volatile("" ::: "memory");
}
typedef unsigned long long mask_t;
#define STICKY 1
#define MK(m) (STICKY ? (m) : ~0ull)
__device__ __forceinline__ mask_t bad4(v4u v, unsigned tag) { return __builtin_amdgcn_ballot_w64(v.y != tag) | __builtin_amdgcn_ballot_w64(v.w != tag); }
__device__ __forceinline__ mask_t bad2(v2u v, unsigned tag) { return __builtin_amdgcn_ballot_w64(v.y != tag); }
#define PL4(n_, o_, m_) "s_mov_b64 exec, %" #m_ "\n\tglobal_load_dwordx4 %" #n_ ", %" #o_ ", %[base] sc1\n\t"
#define PL2(n_, o_, m_) "s_mov_b64 exec, %" #m_ "\n\tglobal_load_dwordx2 %" #n_ ", %" #o_ ", %[base] sc1\n\t"
#define PLEND "s_mov_b64 exec, -1\n\ts_waitcnt vmcnt(0)"
__device__ __forceinline__ void poll1x4(const unsigned char* ws, unsigned tag, unsigned o0, v4u& a, bool nap) {
  a = v4u{0u, ~tag, 0u, ~tag};
  for (unsigned spins = 0; spins <= SPIN_CAP; ++spins) {
    const mask_t m0 = bad4(a, tag);
    if (m0 == 0) break;
    asm volatile(PL4(0, 1, 2) PLEND : "+v"(a) : "v"(o0), "s"(MK(m0)), [base] "s"(ws) : "memory");
    if (nap) __builtin_amdgcn_s_sleep(2);
  }
}
__device__ __forceinline__ void poll1x2(const unsigned char* ws, unsigned tag, unsigned o0, v2u& a, bool nap) {
  a = v2u{0u, ~tag};
  for (unsigned spins = 0; spins <= SPIN_CAP; ++spins) {
    const mask_t m0 = bad2(a, tag);
    if (m0 == 0) break;
    asm volatile(PL2(0, 1, 2) PLEND : "+v"(a) : "v"(o0), "s"(MK(m0)), [base] "s"(ws) : "memory");
    if (nap) __builtin_amdgcn_s_sleep(2);
  }
}
__device__ __forceinline__ void poll2x4(const unsigned char* ws, unsigned tag, unsigned o0, unsigned o1, v4u& a, v4u& b) {
  a = b = v4u{0u, ~tag, 0u, ~tag};
  for (unsigned spins = 0; spins <= SPIN_CAP; ++spins) {
    const mask_t m0 = bad4(a, tag), m1 = bad4(b, tag);
    if ((m0 | m1) == 0) break;
    asm volatile(PL4(0, 2, 4) PL4(1, 3, 5) PLEND : "+v"(a), "+v"(b) : "v"(o0), "v"(o1), "s"(MK(m0)), "s"(MK(m1)), [base] "s"(ws) : "memory");
  }
}
__device__ __forceinline__ void poll4x2(const unsigned char* ws, unsigned tag, unsigned o0, unsigned o1, unsigned o2, unsigned o3, v2u (&v)[4]) {
#pragma unroll
  for (int i = 0; i < 4; ++i) v[i] = v2u{0u, ~tag};
  for (unsigned spins = 0; spins <= SPIN_CAP; ++spins) {
    const mask_t m0 = bad2(v[0], tag), m1 = bad2(v[1], tag), m2 = bad2(v[2], tag), m3 = bad2(v[3], tag);
    if ((m0 | m1 | m2 | m3) == 0) break;
    asm volatile(PL2(0, 4, 8) PL2(1, 5, 9) PL2(2, 6, 10) PL2(3, 7, 11) PLEND
                 : "+v"(v[0]), "+v"(v[1]), "+v"(v[2]), "+v"(v[3])
                 : "v"(o0), "v"(o1), "v"(o2), "v"(o3), "s"(MK(m0)), "s"(MK(m1)), "s"(MK(m2)), "s"(MK(m3)), [base] "s"(ws) : "memory");
  }
}
__device__ __forceinline__ void poll8x2(const unsigned char* ws, unsigned tag, unsigned oa, unsigned ob, v2u (&v)[8]) {
  unsigned o[8];
#pragma unroll
  for (int i = 0; i < 8; ++i) { v[i] = v2u{0u, ~tag}; o[i] = i < 4 ? oa + i * 512 : ob + (i - 4) * 128; }
  for (unsigned spins = 0; spins <= SPIN_CAP; ++spins) {
    mask_t m[8], any = 0;
#pragma unroll
    for (int i = 0; i < 8; ++i) { m[i] = bad2(v[i], tag); any |= m[i]; }
    if (any == 0) break;
    asm volatile(PL2(0, 8, 16) PL2(1, 9, 17) PL2(2, 10, 18) PL2(3, 11, 19) PL2(4, 12, 20) PL2(5, 13, 21) PL2(6, 14, 22) PL2(7, 15, 23) PLEND
                 : "+v"(v[0]), "+v"(v[1]), "+v"(v[2]), "+v"(v[3]), "+v"(v[4]), "+v"(v[5]), "+v"(v[6]), "+v"(v[7])
                 : "v"(o[0]), "v"(o[1]), "v"(o[2]), "v"(o[3]), "v"(o[4]), "v"(o[5]), "v"(o[6]), "v"(o[7]),
                   "s"(MK(m[0])), "s"(MK(m[1])), "s"(MK(m[2])), "s"(MK(m[3])), "s"(MK(m[4])), "s"(MK(m[5])), "s"(MK(m[6])), "s"(MK(m[7])), [base] "s"(ws) : "memory");
  }
}
template <int N, int STEP>
__device__ __forceinline__ void pollNx4(const unsigned char* ws, unsigned tag, unsigned o0, v4u (&v)[8]) {
  unsigned o[8];
#pragma unroll
  for (int i = 0; i < 8; ++i) { v[i] = i < N ? v4u{0u, ~tag, 0u, ~tag} : v4u{0u, tag, 0u, tag}; o[i] = o0 + (i < N ? i : 0) * STEP; }
  for (unsigned spins = 0; spins <= SPIN_CAP; ++spins) {
    mask_t m[8], any = 0;
#pragma unroll
    for (int i = 0; i < 8; ++i) { m[i] = i < N ? bad4(v[i], tag) : 0; any |= m[i]; }
    if (any == 0) break;
    if constexpr (N == 4) {
      asm volatile(PL4(0, 4, 8) PL4(1, 5, 9) PL4(2, 6, 10) PL4(3, 7, 11) PLEND
                   : "+v"(v[0]), "+v"(v[1]), "+v"(v[2]), "+v"(v[3])
                   : "v"(o[0]), "v"(o[1]), "v"(o[2]), "v"(o[3]), "s"(MK(m[0])), "s"(MK(m[1])), "s"(MK(m[2])), "s"(MK(m[3])), [base] "s"(ws) : "memory");
    } else if constexpr (N == 7) {
      asm volatile(PL4(0, 7, 14) PL4(1, 8, 15) PL4(2, 9, 16) PL4(3, 10, 17) PL4(4, 11, 18) PL4(5, 12, 19) PL4(6, 13, 20) PLEND
                   : "+v"(v[0]), "+v"(v[1]), "+v"(v[2]), "+v"(v[3]), "+v"(v[4]), "+v"(v[5]), "+v"(v[6])
                   : "v"(o[0]), "v"(o[1]), "v"(o[2]), "v"(o[3]), "v"(o[4]), "v"(o[5]), "v"(o[6]),
                     "s"(MK(m[0])), "s"(MK(m[1])), "s"(MK(m[2])), "s"(MK(m[3])), "s"(MK(m[4])), "s"(MK(m[5])), "s"(MK(m[6])), [base] "s"(ws) : "memory");
    } else {
      asm volatile(PL4(0, 8, 16) PL4(1, 9, 17) PL4(2, 10, 18) PL4(3, 11, 19) PL4(4, 12, 20) PL4(5, 13, 21) PL4(6, 14, 22) PL4(7, 15, 23) PLEND
                   : "+v"(v[0]), "+v"(v[1]), "+v"(v[2]), "+v"(v[3]), "+v"(v[4]), "+v"(v[5]), "+v"(v[6]), "+v"(v[7])
                   : "v"(o[0]), "v"(o[1]), "v"(o[2]), "v"(o[3]), "v"(o[4]), "v"(o[5]), "v"(o[6]), "v"(o[7]),
                     "s"(MK(m[0])), "s"(MK(m[1])), "s"(MK(m[2])), "s"(MK(m[3])), "s"(MK(m[4])), "s"(MK(m[5])), "s"(MK(m[6])), "s"(MK(m[7])), [base] "s"(ws) : "memory");
    }
  }
}

__device__ __forceinline__ void layernorm8(float (&f)[8]) {
  const float c0 = asf(__builtin_amdgcn_readfirstlane(asu(f[0])));
  float s1 = 0.f, s2 = 0.f;
#pragma unroll
  for (int i = 0; i < 8; ++i) { const float d = f[i] - c0; s1 += d; s2 = fmaf(d, d, s2); }
  s1 += dpp<0xB1>(s1); s2 += dpp<0xB1>(s2);
  s1 += dpp<0x4E>(s1); s2 += dpp<0x4E>(s2);
  s1 += dpp<0x141>(s1); s2 += dpp<0x141>(s2);
  s1 += dpp<0x140>(s1); s2 += dpp<0x140>(s2);
  s1 = x16_sum(s1); s2 = x16_sum(s2);
  s1 = x32_sum(s1) * (1.0f / D); s2 = x32_sum(s2) * (1.0f / D);
  const float var = rbf(fmaxf(fmaf(-s1, s1, s2), 0.f));
  const float den = rbf(__builtin_amdgcn_sqrtf(rbf(var + 1e-5f)));
  const float inv = __builtin_amdgcn_rcpf(den), mb = rbf(c0 + s1);
#pragma unroll
  for (int i = 0; i < 8; ++i) f[i] = rbf(f[i] - mb) * inv;
}

__device__ __forceinline__ void publish_tile(f32x4 d, __amdgpu_buffer_rsrc_t rs, int base, int stride_b, unsigned tag, int lane) {
  const bool t = lane & 8;
  const int src = ((lane >> 3) + 8 * (lane & 1) + 16 * ((lane >> 1) & 3)) * 4;
  const unsigned v0 = (unsigned)__builtin_amdgcn_ds_bpermute(src, (int)asu(t ? d[2] : d[0]));
  const unsigned v1 = (unsigned)__builtin_amdgcn_ds_bpermute(src, (int)asu(t ? d[3] : d[1]));
  __builtin_amdgcn_raw_buffer_store_b128(v4u{v0, tag, v1, tag}, rs, base + (lane >> 3) * stride_b + (lane & 7) * 16, 0, AUX_SC1);
}

#define STAMP(i) do { } while (0)

#define FSTAMP
__device__ __forceinline__ void final_cell(const unsigned char* ws, unsigned tag, int cell, int lane, unsigned short* out, unsigned* ws_epoch, u64* tf) {
  const int p = lane & 7, row = lane >> 4;
  v4u hv;
  poll1x4(ws, tag, WS_GH + cell * 128 + p * 16, hv, true);
  float hsel = row == 0 ? bf_lo(hv.x) : row == 1 ? bf_hi(hv.x) : row == 2 ? bf_lo(hv.z) : bf_hi(hv.z);
  unsigned short* po = out + p * D + 4 * cell + row;
  asm volatile("" : "+v"(hsel), "+v"(po));
  if (FDELAY) __builtin_amdgcn_s_sleep(FDELAY);
  v4u v[8];
  pollNx4<7, 8 * 128>(ws, tag, WS_FP + (cell * FP_C + (lane >> 3)) * 128 + p * 16, v);
  FSTAMP;
  float a0 = 0.f, a1 = 0.f, a2 = 0.f, a3 = 0.f;
#pragma unroll
  for (int i = 0; i < 7; ++i) { a0 += bf_lo(v[i].x); a1 += bf_hi(v[i].x); a2 += bf_lo(v[i].z); a3 += bf_hi(v[i].z); }
  auto s01 = __builtin_amdgcn_permlane16_swap(asu(a0), asu(a1), false, false);
  auto s23 = __builtin_amdgcn_permlane16_swap(asu(a2), asu(a3), false, false);
  const float c0 = asf(s01[0]) + asf(s01[1]), c1 = asf(s23[0]) + asf(s23[1]);
  auto sc = __builtin_amdgcn_permlane32_swap(asu(c0), asu(c1), false, false);
  float e = asf(sc[0]) + asf(sc[1]);
  e += dpp<0x128>(e);
  if ((lane & 8) == 0) *po = (unsigned short)pack_bf(hsel + rbf(e), 0.f);
  if (cell == 0 && lane == 0) __hip_atomic_store(ws_epoch, tag, __ATOMIC_RELAXED, __HIP_MEMORY_SCOPE_AGENT);
}

__global__ void __launch_bounds__(NTHR, 3)
fused_decode(const unsigned short* __restrict__ x, const unsigned short* __restrict__ kc, const unsigned short* __restrict__ vc,
             const unsigned short* __restrict__ wq, const unsigned short* __restrict__ wo, const unsigned short* __restrict__ wg,
             const unsigned short* __restrict__ wu, const unsigned short* __restrict__ wd, unsigned short* __restrict__ out,
             unsigned char* __restrict__ ws) {
  unsigned long long code_base;
  asm volatile("s_getpc_b64 %0" : "=s"(code_base));
  code_base &= ~0xffull;
  extern __shared__ __attribute__((aligned(16))) unsigned char smem[];
  const lds_p L = (lds_p)smem;
  const unsigned lds0 = (unsigned)(size_t)L;
  const int tid = threadIdx.x, lane = tid & 63;
  const int wv = __builtin_amdgcn_readfirstlane(tid >> 6);
  const int w = wv - 4;
  const int c = blockIdx.x, j = c & 7, r = c >> 3;
  const int b = r >> 2, s = r & 3;
  const int g = lane >> 4, n16 = lane & 15;
  const bool is_wo = r < 16, is_mlp = (r >= 16) && (j < 7);
  const __amdgpu_buffer_rsrc_t rs = __builtin_amdgcn_make_buffer_rsrc((void*)ws, 0, WS_END, 0x00020000);
  volatile LAS unsigned* flags = (volatile LAS unsigned*)(L + L_FLAG);
  const int fcell = is_wo ? 16 * j + r : -1;
  STAMP(0);

  const u64 t_start = __builtin_amdgcn_s_memrealtime();
  const int kq0 = r >> 3, ct8 = r & 7;
  v4u xa, xb; unsigned ep, pf;
  asm volatile("" : "=v"(xa), "=v"(xb), "=v"(ep), "=v"(pf));
  if (wv < 4) {
    const int hw = wv;
    __builtin_amdgcn_s_setprio(HPRIO);
    const unsigned zero = 0;
    const unsigned xo = (2 * hw) * (D * 2) + lane * 16;
    asm volatile("global_load_dwordx4 %0, %1, %2" : "+v"(xa) : "v"(xo), "s"(x));
    asm volatile("global_load_dwordx4 %0, %1, %2 offset:1024" : "+v"(xb) : "v"(xo), "s"(x));
    const unsigned char* src = (const unsigned char*)wq + ((size_t)(128 * kq0 + 32 * hw + (lane >> 3)) * (HQ * DH) + 512 * j + 64 * ct8) * 2 + (lane & 7) * 16;
#pragma unroll
    for (int i = 0; i < 4; ++i) glds16(src + (size_t)i * 8 * (HQ * DH) * 2, lds0 + L_WQ + (32 * hw + 8 * i) * 128);
    asm volatile("global_load_dword %0, %1, %2" : "+v"(ep) : "v"(zero), "s"(ws));
    const unsigned po = min((r * 4 + hw) * 2 + (lane & 1), CODE_LINES - 1) * 128;
    asm volatile("global_load_dword %0, %1, %2" : "+v"(pf) : "v"(po), "s"(code_base));
  }
  int wv2 = wv;
  asm volatile("" : "+s"(wv2));

  if (__builtin_expect(wv2 >= 4, 1)) {
    const size_t tile = ((size_t)(b * HKV + j) * SK + (SK - WIN) + 256 * s + 32 * w) * (DH * 2);
    const unsigned char* kb = (const unsigned char*)kc + tile + 4096;
    const unsigned char* vb = (const unsigned char*)vc + tile;
    const unsigned koff = (lane & 7) * 256 + (2 * g + ((lane >> 3) & 1)) * 16;
    const unsigned voff = (16 * (g >> 1) + 4 * (g & 1)) * 256 + n16 * 16;
    v4u K[8], V[8];
#define LDK(i, imm) asm volatile("global_load_dwordx4 %0, %1, %2 offset:" #imm " nt" : "=v"(K[i]) : "v"(koff), "s"(kb))
#define LDV(i, imm) asm volatile("global_load_dwordx4 %0, %1, %2 offset:" #imm " nt" : "=v"(V[i]) : "v"(voff), "s"(vb))
#define THR(n) do { if (n >= PD && n < THRN && (UNTHR == 0 || n < UNTHR || __builtin_amdgcn_readfirstlane(flags[7]) == 0)) asm volatile("s_waitcnt vmcnt(%0)" :: "i"(PD - 1)); } while (0)
    LDK(0, -4096); LDK(1, -3968);
    if (PD > 2) LDK(2, -2048);
    if (w == 0 && lane < 8) flags[lane] = 0;
    BAR();
    if (PD <= 2) { THR(2); LDK(2, -2048); }
    THR(3); LDK(3, -1920);
    THR(4); LDK(4, 0); THR(5); LDK(5, 128); THR(6); LDK(6, 2048); THR(7); LDK(7, 2176);
    THR(8); LDV(0, 0); THR(9); LDV(1, 256); THR(10); LDV(2, 512); THR(11); LDV(3, 768);
    THR(12); LDV(4, 2048); THR(13); LDV(5, 2304); THR(14); LDV(6, 2560); THR(15); LDV(7, 2816);
    if (WODELAY) lds_arrive(flags + 3, lane);
    STAMP(1);
    while (flags[6] < 4u) __builtin_amdgcn_s_sleep(1);
    asm volatile("" ::: "memory");
    STAMP(2);
    const unsigned tag = flags[1];
    const lds_p qa = L + L_Q + (n16 & 3) * 320 + (2 * g + ((n16 >> 2) & 1)) * 16;
    const v4u q0 = lds_ld128(qa), q1 = lds_ld128(qa + 128);
    asm volatile("s_waitcnt vmcnt(8)" : "+v"(K[0]), "+v"(K[1]), "+v"(K[2]), "+v"(K[3]), "+v"(K[4]), "+v"(K[5]), "+v"(K[6]), "+v"(K[7]));
    f32x4 sa[4];
#pragma unroll
    for (int o = 0; o < 4; ++o) {
      sa[o] = mfma(K[2 * o], q0, f32x4{0.f, 0.f, 0.f, 0.f});
      sa[o] = mfma(K[2 * o + 1], q1, sa[o]);
    }
    float sc[8];
    {
      const bool par = lane & 4;
      float raw[8];
#pragma unroll
      for (int p = 0; p < 2; ++p)
#pragma unroll
        for (int i = 0; i < 4; ++i) {
          auto sw = __builtin_amdgcn_permlane32_swap(asu(sa[p][i]), asu(sa[p + 2][i]), false, false);
          const float a = par ? asf(sw[1]) : asf(sw[0]);
          raw[4 * p + i] = a + dpp<0x124>(a);
        }
#pragma unroll
      for (int i = 0; i < 8; i += 2) {
        const unsigned u = pack_bf(raw[i], raw[i + 1]);
        const unsigned t = pack_bf(bf_lo(u) * 0.08838834764831845f, bf_hi(u) * 0.08838834764831845f);
        sc[i] = bf_lo(t); sc[i + 1] = bf_hi(t);
      }
    }
    float m = max3(max3(sc[0], sc[1], sc[2]), max3(sc[3], sc[4], sc[5]), max2(sc[6], sc[7]));
    m = x32_max(x16_max(m));
    float pe[8], ls;
    {
      const float mc = m * 1.4426950408889634f;
#pragma unroll
      for (int i = 0; i < 8; ++i) pe[i] = __builtin_amdgcn_exp2f(fmaf(sc[i], 1.4426950408889634f, -mc));
      ls = ((pe[0] + pe[1]) + (pe[2] + pe[3])) + ((pe[4] + pe[5]) + (pe[6] + pe[7]));
    }
    v4u PA[4];
    {
      const v4u P = {pack_bf(pe[0], pe[1]), pack_bf(pe[2], pe[3]), pack_bf(pe[4], pe[5]), pack_bf(pe[6], pe[7])};
#pragma unroll
      for (int mq = 0; mq < 4; ++mq) {
        const bool on = (n16 >> 2) == mq;
        PA[mq] = v4u{on ? P.x : 0u, on ? P.y : 0u, on ? P.z : 0u, on ? P.w : 0u};
      }
    }
    ls = x32_sum(x16_sum(ls));
    if (lane < 4) {
      ((LAS float*)(L + L_MAX))[w * 4 + lane] = m;
      ((LAS float*)(L + L_SUM))[w * 4 + lane] = ls;
    }
    lds_arrive(flags + 2, lane);
    STAMP(4);
    unsigned T[8][4];
#define XPOSE(n_)                                                                                         \
  _Pragma("unroll") for (int mm = 0; mm < 4; ++mm) {                                                      \
    T[2 * mm][n_] = __builtin_amdgcn_perm(V[2 * n_ + 1][mm], V[2 * n_][mm], 0x05040100u);                 \
    T[2 * mm + 1][n_] = __builtin_amdgcn_perm(V[2 * n_ + 1][mm], V[2 * n_][mm], 0x07060302u);             \
  }                                                                                                       \
  asm volatile("" : "+v"(T[0][n_]), "+v"(T[1][n_]), "+v"(T[2][n_]), "+v"(T[3][n_]), "+v"(T[4][n_]), "+v"(T[5][n_]), "+v"(T[6][n_]), "+v"(T[7][n_]));
    int ooff = WS_OP + ((b * HQ + 4 * j + (w >> 1)) * 4 + s) * 512 + (w & 1) * 256 + n16 * 16;
    const int dr = 64 * (w & 1) + 4 * n16 + g, dw = 8 * n16 + g;
    unsigned redr_a = lds0 + L_RED + ((w >> 1) * 128 + (dr ^ ((dr >> 5) << 1))) * 4;
    unsigned redw_a = lds0 + L_RED + w * 2048 + (dw ^ ((dw >> 5) << 1)) * 4;
    asm volatile("" : "+v"(PA[0]), "+v"(PA[1]), "+v"(PA[2]), "+v"(PA[3]), "+v"(ooff), "+v"(redr_a), "+v"(redw_a));
    asm volatile("s_waitcnt vmcnt(6)" : "+v"(V[0]), "+v"(V[1])); XPOSE(0)
    asm volatile("s_waitcnt vmcnt(4)" : "+v"(V[2]), "+v"(V[3])); XPOSE(1)
    asm volatile("s_waitcnt vmcnt(2)" : "+v"(V[4]), "+v"(V[5])); XPOSE(2)
    asm volatile("s_waitcnt vmcnt(0)" : "+v"(V[6]), "+v"(V[7]));
    STAMP(5);
    XPOSE(3)
    {
      f32x4 oa = {0.f, 0.f, 0.f, 0.f}, ob = {0.f, 0.f, 0.f, 0.f};
#pragma unroll
      for (int i = 0; i < 4; ++i) {
        oa = mfma(PA[i], v4u{T[i][0], T[i][1], T[i][2], T[i][3]}, oa);
        ob = mfma(PA[i], v4u{T[i + 4][0], T[i + 4][1], T[i + 4][2], T[i + 4][3]}, ob);
      }
      LAS float* red = (LAS float*)(size_t)redw_a;
#pragma unroll
      for (int hh = 0; hh < 4; ++hh) { red[hh * 128] = oa[hh]; *(LAS float*)(size_t)((redw_a ^ 16) + hh * 512) = ob[hh]; }
    }
    lds_arrive(flags + 4, lane);
    STAMP(6);
    const LAS float* redr = (const LAS float*)(size_t)redr_a;
    lds_wait(flags + 4, 9);
    {
      const f32x4 ma = *(const LAS f32x4*)(L + L_MSC + (w >> 1) * 32), mb = *(const LAS f32x4*)(L + L_MSC + (w >> 1) * 32 + 16);
      float o = redr[0] * ma[0], o2 = redr[512] * ma[1];
      o = fmaf(redr[2 * 512], ma[2], o); o2 = fmaf(redr[3 * 512], ma[3], o2);
      o = fmaf(redr[4 * 512], mb[0], o); o2 = fmaf(redr[5 * 512], mb[1], o2);
      o = fmaf(redr[6 * 512], mb[2], o); o2 = fmaf(redr[7 * 512], mb[3], o2);
      o += o2;
      auto s16 = __builtin_amdgcn_permlane16_swap(asu(o), asu(o), false, false);
      const unsigned pr = pack_bf(asf(s16[0]), asf(s16[1]));
      auto s32 = __builtin_amdgcn_permlane32_swap(pr, pr, false, false);
      if (lane < 16) __builtin_amdgcn_raw_buffer_store_b128(v4u{s32[0], tag, s32[1], tag}, rs, ooff, 0, AUX_SC1);
    }
    STAMP(7);

    if (is_wo) {
      const int h = 4 * j + (r >> 2), ns = r & 3;
      unsigned doff = WS_OP + (w * HQ + h) * 4 * 512 + lane * 8;
      int roff = WS_RP + ((32 * ns + 4 * w + g) * RP_C + h) * 128 + (lane & 7) * 16;
      unsigned osw = lds0 + L_OS + w * 288 + lane * 4, osr = lds0 + L_OS + (lane & 7) * 288 + g * 16;
      BAR();
      v4u wf[4];
      {
        const lds_p p = L + L_W + (8 * g + (n16 >> 2)) * 256 + (16 * w + 4 * (lane & 3)) * 2;
#pragma unroll
        for (int t = 0; t < 4; ++t) wf[t] = tr_frag(p + t * 32 * 256, 256);
      }
      unsigned loff = WS_LS + (w * HKV + j) * 4 * 128 + (h & 3) * 8;
      asm volatile("" : "+v"(doff), "+v"(loff), "+v"(roff), "+v"(osw), "+v"(osr), "+v"(wf[0]), "+v"(wf[1]), "+v"(wf[2]), "+v"(wf[3]));
      if (TDELAY) __builtin_amdgcn_s_sleep(TDELAY);
      v2u dv[8];
      poll8x2(ws, tag, doff, loff, dv);
      STAMP(8);
      {
        const float mx = max2(max3(asf(dv[4].x), asf(dv[5].x), asf(dv[6].x)), asf(dv[7].x));
        const float e0 = __builtin_amdgcn_exp2f(asf(dv[4].x) - mx), e1 = __builtin_amdgcn_exp2f(asf(dv[5].x) - mx);
        const float e2 = __builtin_amdgcn_exp2f(asf(dv[6].x) - mx), e3 = __builtin_amdgcn_exp2f(asf(dv[7].x) - mx);
        const float inv = __builtin_amdgcn_rcpf((e0 + e1) + (e2 + e3));
        const float o0 = (fmaf(e0, bf_lo(dv[0].x), e1 * bf_lo(dv[1].x)) + fmaf(e2, bf_lo(dv[2].x), e3 * bf_lo(dv[3].x))) * inv;
        const float o1 = (fmaf(e0, bf_hi(dv[0].x), e1 * bf_hi(dv[1].x)) + fmaf(e2, bf_hi(dv[2].x), e3 * bf_hi(dv[3].x))) * inv;
        *(LAS unsigned*)(size_t)osw = pack_bf(o0, o1);
      }
      BAR();
      {
        const lds_p pa = (lds_p)(size_t)osr;
        f32x4 acc0 = mfma(wf[0], lds_ld128(pa), f32x4{0.f, 0.f, 0.f, 0.f});
        f32x4 acc1 = mfma(wf[1], lds_ld128(pa + 64), f32x4{0.f, 0.f, 0.f, 0.f});
        acc0 = mfma(wf[2], lds_ld128(pa + 128), acc0);
        acc1 = mfma(wf[3], lds_ld128(pa + 192), acc1);
        acc0 += acc1;
        if ((lane & 8) == 0) __builtin_amdgcn_raw_buffer_store_b128(v4u{pack_bf(acc0[0], acc0[1]), tag, pack_bf(acc0[2], acc0[3]), tag}, rs, roff, 0, AUX_SC1);
      }
      STAMP(9);
      if (w == 0) {
        const int rho = 16 * j + r;
        const unsigned xv = *(const unsigned*)(x + (lane & 7) * D + 4 * rho + (g & 2));
        float xsel = (g & 1) ? bf_hi(xv) : bf_lo(xv);
        asm volatile("" : "+v"(xsel));
        if (RDELAY) __builtin_amdgcn_s_sleep(RDELAY);
        v4u v[8];
        pollNx4<4, 8 * 128>(ws, tag, WS_RP + (rho * RP_C + (lane >> 3)) * 128 + (lane & 7) * 16, v);
        STAMP(10);
        const float a0 = (bf_lo(v[0].x) + bf_lo(v[1].x)) + (bf_lo(v[2].x) + bf_lo(v[3].x)), a1 = (bf_hi(v[0].x) + bf_hi(v[1].x)) + (bf_hi(v[2].x) + bf_hi(v[3].x));
        const float a2 = (bf_lo(v[0].z) + bf_lo(v[1].z)) + (bf_lo(v[2].z) + bf_lo(v[3].z)), a3 = (bf_hi(v[0].z) + bf_hi(v[1].z)) + (bf_hi(v[2].z) + bf_hi(v[3].z));
        auto s01 = __builtin_amdgcn_permlane16_swap(asu(a0), asu(a1), false, false);
        auto s23 = __builtin_amdgcn_permlane16_swap(asu(a2), asu(a3), false, false);
        auto sc = __builtin_amdgcn_permlane32_swap(asu(asf(s01[0]) + asf(s01[1])), asu(asf(s23[0]) + asf(s23[1])), false, false);
        float e = asf(sc[0]) + asf(sc[1]);
        e += dpp<0x128>(e);
        const unsigned hb = pack_bf(xsel + rbf(e), 0.f) & 0xffffu;
        auto hp = __builtin_amdgcn_permlane16_swap(hb, hb, false, false);
        const unsigned hq = (unsigned)__builtin_amdgcn_ds_bpermute((((lane >> 1) & 7) + 32 * (lane & 1)) * 4, (int)(hp[0] | (hp[1] << 16)));
        if (lane < 16) __builtin_amdgcn_raw_buffer_store_b64(v2u{hq, tag}, rs, WS_GH + rho * 128 + lane * 8, 0, AUX_SC1);
        STAMP(11);
      } else if (w == 1) {
        u64 tf_ = 0;
        final_cell(ws, tag, fcell, lane, out, (unsigned*)(ws + WS_EPOCH), &tf_);
        STAMP(15);
      }
    } else if (is_mlp) {
      const int fg = 8 * j + ((r - 16) >> 1), nh = r & 1;
      const int kq = w >> 1, cth = w & 1;
      BAR();
      STAMP(8);
      v4u gf[4], uf[4], df[2];
      {
        const lds_p p = L + L_GT + (128 * kq + 8 * g + (n16 >> 2)) * 64 + (16 * cth + 4 * (lane & 3)) * 2;
#pragma unroll
        for (int t = 0; t < 4; ++t) { gf[t] = tr_frag(p + t * 32 * 64, 64); uf[t] = tr_frag(p + 32768 + t * 32 * 64, 64); }
        const lds_p pd = L + L_DN + (8 * g + (n16 >> 2)) * 512 + (32 * w + 4 * (lane & 3)) * 2;
#pragma unroll
        for (int t = 0; t < 2; ++t) df[t] = tr_frag(pd + t * 32, 512);
      }
      const bool up = lane & 8;
      unsigned r3w = lds0 + L_R3 + ((((kq * 2 + (up ? 1 : 0)) * 2 + cth) * 8 + (lane & 7)) * 16 + 4 * g) * 4;
      {
        const v4u ones = {0x3f803f80u, 0x3f803f80u, 0x3f803f80u, 0x3f803f80u};
        f32x4 cg = {0.f, 0.f, 0.f, 0.f}, cu = {0.f, 0.f, 0.f, 0.f};
#pragma unroll
        for (int t = 0; t < 4; ++t) { cg = mfma(gf[t], ones, cg); cu = mfma(uf[t], ones, cu); }
        *(LAS f32x4*)(size_t)r3w = f32x4{up ? cu[0] : cg[0], up ? cu[1] : cg[1], up ? cu[2] : cg[2], up ? cu[3] : cg[3]};
        BAR();
        if (w == 0) {
          const LAS float* r3 = (const LAS float*)(L + L_R3) + (lane >> 5) * 256 + ((lane >> 4) & 1) * 128 + (lane & 15);
          ((LAS float*)(L + L_CS))[lane] = (r3[0] + r3[512]) + (r3[1024] + r3[1536]);
        }
      }
      unsigned goff = WS_GH + 16 * w * 128 + lane * 16;
      unsigned hnw = lds0 + L_HN + (lane & 7) * 1056 + (16 * w + (lane >> 3)) * 8, hrow = lds0 + L_HN + w * 1056 + lane * 16, hnr = lds0 + L_HN + (lane & 7) * 1056 + (128 * kq + 8 * g) * 2;
      unsigned r3r = lds0 + L_R3 + (((lane >> 4) & 1) * 8 + 2 * (w & 3) + (lane >> 5)) * 64 + (lane & 15) * 4;
      unsigned hhw = lds0 + L_HH + (2 * (w & 3) + (lane >> 5)) * 96 + (lane & 31) * 2, hhr = lds0 + L_HH + (lane & 7) * 96 + g * 16;
      int foff = WS_FP + ((64 * nh + 8 * w + (up ? 4 : 0) + g) * FP_C + fg) * 128 + (lane & 7) * 16;
      asm volatile("" : "+v"(goff), "+v"(hnw), "+v"(hrow), "+v"(hnr), "+v"(r3w), "+v"(r3r), "+v"(hhw), "+v"(hhr), "+v"(foff));
      asm volatile("" : "+v"(gf[0]), "+v"(gf[1]), "+v"(gf[2]), "+v"(gf[3]), "+v"(uf[0]), "+v"(uf[1]), "+v"(uf[2]), "+v"(uf[3]));
      asm volatile("" : "+v"(df[0]), "+v"(df[1]));
      v4u h0, h1;
      if (GSENT) {
        v2u sv;
        poll1x2(ws, tag, WS_GH + (16 * w + (fg & 15)) * 128, sv, true);
      }
      poll2x4(ws, tag, goff, goff + 1024, h0, h1);
      STAMP(9);
      *(LAS v2u*)(size_t)hnw = v2u{h0.x, h0.z};
      *(LAS v2u*)(size_t)(hnw + 64) = v2u{h1.x, h1.z};
      BAR();
      {
        const lds_p pa = (lds_p)(size_t)hnr;
        f32x4 ag = {0.f, 0.f, 0.f, 0.f}, au = {0.f, 0.f, 0.f, 0.f};
#pragma unroll
        for (int t = 0; t < 4; ++t) {
          const v4u a = lds_ld128(pa + t * 64);
          ag = mfma(gf[t], a, ag);
          au = mfma(uf[t], a, au);
        }
        {
          const v4u hr = lds_ld128((lds_p)(size_t)hrow);
          const float f[8] = {bf_lo(hr.x), bf_hi(hr.x), bf_lo(hr.y), bf_hi(hr.y), bf_lo(hr.z), bf_hi(hr.z), bf_lo(hr.w), bf_hi(hr.w)};
          const float c0 = asf(__builtin_amdgcn_readfirstlane(asu(f[0])));
          float s1 = 0.f, s2 = 0.f;
#pragma unroll
          for (int i = 0; i < 8; ++i) { const float d = f[i] - c0; s1 += d; s2 = fmaf(d, d, s2); }
          s1 += dpp<0xB1>(s1); s2 += dpp<0xB1>(s2);
          s1 += dpp<0x4E>(s1); s2 += dpp<0x4E>(s2);
          s1 += dpp<0x141>(s1); s2 += dpp<0x141>(s2);
          s1 += dpp<0x140>(s1); s2 += dpp<0x140>(s2);
          auto sw = __builtin_amdgcn_permlane16_swap(asu(s1), asu(s2), false, false);
          const float t = x32_sum(asf(sw[0]) + asf(sw[1])) * (1.0f / D);
          const float m1 = asf(__builtin_amdgcn_readlane(asu(t), 0)), m2 = asf(__builtin_amdgcn_readlane(asu(t), 16));
          const float var = rbf(fmaxf(fmaf(-m1, m1, m2), 0.f));
          const float den = rbf(__builtin_amdgcn_sqrtf(rbf(var + 1e-5f)));
          if (lane == 0) *(LAS v2u*)(L + L_ST + w * 8) = v2u{asu(rbf(c0 + m1)), asu(__builtin_amdgcn_rcpf(den))};
        }
        *(LAS f32x4*)(size_t)r3w = f32x4{up ? au[0] : ag[0], up ? au[1] : ag[1], up ? au[2] : ag[2], up ? au[3] : ag[3]};
      }
      BAR();
      if (w < 4) {
        const int bb = 2 * w + (lane >> 5), n32 = lane & 31;
        const LAS float* r3 = (const LAS float*)(size_t)r3r;
        const v2u stt = *(const LAS v2u*)(L + L_ST + bb * 8);
        const float csg = ((const LAS float*)(L + L_CS))[n32], csu = ((const LAS float*)(L + L_CS))[32 + n32];
        float gg = (r3[0] + r3[512]) + (r3[1024] + r3[1536]);
        float uu = (r3[256] + r3[768]) + (r3[1280] + r3[1792]);
        gg = rbf(fmaf(-asf(stt.x), csg, gg) * asf(stt.y));
        uu = rbf(fmaf(-asf(stt.x), csu, uu) * asf(stt.y));
        const float sg = rbf(__builtin_amdgcn_rcpf(1.0f + __builtin_amdgcn_exp2f(-1.4426950408889634f * gg)));
        *(LAS unsigned short*)(size_t)hhw = (unsigned short)pack_bf(sg * uu, 0.f);
      }
      BAR();
      {
        const v4u a = lds_ld128((lds_p)(size_t)hhr);
        const f32x4 z = {0.f, 0.f, 0.f, 0.f};
        const f32x4 d0 = mfma(df[0], a, z), d1 = mfma(df[1], a, z);
        __builtin_amdgcn_raw_buffer_store_b128(v4u{pack_bf(up ? d1[0] : d0[0], up ? d1[1] : d0[1]), tag, pack_bf(up ? d1[2] : d0[2], up ? d1[3] : d0[3]), tag}, rs, foff, 0, AUX_SC1);
      }
      STAMP(10);
    }
  } else {
    const int hw = wv, kq = kq0;
    BAR();
    asm volatile("s_waitcnt vmcnt(6)" : "+v"(xa), "+v"(xb));
    STAMP(1);
    {
      float fa[8] = {bf_lo(xa.x), bf_hi(xa.x), bf_lo(xa.y), bf_hi(xa.y), bf_lo(xa.z), bf_hi(xa.z), bf_lo(xa.w), bf_hi(xa.w)};
      float fb[8] = {bf_lo(xb.x), bf_hi(xb.x), bf_lo(xb.y), bf_hi(xb.y), bf_lo(xb.z), bf_hi(xb.z), bf_lo(xb.w), bf_hi(xb.w)};
      layernorm8(fa);
      layernorm8(fb);
      if (g == kq) {
        *(LAS v4u*)(L + L_HN + (2 * hw) * 288 + n16 * 16) = v4u{pack_bf(fa[0], fa[1]), pack_bf(fa[2], fa[3]), pack_bf(fa[4], fa[5]), pack_bf(fa[6], fa[7])};
        *(LAS v4u*)(L + L_HN + (2 * hw + 1) * 288 + n16 * 16) = v4u{pack_bf(fb[0], fb[1]), pack_bf(fb[2], fb[3]), pack_bf(fb[4], fb[5]), pack_bf(fb[6], fb[7])};
      }
    }
    asm volatile("s_waitcnt vmcnt(0)" : "+v"(ep), "+v"(pf) :: "memory");
    STAMP(2);
    const unsigned tag = ep + 1;
    lds_arrive(flags + 0, lane);
    lds_wait(flags + 0, 4);
    {
      const lds_p pa = L + L_HN + (lane & 7) * 288 + g * 16;
      const lds_p pb = L + L_WQ + (8 * g + (n16 >> 2)) * 128 + (16 * hw + 4 * (lane & 3)) * 2;
      f32x4 acc0 = mfma(tr_frag(pb, 128), lds_ld128(pa), f32x4{0.f, 0.f, 0.f, 0.f});
      f32x4 acc1 = mfma(tr_frag(pb + 32 * 128, 128), lds_ld128(pa + 64), f32x4{0.f, 0.f, 0.f, 0.f});
      acc0 = mfma(tr_frag(pb + 64 * 128, 128), lds_ld128(pa + 128), acc0);
      acc1 = mfma(tr_frag(pb + 96 * 128, 128), lds_ld128(pa + 192), acc1);
      if (!ABL_NOQP) publish_tile(acc0 + acc1, rs, WS_QP + (kq * QP_K + 32 * j + 4 * ct8 + hw) * 128, QP_B * 128, tag, lane);
    }
    STAMP(3);
    while ((unsigned)(__builtin_amdgcn_s_memrealtime() - t_start) < (unsigned)QDELAY) __builtin_amdgcn_s_sleep(1);
    if (UNTHR) lds_arrive(flags + 7, lane);
    {
      const int off = WS_QP + (b * QP_B + 32 * j + 8 * hw) * 128 + lane * 16;
      v4u v[8];
      if (QBUF) {
        for (unsigned spins = 0;; ++spins) {
          asm volatile("" ::: "memory");
#pragma unroll
          for (int i = 0; i < 4; ++i) v[i] = __builtin_amdgcn_raw_buffer_load_b128(rs, off + i * QP_K * 128, 0, AUX_SC1);
          if ((bad4(v[0], tag) | bad4(v[1], tag) | bad4(v[2], tag) | bad4(v[3], tag)) == 0 || spins > SPIN_CAP) break;
        }
      } else pollNx4<4, QP_K * 128>(ws, tag, off, v);
      const float q0 = (asf(v[0].x) + asf(v[1].x)) + (asf(v[2].x) + asf(v[3].x)), q1 = (asf(v[0].z) + asf(v[1].z)) + (asf(v[2].z) + asf(v[3].z));
      *(LAS unsigned*)(L + L_Q + hw * 320 + lane * 4) = pack_bf(q0, q1);
      if (hw == 0 && lane == 0) flags[1] = tag;
    }
    lds_arrive(flags + 6, lane);
    STAMP(4);
    __builtin_amdgcn_s_setprio(0);
    if (hw == 0) {
      while (flags[2] < 8u) __builtin_amdgcn_s_sleep(1);
      asm volatile("" ::: "memory");
      const float mw = ((const LAS float*)(L + L_MAX))[lane & 31], lw = ((const LAS float*)(L + L_SUM))[lane & 31];
      float mx = max2(mw, dpp<0x124>(mw));
      mx = max2(mx, dpp<0x128>(mx));
      mx = x16_max(mx);
      const float f = __builtin_amdgcn_exp2f((mw - mx) * 1.4426950408889634f);
      float l = f * lw;
      l += dpp<0x124>(l);
      l += dpp<0x128>(l);
      l = x16_sum(l);
      if (lane < 32) ((LAS float*)(L + L_MSC))[(lane & 3) * 8 + (lane >> 2)] = f * __builtin_amdgcn_rcpf(l);
      lds_arrive(flags + 4, lane);
      if (lane < 16) {
        const float lse = mx * 1.4426950408889634f + __builtin_amdgcn_logf(l);
        __builtin_amdgcn_raw_buffer_store_b64(v2u{asu(lse), tag}, rs, WS_LS + ((b * HKV + j) * 4 + s) * 128 + lane * 8, 0, AUX_SC1);
      }
    }
    if (is_wo) {
      const int h = 4 * j + (r >> 2), ns = r & 3;
      while (flags[4] < DMAFLAG && !(WODELAY && flags[3] == 8u && (unsigned)(__builtin_amdgcn_s_memrealtime() - t_start) >= (unsigned)WODELAY)) __builtin_amdgcn_s_sleep(2);
      if (!ABL_NOW) {
        const unsigned char* src = (const unsigned char*)wo + ((size_t)(128 * h + 32 * hw + g) * D + 128 * ns) * 2 + n16 * 16;
#pragma unroll
        for (int i = 0; i < 8; ++i) glds16(src + (size_t)i * 4 * D * 2, lds0 + L_W + (32 * hw + 4 * i) * 256);
      }
      STAMP(5);
      asm volatile("s_waitcnt vmcnt(0)" ::: "memory");
      STAMP(6);
      BAR();
    } else if (is_mlp && ABL_NOW) { BAR(); } else if (is_mlp) {
      while (flags[4] < DMAFLAG) __builtin_amdgcn_s_sleep(2);
      const int fg = 8 * j + ((r - 16) >> 1), nh = r & 1;
      const size_t go = ((size_t)(128 * hw + (lane >> 2)) * FF + 32 * fg) * 2 + (lane & 3) * 16;
#pragma unroll
      for (int i = 0; i < 8; ++i) {
        if (i >= 2) asm volatile("s_waitcnt vmcnt(3)" ::: "memory");
        glds16((const unsigned char*)wg + go + (size_t)i * 16 * FF * 2, lds0 + L_GT + (128 * hw + 16 * i) * 64);
        if (i >= 2) asm volatile("s_waitcnt vmcnt(3)" ::: "memory");
        glds16((const unsigned char*)wu + go + (size_t)i * 16 * FF * 2, lds0 + L_UP + (128 * hw + 16 * i) * 64);
      }
      const unsigned char* sd = (const unsigned char*)wd + ((size_t)(32 * fg + 8 * hw + (lane >> 5)) * D + 256 * nh) * 2 + (lane & 31) * 16;
#pragma unroll
      for (int i = 0; i < 4; ++i) {
        asm volatile("s_waitcnt vmcnt(5)" ::: "memory");
        glds16(sd + (size_t)i * 2 * D * 2, lds0 + L_DN + (8 * hw + 2 * i) * 512);
      }
      STAMP(5);
      asm volatile("s_waitcnt vmcnt(0)" ::: "memory");
      STAMP(6);
      BAR();
    }
  }
}

}

extern "C" void kernel_launch(void* const* d_in, const int* in_sizes, int n_in, void* d_out, int out_size, void* d_ws,
                              size_t ws_size, hipStream_t stream) {
  (void)in_sizes; (void)n_in; (void)out_size;
  const size_t ws_base = ((WS_TOP) != 0 && ws_size >= (size_t)WS_TOP) ? ((ws_size - (size_t)WS_TOP) & ~(size_t)4095) : 0;
  (void)hipFuncSetAttribute((const void*)fused_decode, hipFuncAttributeMaxDynamicSharedMemorySize, LDS_BYTES);
  fused_decode<<<256, NTHR, LDS_BYTES, stream>>>(
      (const unsigned short*)d_in[0], (const unsigned short*)d_in[1], (const unsigned short*)d_in[2], (const unsigned short*)d_in[3],
      (const unsigned short*)d_in[4], (const unsigned short*)d_in[6], (const unsigned short*)d_in[7], (const unsigned short*)d_in[8],
      (unsigned short*)d_out, (unsigned char*)d_ws + ws_base);
}
```

```cpp
#include <hip/hip_runtime.h>
#include <hip/hip_bf16.h>
#include <stdint.h>

namespace {

typedef unsigned v4u __attribute__((ext_vector_type(4)));
typedef unsigned v2u __attribute__((ext_vector_type(2)));
typedef float f32x4 __attribute__((ext_vector_type(4)));
typedef __bf16 bf16x8 __attribute__((ext_vector_type(8)));
typedef unsigned long long u64;
#define LAS __attribute__((address_space(3)))
typedef LAS unsigned char* lds_p;

constexpr int D = 512, HQ = 32, HKV = 8, SK = 4096, DH = 128, WIN = 1024, FF = 1792;
constexpr int NTHR = 768;
constexpr int AUX_SC1 = 16;
constexpr unsigned SPIN_CAP = 1u << 16;
constexpr int PD = 3;
constexpr int THRN = 12;
constexpr unsigned DMAFLAG = 2;
constexpr unsigned QDELAY = 310;
constexpr int CODE_LINES = 116;

constexpr int WS_EPOCH = 0;
constexpr int PAD = 1;
constexpr int QP_B = 256 + PAD, QP_K = 8 * QP_B;
constexpr int RP_C = 32 + PAD;
constexpr int FP_C = 56 + PAD;
constexpr int WS_QP = 1 << 16;
constexpr int WS_OP = WS_QP + 4 * QP_K * 128;
constexpr int WS_LS = WS_OP + (1 << 19);
constexpr int WS_RP = WS_LS + (1 << 15);
constexpr int WS_GH = WS_RP + 128 * RP_C * 128;
constexpr int WS_FP = WS_GH + (1 << 14);
constexpr int WS_END = WS_FP + 128 * FP_C * 128;

constexpr int L_RED = 0;
constexpr int L_WQ = 0;
constexpr int L_Q = 16384;
constexpr int L_MAX = L_Q + 4 * 320;
constexpr int L_SUM = L_MAX + 128;
constexpr int L_FLAG = L_SUM + 128;
constexpr int L_MSC = L_FLAG + 64;
constexpr int L_CS = L_MSC + 128;
constexpr int L_ST = L_CS + 256;
constexpr int L_OS = L_ST + 64;
constexpr int L_HN = L_OS + 8 * 288;
constexpr int L_R3 = L_HN + 8 * 1056;
constexpr int L_HH = L_R3 + 8192;
constexpr int L_W = ((L_HH + 8 * 96 + 255) / 256) * 256;
constexpr int L_GT = L_W, L_UP = L_W + 32768, L_DN = L_W + 65536;
constexpr int LDS_BYTES = L_W + 98304;
static_assert(LDS_BYTES <= 160 * 1024 && LDS_BYTES > 80 * 1024, "one workgroup per CU");

__device__ __forceinline__ float bf_lo(unsigned u) { return __builtin_bit_cast(float, u << 16); }
__device__ __forceinline__ float bf_hi(unsigned u) { return __builtin_bit_cast(float, u & 0xffff0000u); }
__device__ __forceinline__ unsigned pack_bf(float lo, float hi) {
  typedef __bf16 bf2 __attribute__((ext_vector_type(2)));
  bf2 r = {(__bf16)lo, (__bf16)hi};
  return __builtin_bit_cast(unsigned, r);
}
__device__ __forceinline__ float rbf(float f) { return bf_lo(pack_bf(f, f)); }
__device__ __forceinline__ float asf(unsigned u) { return __builtin_bit_cast(float, u); }
__device__ __forceinline__ unsigned asu(float f) { return __builtin_bit_cast(unsigned, f); }
template <int CTRL> __device__ __forceinline__ float dpp(float x) {
  return __builtin_bit_cast(float, __builtin_amdgcn_mov_dpp(__builtin_bit_cast(int, x), CTRL, 0xf, 0xf, true));
}
__device__ __forceinline__ float max3(float a, float b, float c) {
  float r;
  asm("v_max3_f32 %0, %1, %2, %3" : "=v"(r) : "v"(a), "v"(b), "v"(c));
  return r;
}
__device__ __forceinline__ float max2(float a, float b) {
  float r;
  asm("v_max_f32 %0, %1, %2" : "=v"(r) : "v"(a), "v"(b));
  return r;
}
__device__ __forceinline__ float x16_sum(float x) {
  auto s = __builtin_amdgcn_permlane16_swap(asu(x), asu(x), false, false);
  return asf(s[0]) + asf(s[1]);
}
__device__ __forceinline__ float x32_sum(float x) {
  auto s = __builtin_amdgcn_permlane32_swap(asu(x), asu(x), false, false);
  return asf(s[0]) + asf(s[1]);
}
__device__ __forceinline__ float x16_max(float x) {
  auto s = __builtin_amdgcn_permlane16_swap(asu(x), asu(x), false, false);
  return max2(asf(s[0]), asf(s[1]));
}
__device__ __forceinline__ float x32_max(float x) {
  auto s = __builtin_amdgcn_permlane32_swap(asu(x), asu(x), false, false);
  return max2(asf(s[0]), asf(s[1]));
}
__device__ __forceinline__ f32x4 mfma(v4u a, v4u b, f32x4 c) {
  return __builtin_amdgcn_mfma_f32_16x16x32_bf16(__builtin_bit_cast(bf16x8, a), __builtin_bit_cast(bf16x8, b), c, 0, 0, 0);
}
__device__ __forceinline__ v2u tr_read(lds_p p) {
  typedef short v4s __attribute__((ext_vector_type(4)));
  return __builtin_bit_cast(v2u, __builtin_amdgcn_ds_read_tr16_b64_v4i16((LAS v4s*)p));
}
__device__ __forceinline__ v4u tr_frag(lds_p p, int row_stride) {
  const v2u a = tr_read(p), b = tr_read(p + 4 * row_stride);
  return v4u{a.x, a.y, b.x, b.y};
}
__device__ __forceinline__ v4u lds_ld128(lds_p p) { return *(const LAS v4u*)p; }
__device__ __forceinline__ void glds16(const void* gsrc, unsigned lds_dst) {
  unsigned keep;
  asm volatile("s_mov_b32 %0, m0\n\ts_mov_b32 m0, %2\n\ts_nop 0\n\tglobal_load_lds_dwordx4 %1, off nt\n\ts_mov_b32 m0, %0"
               : "=&s"(keep) : "v"(gsrc), "s"(lds_dst) : "memory");
}
#define BAR() do { asm volatile("s_waitcnt lgkmcnt(0)" ::: "memory"); __builtin_amdgcn_s_barrier(); asm volatile("" ::: "memory"); } while (0)
__device__ __forceinline__ void lds_arrive(volatile LAS unsigned* p, int lane) {
  asm volatile("" ::: "memory");
  if (lane == 0) asm volatile("ds_add_u32 %0, %1" :: "v"((unsigned)(size_t)p), "v"(1u) : "memory");
  asm volatile("" ::: "memory");
}
__device__ __forceinline__ void lds_wait(volatile LAS unsigned* p, unsigned n) {
  while (*p < n) { }
  asm volatile("" ::: "memory");
}
typedef unsigned long long mask_t;
__device__ __forceinline__ mask_t bad4(v4u v, unsigned tag) { return __builtin_amdgcn_ballot_w64(v.y != tag) | __builtin_amdgcn_ballot_w64(v.w != tag); }
__device__ __forceinline__ mask_t bad2(v2u v, unsigned tag) { return __builtin_amdgcn_ballot_w64(v.y != tag); }
#define PL4(n_, o_, m_) "s_mov_b64 exec, %" #m_ "\n\tglobal_load_dwordx4 %" #n_ ", %" #o_ ", %[base] sc1\n\t"
#define PL2(n_, o_, m_) "s_mov_b64 exec, %" #m_ "\n\tglobal_load_dwordx2 %" #n_ ", %" #o_ ", %[base] sc1\n\t"
#define PLEND "s_mov_b64 exec, -1\n\ts_waitcnt vmcnt(0)"
__device__ __forceinline__ void poll1x4(const unsigned char* ws, unsigned tag, unsigned o0, v4u& a) {
  a = v4u{0u, ~tag, 0u, ~tag};
  for (unsigned spins = 0; spins <= SPIN_CAP; ++spins) {
    const mask_t m0 = bad4(a, tag);
    if (m0 == 0) break;
    asm volatile(PL4(0, 1, 2) PLEND : "+v"(a) : "v"(o0), "s"(m0), [base] "s"(ws) : "memory");
    __builtin_amdgcn_s_sleep(2);
  }
}
__device__ __forceinline__ void poll2x4(const unsigned char* ws, unsigned tag, unsigned o0, unsigned o1, v4u& a, v4u& b) {
  a = b = v4u{0u, ~tag, 0u, ~tag};
  for (unsigned spins = 0; spins <= SPIN_CAP; ++spins) {
    const mask_t m0 = bad4(a, tag), m1 = bad4(b, tag);
    if ((m0 | m1) == 0) break;
    asm volatile(PL4(0, 2, 4) PL4(1, 3, 5) PLEND : "+v"(a), "+v"(b) : "v"(o0), "v"(o1), "s"(m0), "s"(m1), [base] "s"(ws) : "memory");
  }
}
__device__ __forceinline__ void poll5x2(const unsigned char* ws, unsigned tag, unsigned oa, unsigned ob, v2u (&v)[5]) {
  unsigned o[4];
#pragma unroll
  for (int i = 0; i < 4; ++i) o[i] = oa + i * 512;
#pragma unroll
  for (int i = 0; i < 5; ++i) v[i] = v2u{0u, ~tag};
  for (unsigned spins = 0; spins <= SPIN_CAP; ++spins) {
    mask_t m[5], any = 0;
#pragma unroll
    for (int i = 0; i < 5; ++i) { m[i] = bad2(v[i], tag); any |= m[i]; }
    if (any == 0) break;
    asm volatile(PL2(0, 5, 10) PL2(1, 6, 11) PL2(2, 7, 12) PL2(3, 8, 13) PL2(4, 9, 14) PLEND
                 : "+v"(v[0]), "+v"(v[1]), "+v"(v[2]), "+v"(v[3]), "+v"(v[4])
                 : "v"(o[0]), "v"(o[1]), "v"(o[2]), "v"(o[3]), "v"(ob),
                   "s"(m[0]), "s"(m[1]), "s"(m[2]), "s"(m[3]), "s"(m[4]), [base] "s"(ws) : "memory");
  }
}
template <int N, int STEP>
__device__ __forceinline__ void pollNx4(const unsigned char* ws, unsigned tag, unsigned o0, v4u (&v)[N]) {
  static_assert(N == 4 || N == 7, "");
  unsigned o[N];
#pragma unroll
  for (int i = 0; i < N; ++i) { v[i] = v4u{0u, ~tag, 0u, ~tag}; o[i] = o0 + i * STEP; }
  for (unsigned spins = 0; spins <= SPIN_CAP; ++spins) {
    mask_t m[N], any = 0;
#pragma unroll
    for (int i = 0; i < N; ++i) { m[i] = bad4(v[i], tag); any |= m[i]; }
    if (any == 0) break;
    if constexpr (N == 4) {
      asm volatile(PL4(0, 4, 8) PL4(1, 5, 9) PL4(2, 6, 10) PL4(3, 7, 11) PLEND
                   : "+v"(v[0]), "+v"(v[1]), "+v"(v[2]), "+v"(v[3])
                   : "v"(o[0]), "v"(o[1]), "v"(o[2]), "v"(o[3]), "s"(m[0]), "s"(m[1]), "s"(m[2]), "s"(m[3]), [base] "s"(ws) : "memory");
    } else {
      asm volatile(PL4(0, 7, 14) PL4(1, 8, 15) PL4(2, 9, 16) PL4(3, 10, 17) PL4(4, 11, 18) PL4(5, 12, 19) PL4(6, 13, 20) PLEND
                   : "+v"(v[0]), "+v"(v[1]), "+v"(v[2]), "+v"(v[3]), "+v"(v[4]), "+v"(v[5]), "+v"(v[6])
                   : "v"(o[0]), "v"(o[1]), "v"(o[2]), "v"(o[3]), "v"(o[4]), "v"(o[5]), "v"(o[6]),
                     "s"(m[0]), "s"(m[1]), "s"(m[2]), "s"(m[3]), "s"(m[4]), "s"(m[5]), "s"(m[6]), [base] "s"(ws) : "memory");
    }
  }
}

__device__ __forceinline__ void layernorm8(float (&f)[8]) {
  const float c0 = asf(__builtin_amdgcn_readfirstlane(asu(f[0])));
  float s1 = 0.f, s2 = 0.f;
#pragma unroll
  for (int i = 0; i < 8; ++i) { const float d = f[i] - c0; s1 += d; s2 = fmaf(d, d, s2); }
  s1 += dpp<0xB1>(s1); s2 += dpp<0xB1>(s2);
  s1 += dpp<0x4E>(s1); s2 += dpp<0x4E>(s2);
  s1 += dpp<0x141>(s1); s2 += dpp<0x141>(s2);
  s1 += dpp<0x140>(s1); s2 += dpp<0x140>(s2);
  s1 = x16_sum(s1); s2 = x16_sum(s2);
  s1 = x32_sum(s1) * (1.0f / D); s2 = x32_sum(s2) * (1.0f / D);
  const float var = rbf(fmaxf(fmaf(-s1, s1, s2), 0.f));
  const float den = rbf(__builtin_amdgcn_sqrtf(rbf(var + 1e-5f)));
  const float inv = __builtin_amdgcn_rcpf(den), mb = rbf(c0 + s1);
#pragma unroll
  for (int i = 0; i < 8; ++i) f[i] = rbf(f[i] - mb) * inv;
}

__device__ __forceinline__ void publish_tile(f32x4 d, __amdgpu_buffer_rsrc_t rs, int base, int stride_b, unsigned tag, int lane) {
  const bool t = lane & 8;
  const int src = ((lane >> 3) + 8 * (lane & 1) + 16 * ((lane >> 1) & 3)) * 4;
  const unsigned v0 = (unsigned)__builtin_amdgcn_ds_bpermute(src, (int)asu(t ? d[2] : d[0]));
  const unsigned v1 = (unsigned)__builtin_amdgcn_ds_bpermute(src, (int)asu(t ? d[3] : d[1]));
  __builtin_amdgcn_raw_buffer_store_b128(v4u{v0, tag, v1, tag}, rs, base + (lane >> 3) * stride_b + (lane & 7) * 16, 0, AUX_SC1);
}

__device__ __forceinline__ void final_cell(const unsigned char* ws, unsigned tag, int cell, int lane, unsigned short* out, unsigned* ws_epoch) {
  const int p = lane & 7, row = lane >> 4;
  v4u hv;
  poll1x4(ws, tag, WS_GH + cell * 128 + p * 16, hv);
  float hsel = row == 0 ? bf_lo(hv.x) : row == 1 ? bf_hi(hv.x) : row == 2 ? bf_lo(hv.z) : bf_hi(hv.z);
  unsigned short* po = out + p * D + 4 * cell + row;
  asm volatile("" : "+v"(hsel), "+v"(po));
  v4u v[7];
  pollNx4<7, 8 * 128>(ws, tag, WS_FP + (cell * FP_C + (lane >> 3)) * 128 + p * 16, v);
  float a0 = 0.f, a1 = 0.f, a2 = 0.f, a3 = 0.f;
#pragma unroll
  for (int i = 0; i < 7; ++i) { a0 += bf_lo(v[i].x); a1 += bf_hi(v[i].x); a2 += bf_lo(v[i].z); a3 += bf_hi(v[i].z); }
  auto s01 = __builtin_amdgcn_permlane16_swap(asu(a0), asu(a1), false, false);
  auto s23 = __builtin_amdgcn_permlane16_swap(asu(a2), asu(a3), false, false);
  const float c0 = asf(s01[0]) + asf(s01[1]), c1 = asf(s23[0]) + asf(s23[1]);
  auto sc = __builtin_amdgcn_permlane32_swap(asu(c0), asu(c1), false, false);
  float e = asf(sc[0]) + asf(sc[1]);
  e += dpp<0x128>(e);
  if ((lane & 8) == 0) *po = (unsigned short)pack_bf(hsel + rbf(e), 0.f);
  if (cell == 0 && lane == 0) __hip_atomic_store(ws_epoch, tag, __ATOMIC_RELAXED, __HIP_MEMORY_SCOPE_AGENT);
}

__global__ void __launch_bounds__(NTHR, 3)
fused_decode(const unsigned short* __restrict__ x, const unsigned short* __restrict__ kc, const unsigned short* __restrict__ vc,
             const unsigned short* __restrict__ wq, const unsigned short* __restrict__ wo, const unsigned short* __restrict__ wg,
             const unsigned short* __restrict__ wu, const unsigned short* __restrict__ wd, unsigned short* __restrict__ out,
             unsigned char* __restrict__ ws) {
  unsigned long long code_base;
  asm volatile("s_getpc_b64 %0" : "=s"(code_base));
  code_base &= ~0xffull;
  extern __shared__ __attribute__((aligned(16))) unsigned char smem[];
  const lds_p L = (lds_p)smem;
  const unsigned lds0 = (unsigned)(size_t)L;
  const int tid = threadIdx.x, lane = tid & 63;
  const int wv = __builtin_amdgcn_readfirstlane(tid >> 6);
  const int w = wv - 4;
  const int c = blockIdx.x, j = c & 7, r = c >> 3;
  const int b = r >> 2, s = r & 3;
  const int g = lane >> 4, n16 = lane & 15;
  const bool is_wo = r < 16, is_mlp = (r >= 16) && (j < 7);
  const __amdgpu_buffer_rsrc_t rs = __builtin_amdgcn_make_buffer_rsrc((void*)ws, 0, WS_END, 0x00020000);
  volatile LAS unsigned* flags = (volatile LAS unsigned*)(L + L_FLAG);

  const u64 t_start = __builtin_amdgcn_s_memrealtime();
  const int kq0 = r >> 3, ct8 = r & 7;
  v4u xa, xb; unsigned ep, pf;
  asm volatile("" : "=v"(xa), "=v"(xb), "=v"(ep), "=v"(pf));
  if (wv < 4) {
    const int hw = wv;
    __builtin_amdgcn_s_setprio(2);
    const unsigned zero = 0;
    const unsigned xo = (2 * hw) * (D * 2) + lane * 16;
    asm volatile("global_load_dwordx4 %0, %1, %2" : "+v"(xa) : "v"(xo), "s"(x));
    asm volatile("global_load_dwordx4 %0, %1, %2 offset:1024" : "+v"(xb) : "v"(xo), "s"(x));
    const unsigned char* src = (const unsigned char*)wq + ((size_t)(128 * kq0 + 32 * hw + (lane >> 3)) * (HQ * DH) + 512 * j + 64 * ct8) * 2 + (lane & 7) * 16;
#pragma unroll
    for (int i = 0; i < 4; ++i) glds16(src + (size_t)i * 8 * (HQ * DH) * 2, lds0 + L_WQ + (32 * hw + 8 * i) * 128);
    asm volatile("global_load_dword %0, %1, %2" : "+v"(ep) : "v"(zero), "s"(ws));
    const unsigned po = min((r * 4 + hw) * 2 + (lane & 1), CODE_LINES - 1) * 128;
    asm volatile("global_load_dword %0, %1, %2" : "+v"(pf) : "v"(po), "s"(code_base));
  }
  int wv2 = wv;
  asm volatile("" : "+s"(wv2));

  if (__builtin_expect(wv2 >= 4, 1)) {
    const size_t tile = ((size_t)(b * HKV + j) * SK + (SK - WIN) + 256 * s + 32 * w) * (DH * 2);
    const unsigned char* kb = (const unsigned char*)kc + tile + 4096;
    const unsigned char* vb = (const unsigned char*)vc + tile;
    const unsigned koff = (lane & 7) * 256 + (2 * g + ((lane >> 3) & 1)) * 16;
    const unsigned voff = (16 * (g >> 1) + 4 * (g & 1)) * 256 + n16 * 16;
    v4u K[8], V[8];
#define LDK(i, imm) asm volatile("global_load_dwordx4 %0, %1, %2 offset:" #imm " nt" : "=v"(K[i]) : "v"(koff), "s"(kb))
#define LDV(i, imm) asm volatile("global_load_dwordx4 %0, %1, %2 offset:" #imm " nt" : "=v"(V[i]) : "v"(voff), "s"(vb))
#define THR(n) do { if (n >= PD && n < THRN) asm volatile("s_waitcnt vmcnt(%0)" :: "i"(PD - 1)); } while (0)
    static_assert(PD == 3, "three loads go out in front of the barrier");
    LDK(0, -4096); LDK(1, -3968); LDK(2, -2048);
    if (w == 0 && lane < 8) flags[lane] = 0;
    BAR();
    THR(3); LDK(3, -1920);
    THR(4); LDK(4, 0); THR(5); LDK(5, 128); THR(6); LDK(6, 2048); THR(7); LDK(7, 2176);
    THR(8); LDV(0, 0); THR(9); LDV(1, 256); THR(10); LDV(2, 512); THR(11); LDV(3, 768);
    THR(12); LDV(4, 2048); THR(13); LDV(5, 2304); THR(14); LDV(6, 2560); THR(15); LDV(7, 2816);
    while (flags[6] < 4u) __builtin_amdgcn_s_sleep(1);
    asm volatile("" ::: "memory");
    const unsigned tag = flags[1];
    const lds_p qa = L + L_Q + (n16 & 3) * 320 + (2 * g + ((n16 >> 2) & 1)) * 16;
    const v4u q0 = lds_ld128(qa), q1 = lds_ld128(qa + 128);
    asm volatile("s_waitcnt vmcnt(8)" : "+v"(K[0]), "+v"(K[1]), "+v"(K[2]), "+v"(K[3]), "+v"(K[4]), "+v"(K[5]), "+v"(K[6]), "+v"(K[7]));
    f32x4 sa[4];
#pragma unroll
    for (int o = 0; o < 4; ++o) {
      sa[o] = mfma(K[2 * o], q0, f32x4{0.f, 0.f, 0.f, 0.f});
      sa[o] = mfma(K[2 * o + 1], q1, sa[o]);
    }
    float sc[8];
    {
      const bool par = lane & 4;
      float raw[8];
#pragma unroll
      for (int p = 0; p < 2; ++p)
#pragma unroll
        for (int i = 0; i < 4; ++i) {
          auto sw = __builtin_amdgcn_permlane32_swap(asu(sa[p][i]), asu(sa[p + 2][i]), false, false);
          const float a = par ? asf(sw[1]) : asf(sw[0]);
          raw[4 * p + i] = a + dpp<0x124>(a);
        }
#pragma unroll
      for (int i = 0; i < 8; i += 2) {
        const unsigned u = pack_bf(raw[i], raw[i + 1]);
        const unsigned t = pack_bf(bf_lo(u) * 0.08838834764831845f, bf_hi(u) * 0.08838834764831845f);
        sc[i] = bf_lo(t); sc[i + 1] = bf_hi(t);
      }
    }
    float m = max3(max3(sc[0], sc[1], sc[2]), max3(sc[3], sc[4], sc[5]), max2(sc[6], sc[7]));
    m = x32_max(x16_max(m));
    float pe[8], ls;
    {
      const float mc = m * 1.4426950408889634f;
#pragma unroll
      for (int i = 0; i < 8; ++i) pe[i] = __builtin_amdgcn_exp2f(fmaf(sc[i], 1.4426950408889634f, -mc));
      ls = ((pe[0] + pe[1]) + (pe[2] + pe[3])) + ((pe[4] + pe[5]) + (pe[6] + pe[7]));
    }
    v4u PA[4];
    {
      const v4u P = {pack_bf(pe[0], pe[1]), pack_bf(pe[2], pe[3]), pack_bf(pe[4], pe[5]), pack_bf(pe[6], pe[7])};
#pragma unroll
      for (int mq = 0; mq < 4; ++mq) {
        const bool on = (n16 >> 2) == mq;
        PA[mq] = v4u{on ? P.x : 0u, on ? P.y : 0u, on ? P.z : 0u, on ? P.w : 0u};
      }
    }
    ls = x32_sum(x16_sum(ls));
    if (lane < 4) {
      ((LAS float*)(L + L_MAX))[w * 4 + lane] = m;
      ((LAS float*)(L + L_SUM))[w * 4 + lane] = ls;
    }
    lds_arrive(flags + 2, lane);
    unsigned T[8][4];
#define XPOSE(n_)                                                                                         \
  _Pragma("unroll") for (int mm = 0; mm < 4; ++mm) {                                                      \
    T[2 * mm][n_] = __builtin_amdgcn_perm(V[2 * n_ + 1][mm], V[2 * n_][mm], 0x05040100u);                 \
    T[2 * mm + 1][n_] = __builtin_amdgcn_perm(V[2 * n_ + 1][mm], V[2 * n_][mm], 0x07060302u);             \
  }                                                                                                       \
  asm volatile("" : "+v"(T[0][n_]), "+v"(T[1][n_]), "+v"(T[2][n_]), "+v"(T[3][n_]), "+v"(T[4][n_]), "+v"(T[5][n_]), "+v"(T[6][n_]), "+v"(T[7][n_]));
    int ooff = WS_OP + ((b * HQ + 4 * j + (w >> 1)) * 4 + s) * 512 + (w & 1) * 256 + n16 * 16;
    const int dr = 64 * (w & 1) + 4 * n16 + g, dw = 8 * n16 + g;
    unsigned redr_a = lds0 + L_RED + ((w >> 1) * 128 + (dr ^ ((dr >> 5) << 1))) * 4;
    unsigned redw_a = lds0 + L_RED + w * 2048 + (dw ^ ((dw >> 5) << 1)) * 4;
    const int h = 4 * j + (r >> 2), ns = r & 3;
    unsigned doff = WS_OP + (w * HQ + h) * 4 * 512 + lane * 8;
    unsigned loff = WS_LS + (w * HKV + j) * 128 + ((h & 3) * 4 + (lane & 3)) * 8;
    int roff = WS_RP + ((32 * ns + 4 * w + g) * RP_C + h) * 128 + (lane & 7) * 16;
    unsigned osw = lds0 + L_OS + w * 288 + lane * 4, osr = lds0 + L_OS + (lane & 7) * 288 + g * 16;
    unsigned wfa = lds0 + L_W + (8 * g + (n16 >> 2)) * 256 + (16 * w + 4 * (lane & 3)) * 2;
    asm volatile("" : "+v"(PA[0]), "+v"(PA[1]), "+v"(PA[2]), "+v"(PA[3]), "+v"(ooff), "+v"(redr_a), "+v"(redw_a));
    asm volatile("" : "+v"(doff), "+v"(loff), "+v"(roff), "+v"(osw), "+v"(osr), "+v"(wfa));
    asm volatile("s_waitcnt vmcnt(6)" : "+v"(V[0]), "+v"(V[1])); XPOSE(0)
    asm volatile("s_waitcnt vmcnt(4)" : "+v"(V[2]), "+v"(V[3])); XPOSE(1)
    asm volatile("s_waitcnt vmcnt(2)" : "+v"(V[4]), "+v"(V[5])); XPOSE(2)
    asm volatile("s_waitcnt vmcnt(0)" : "+v"(V[6]), "+v"(V[7]));
    XPOSE(3)
    {
      f32x4 oa = {0.f, 0.f, 0.f, 0.f}, ob = {0.f, 0.f, 0.f, 0.f};
#pragma unroll
      for (int i = 0; i < 4; ++i) {
        oa = mfma(PA[i], v4u{T[i][0], T[i][1], T[i][2], T[i][3]}, oa);
        ob = mfma(PA[i], v4u{T[i + 4][0], T[i + 4][1], T[i + 4][2], T[i + 4][3]}, ob);
      }
      LAS float* red = (LAS float*)(size_t)redw_a;
#pragma unroll
      for (int hh = 0; hh < 4; ++hh) { red[hh * 128] = oa[hh]; *(LAS float*)(size_t)((redw_a ^ 16) + hh * 512) = ob[hh]; }
    }
    lds_arrive(flags + 4, lane);
    const LAS float* redr = (const LAS float*)(size_t)redr_a;
    lds_wait(flags + 4, 9);
    {
      const f32x4 ma = *(const LAS f32x4*)(L + L_MSC + (w >> 1) * 32), mb = *(const LAS f32x4*)(L + L_MSC + (w >> 1) * 32 + 16);
      float o = redr[0] * ma[0], o2 = redr[512] * ma[1];
      o = fmaf(redr[2 * 512], ma[2], o); o2 = fmaf(redr[3 * 512], ma[3], o2);
      o = fmaf(redr[4 * 512], mb[0], o); o2 = fmaf(redr[5 * 512], mb[1], o2);
      o = fmaf(redr[6 * 512], mb[2], o); o2 = fmaf(redr[7 * 512], mb[3], o2);
      o += o2;
      auto s16 = __builtin_amdgcn_permlane16_swap(asu(o), asu(o), false, false);
      const unsigned pr = pack_bf(asf(s16[0]), asf(s16[1]));
      auto s32 = __builtin_amdgcn_permlane32_swap(pr, pr, false, false);
      if (lane < 16) __builtin_amdgcn_raw_buffer_store_b128(v4u{s32[0], tag, s32[1], tag}, rs, ooff, 0, AUX_SC1);
    }

    if (is_wo) {
      BAR();
      v4u wf[4];
      {
        const lds_p p = (lds_p)(size_t)wfa;
#pragma unroll
        for (int t = 0; t < 4; ++t) wf[t] = tr_frag(p + t * 32 * 256, 256);
      }
      v2u dv[5];
      poll5x2(ws, tag, doff, loff, dv);
      {
        const float lse = asf(dv[4].x);
        float mx = max2(lse, dpp<0xB1>(lse));
        mx = max2(mx, dpp<0x4E>(mx));
        const float e = __builtin_amdgcn_exp2f(lse - mx);
        float es = e + dpp<0xB1>(e);
        es += dpp<0x4E>(es);
        const float wt = e * __builtin_amdgcn_rcpf(es);
        const float e0 = dpp<0x00>(wt), e1 = dpp<0x55>(wt), e2 = dpp<0xAA>(wt), e3 = dpp<0xFF>(wt);
        const float o0 = fmaf(e0, bf_lo(dv[0].x), e1 * bf_lo(dv[1].x)) + fmaf(e2, bf_lo(dv[2].x), e3 * bf_lo(dv[3].x));
        const float o1 = fmaf(e0, bf_hi(dv[0].x), e1 * bf_hi(dv[1].x)) + fmaf(e2, bf_hi(dv[2].x), e3 * bf_hi(dv[3].x));
        *(LAS unsigned*)(size_t)osw = pack_bf(o0, o1);
      }
      BAR();
      {
        const lds_p pa = (lds_p)(size_t)osr;
        f32x4 acc0 = mfma(wf[0], lds_ld128(pa), f32x4{0.f, 0.f, 0.f, 0.f});
        f32x4 acc1 = mfma(wf[1], lds_ld128(pa + 64), f32x4{0.f, 0.f, 0.f, 0.f});
        acc0 = mfma(wf[2], lds_ld128(pa + 128), acc0);
        acc1 = mfma(wf[3], lds_ld128(pa + 192), acc1);
        acc0 += acc1;
        if ((lane & 8) == 0) __builtin_amdgcn_raw_buffer_store_b128(v4u{pack_bf(acc0[0], acc0[1]), tag, pack_bf(acc0[2], acc0[3]), tag}, rs, roff, 0, AUX_SC1);
      }
      if (w == 0) {
        const int rho = 16 * j + r;
        const unsigned xv = *(const unsigned*)(x + (lane & 7) * D + 4 * rho + (g & 2));
        float xsel = (g & 1) ? bf_hi(xv) : bf_lo(xv);
        asm volatile("" : "+v"(xsel));
        v4u v[4];
        pollNx4<4, 8 * 128>(ws, tag, WS_RP + (rho * RP_C + (lane >> 3)) * 128 + (lane & 7) * 16, v);
        const float a0 = (bf_lo(v[0].x) + bf_lo(v[1].x)) + (bf_lo(v[2].x) + bf_lo(v[3].x)), a1 = (bf_hi(v[0].x) + bf_hi(v[1].x)) + (bf_hi(v[2].x) + bf_hi(v[3].x));
        const float a2 = (bf_lo(v[0].z) + bf_lo(v[1].z)) + (bf_lo(v[2].z) + bf_lo(v[3].z)), a3 = (bf_hi(v[0].z) + bf_hi(v[1].z)) + (bf_hi(v[2].z) + bf_hi(v[3].z));
        auto s01 = __builtin_amdgcn_permlane16_swap(asu(a0), asu(a1), false, false);
        auto s23 = __builtin_amdgcn_permlane16_swap(asu(a2), asu(a3), false, false);
        auto sc = __builtin_amdgcn_permlane32_swap(asu(asf(s01[0]) + asf(s01[1])), asu(asf(s23[0]) + asf(s23[1])), false, false);
        float e = asf(sc[0]) + asf(sc[1]);
        e += dpp<0x128>(e);
        const unsigned hb = pack_bf(xsel + rbf(e), 0.f) & 0xffffu;
        auto hp = __builtin_amdgcn_permlane16_swap(hb, hb, false, false);
        const unsigned hq = (unsigned)__builtin_amdgcn_ds_bpermute((((lane >> 1) & 7) + 32 * (lane & 1)) * 4, (int)(hp[0] | (hp[1] << 16)));
        if (lane < 16) __builtin_amdgcn_raw_buffer_store_b64(v2u{hq, tag}, rs, WS_GH + rho * 128 + lane * 8, 0, AUX_SC1);
      } else if (w == 1) {
        final_cell(ws, tag, 16 * j + r, lane, out, (unsigned*)(ws + WS_EPOCH));
      }
    } else if (is_mlp) {
      const int fg = 8 * j + ((r - 16) >> 1), nh = r & 1;
      const int kq = w >> 1, cth = w & 1;
      BAR();
      v4u gf[4], uf[4], df[2];
      {
        const lds_p p = L + L_GT + (128 * kq + 8 * g + (n16 >> 2)) * 64 + (16 * cth + 4 * (lane & 3)) * 2;
#pragma unroll
        for (int t = 0; t < 4; ++t) { gf[t] = tr_frag(p + t * 32 * 64, 64); uf[t] = tr_frag(p + 32768 + t * 32 * 64, 64); }
        const lds_p pd = L + L_DN + (8 * g + (n16 >> 2)) * 512 + (32 * w + 4 * (lane & 3)) * 2;
#pragma unroll
        for (int t = 0; t < 2; ++t) df[t] = tr_frag(pd + t * 32, 512);
      }
      const bool up = lane & 8;
      unsigned r3w = lds0 + L_R3 + ((((kq * 2 + (up ? 1 : 0)) * 2 + cth) * 8 + (lane & 7)) * 16 + 4 * g) * 4;
      {
        const v4u ones = {0x3f803f80u, 0x3f803f80u, 0x3f803f80u, 0x3f803f80u};
        f32x4 cg = {0.f, 0.f, 0.f, 0.f}, cu = {0.f, 0.f, 0.f, 0.f};
#pragma unroll
        for (int t = 0; t < 4; ++t) { cg = mfma(gf[t], ones, cg); cu = mfma(uf[t], ones, cu); }
        *(LAS f32x4*)(size_t)r3w = f32x4{up ? cu[0] : cg[0], up ? cu[1] : cg[1], up ? cu[2] : cg[2], up ? cu[3] : cg[3]};
        BAR();
        if (w == 0) {
          const LAS float* r3 = (const LAS float*)(L + L_R3) + (lane >> 5) * 256 + ((lane >> 4) & 1) * 128 + (lane & 15);
          ((LAS float*)(L + L_CS))[lane] = (r3[0] + r3[512]) + (r3[1024] + r3[1536]);
        }
      }
      unsigned goff = WS_GH + 16 * w * 128 + lane * 16;
      unsigned hnw = lds0 + L_HN + (lane & 7) * 1056 + (16 * w + (lane >> 3)) * 8, hrow = lds0 + L_HN + w * 1056 + lane * 16, hnr = lds0 + L_HN + (lane & 7) * 1056 + (128 * kq + 8 * g) * 2;
      unsigned r3r = lds0 + L_R3 + (((lane >> 4) & 1) * 8 + 2 * (w & 3) + (lane >> 5)) * 64 + (lane & 15) * 4;
      unsigned hhw = lds0 + L_HH + (2 * (w & 3) + (lane >> 5)) * 96 + (lane & 31) * 2, hhr = lds0 + L_HH + (lane & 7) * 96 + g * 16;
      int foff = WS_FP + ((64 * nh + 8 * w + (up ? 4 : 0) + g) * FP_C + fg) * 128 + (lane & 7) * 16;
      asm volatile("" : "+v"(goff), "+v"(hnw), "+v"(hrow), "+v"(hnr), "+v"(r3w), "+v"(r3r), "+v"(hhw), "+v"(hhr), "+v"(foff));
      asm volatile("" : "+v"(gf[0]), "+v"(gf[1]), "+v"(gf[2]), "+v"(gf[3]), "+v"(uf[0]), "+v"(uf[1]), "+v"(uf[2]), "+v"(uf[3]));
      asm volatile("" : "+v"(df[0]), "+v"(df[1]));
      v4u h0, h1;
      poll2x4(ws, tag, goff, goff + 1024, h0, h1);
      *(LAS v2u*)(size_t)hnw = v2u{h0.x, h0.z};
      *(LAS v2u*)(size_t)(hnw + 64) = v2u{h1.x, h1.z};
      BAR();
      {
        const lds_p pa = (lds_p)(size_t)hnr;
        f32x4 ag = {0.f, 0.f, 0.f, 0.f}, au = {0.f, 0.f, 0.f, 0.f};
#pragma unroll
        for (int t = 0; t < 4; ++t) {
          const v4u a = lds_ld128(pa + t * 64);
          ag = mfma(gf[t], a, ag);
          au = mfma(uf[t], a, au);
        }
        {
          const v4u hr = lds_ld128((lds_p)(size_t)hrow);
          const float f[8] = {bf_lo(hr.x), bf_hi(hr.x), bf_lo(hr.y), bf_hi(hr.y), bf_lo(hr.z), bf_hi(hr.z), bf_lo(hr.w), bf_hi(hr.w)};
          const float c0 = asf(__builtin_amdgcn_readfirstlane(asu(f[0])));
          float s1 = 0.f, s2 = 0.f;
#pragma unroll
          for (int i = 0; i < 8; ++i) { const float d = f[i] - c0; s1 += d; s2 = fmaf(d, d, s2); }
          s1 += dpp<0xB1>(s1); s2 += dpp<0xB1>(s2);
          s1 += dpp<0x4E>(s1); s2 += dpp<0x4E>(s2);
          s1 += dpp<0x141>(s1); s2 += dpp<0x141>(s2);
          s1 += dpp<0x140>(s1); s2 += dpp<0x140>(s2);
          auto sw = __builtin_amdgcn_permlane16_swap(asu(s1), asu(s2), false, false);
          const float t = x32_sum(asf(sw[0]) + asf(sw[1])) * (1.0f / D);
          const float m1 = asf(__builtin_amdgcn_readlane(asu(t), 0)), m2 = asf(__builtin_amdgcn_readlane(asu(t), 16));
          const float var = rbf(fmaxf(fmaf(-m1, m1, m2), 0.f));
          const float den = rbf(__builtin_amdgcn_sqrtf(rbf(var + 1e-5f)));
          if (lane == 0) *(LAS v2u*)(L + L_ST + w * 8) = v2u{asu(rbf(c0 + m1)), asu(__builtin_amdgcn_rcpf(den))};
        }
        *(LAS f32x4*)(size_t)r3w = f32x4{up ? au[0] : ag[0], up ? au[1] : ag[1], up ? au[2] : ag[2], up ? au[3] : ag[3]};
      }
      BAR();
      if (w < 4) {
        const int bb = 2 * w + (lane >> 5), n32 = lane & 31;
        const LAS float* r3 = (const LAS float*)(size_t)r3r;
        const v2u stt = *(const LAS v2u*)(L + L_ST + bb * 8);
        const float csg = ((const LAS float*)(L + L_CS))[n32], csu = ((const LAS float*)(L + L_CS))[32 + n32];
        float gg = (r3[0] + r3[512]) + (r3[1024] + r3[1536]);
        float uu = (r3[256] + r3[768]) + (r3[1280] + r3[1792]);
        gg = rbf(fmaf(-asf(stt.x), csg, gg) * asf(stt.y));
        uu = rbf(fmaf(-asf(stt.x), csu, uu) * asf(stt.y));
        const float sg = rbf(__builtin_amdgcn_rcpf(1.0f + __builtin_amdgcn_exp2f(-1.4426950408889634f * gg)));
        *(LAS unsigned short*)(size_t)hhw = (unsigned short)pack_bf(sg * uu, 0.f);
      }
      BAR();
      {
        const v4u a = lds_ld128((lds_p)(size_t)hhr);
        const f32x4 z = {0.f, 0.f, 0.f, 0.f};
        const f32x4 d0 = mfma(df[0], a, z), d1 = mfma(df[1], a, z);
        __builtin_amdgcn_raw_buffer_store_b128(v4u{pack_bf(up ? d1[0] : d0[0], up ? d1[1] : d0[1]), tag, pack_bf(up ? d1[2] : d0[2], up ? d1[3] : d0[3]), tag}, rs, foff, 0, AUX_SC1);
      }
    }
  } else {
    const int hw = wv, kq = kq0;
    BAR();
    asm volatile("s_waitcnt vmcnt(6)" : "+v"(xa), "+v"(xb));
    {
      float fa[8] = {bf_lo(xa.x), bf_hi(xa.x), bf_lo(xa.y), bf_hi(xa.y), bf_lo(xa.z), bf_hi(xa.z), bf_lo(xa.w), bf_hi(xa.w)};
      float fb[8] = {bf_lo(xb.x), bf_hi(xb.x), bf_lo(xb.y), bf_hi(xb.y), bf_lo(xb.z), bf_hi(xb.z), bf_lo(xb.w), bf_hi(xb.w)};
      layernorm8(fa);
      layernorm8(fb);
      if (g == kq) {
        *(LAS v4u*)(L + L_HN + (2 * hw) * 288 + n16 * 16) = v4u{pack_bf(fa[0], fa[1]), pack_bf(fa[2], fa[3]), pack_bf(fa[4], fa[5]), pack_bf(fa[6], fa[7])};
        *(LAS v4u*)(L + L_HN + (2 * hw + 1) * 288 + n16 * 16) = v4u{pack_bf(fb[0], fb[1]), pack_bf(fb[2], fb[3]), pack_bf(fb[4], fb[5]), pack_bf(fb[6], fb[7])};
      }
    }
    asm volatile("s_waitcnt vmcnt(0)" : "+v"(ep), "+v"(pf) :: "memory");
    const unsigned tag = ep + 1;
    lds_arrive(flags + 0, lane);
    lds_wait(flags + 0, 4);
    {
      const lds_p pa = L + L_HN + (lane & 7) * 288 + g * 16;
      const lds_p pb = L + L_WQ + (8 * g + (n16 >> 2)) * 128 + (16 * hw + 4 * (lane & 3)) * 2;
      f32x4 acc0 = mfma(tr_frag(pb, 128), lds_ld128(pa), f32x4{0.f, 0.f, 0.f, 0.f});
      f32x4 acc1 = mfma(tr_frag(pb + 32 * 128, 128), lds_ld128(pa + 64), f32x4{0.f, 0.f, 0.f, 0.f});
      acc0 = mfma(tr_frag(pb + 64 * 128, 128), lds_ld128(pa + 128), acc0);
      acc1 = mfma(tr_frag(pb + 96 * 128, 128), lds_ld128(pa + 192), acc1);
      publish_tile(acc0 + acc1, rs, WS_QP + (kq * QP_K + 32 * j + 4 * ct8 + hw) * 128, QP_B * 128, tag, lane);
    }
    while ((unsigned)(__builtin_amdgcn_s_memrealtime() - t_start) < QDELAY) __builtin_amdgcn_s_sleep(1);
    {
      const int off = WS_QP + (b * QP_B + 32 * j + 8 * hw) * 128 + lane * 16;
      v4u v[4];
      pollNx4<4, QP_K * 128>(ws, tag, off, v);
      const float q0 = (asf(v[0].x) + asf(v[1].x)) + (asf(v[2].x) + asf(v[3].x)), q1 = (asf(v[0].z) + asf(v[1].z)) + (asf(v[2].z) + asf(v[3].z));
      *(LAS unsigned*)(L + L_Q + hw * 320 + lane * 4) = pack_bf(q0, q1);
      if (hw == 0 && lane == 0) flags[1] = tag;
    }
    lds_arrive(flags + 6, lane);
    __builtin_amdgcn_s_setprio(0);
    if (hw == 0) {
      while (flags[2] < 8u) __builtin_amdgcn_s_sleep(1);
      asm volatile("" ::: "memory");
      const float mw = ((const LAS float*)(L + L_MAX))[lane & 31], lw = ((const LAS float*)(L + L_SUM))[lane & 31];
      float mx = max2(mw, dpp<0x124>(mw));
      mx = max2(mx, dpp<0x128>(mx));
      mx = x16_max(mx);
      const float f = __builtin_amdgcn_exp2f((mw - mx) * 1.4426950408889634f);
      float l = f * lw;
      l += dpp<0x124>(l);
      l += dpp<0x128>(l);
      l = x16_sum(l);
      if (lane < 32) ((LAS float*)(L + L_MSC))[(lane & 3) * 8 + (lane >> 2)] = f * __builtin_amdgcn_rcpf(l);
      lds_arrive(flags + 4, lane);
      if (lane < 4) {
        const float lse = mx * 1.4426950408889634f + __builtin_amdgcn_logf(l);
        __builtin_amdgcn_raw_buffer_store_b64(v2u{asu(lse), tag}, rs, WS_LS + (b * HKV + j) * 128 + (lane * 4 + s) * 8, 0, AUX_SC1);
      }
    }
    if (is_wo) {
      const int h = 4 * j + (r >> 2), ns = r & 3;
      while (flags[4] < DMAFLAG) __builtin_amdgcn_s_sleep(2);
      const unsigned char* src = (const unsigned char*)wo + ((size_t)(128 * h + 32 * hw + g) * D + 128 * ns) * 2 + n16 * 16;
#pragma unroll
      for (int i = 0; i < 8; ++i) glds16(src + (size_t)i * 4 * D * 2, lds0 + L_W + (32 * hw + 4 * i) * 256);
      asm volatile("s_waitcnt vmcnt(0)" ::: "memory");
      BAR();
    } else if (is_mlp) {
      while (flags[4] < DMAFLAG) __builtin_amdgcn_s_sleep(2);
      const int fg = 8 * j + ((r - 16) >> 1), nh = r & 1;
      const size_t go = ((size_t)(128 * hw + (lane >> 2)) * FF + 32 * fg) * 2 + (lane & 3) * 16;
#pragma unroll
      for (int i = 0; i < 8; ++i) {
        if (i >= 2) asm volatile("s_waitcnt vmcnt(3)" ::: "memory");
        glds16((const unsigned char*)wg + go + (size_t)i * 16 * FF * 2, lds0 + L_GT + (128 * hw + 16 * i) * 64);
        if (i >= 2) asm volatile("s_waitcnt vmcnt(3)" ::: "memory");
        glds16((const unsigned char*)wu + go + (size_t)i * 16 * FF * 2, lds0 + L_UP + (128 * hw + 16 * i) * 64);
      }
      const unsigned char* sd = (const unsigned char*)wd + ((size_t)(32 * fg + 8 * hw + (lane >> 5)) * D + 256 * nh) * 2 + (lane & 31) * 16;
#pragma unroll
      for (int i = 0; i < 4; ++i) {
        asm volatile("s_waitcnt vmcnt(5)" ::: "memory");
        glds16(sd + (size_t)i * 2 * D * 2, lds0 + L_DN + (8 * hw + 2 * i) * 512);
      }
      asm volatile("s_waitcnt vmcnt(0)" ::: "memory");
      BAR();
    }
  }
}

}

extern "C" void kernel_launch(void* const* d_in, const int* in_sizes, int n_in, void* d_out, int out_size, void* d_ws,
                              size_t ws_size, hipStream_t stream) {
  (void)in_sizes; (void)n_in; (void)out_size; (void)ws_size;
  (void)hipFuncSetAttribute((const void*)fused_decode, hipFuncAttributeMaxDynamicSharedMemorySize, LDS_BYTES);
  fused_decode<<<256, NTHR, LDS_BYTES, stream>>>(
      (const unsigned short*)d_in[0], (const unsigned short*)d_in[1], (const unsigned short*)d_in[2], (const unsigned short*)d_in[3],
      (const unsigned short*)d_in[4], (const unsigned short*)d_in[6], (const unsigned short*)d_in[7], (const unsigned short*)d_in[8],
      (unsigned short*)d_out, (unsigned char*)d_ws);
}
```

```cpp
#include <hip/hip_runtime.h>
#include <hip/hip_bf16.h>
#include <stdint.h>

namespace {

typedef unsigned v4u __attribute__((ext_vector_type(4)));
typedef unsigned v2u __attribute__((ext_vector_type(2)));
typedef float f32x4 __attribute__((ext_vector_type(4)));
typedef __bf16 bf16x8 __attribute__((ext_vector_type(8)));
typedef unsigned long long u64;
#define LAS __attribute__((address_space(3)))
typedef LAS unsigned char* lds_p;

constexpr int D = 512, HQ = 32, HKV = 8, SK = 4096, DH = 128, WIN = 1024, FF = 1792;
constexpr int NTHR = 768;
constexpr int AUX_SC1 = 16;
constexpr unsigned SPIN_CAP = 1u << 16;
constexpr int PD = 3;
constexpr int THRN = 10;
constexpr unsigned DMAFLAG = 2;
constexpr unsigned QDELAY = 290;
constexpr unsigned QAFTER = 60;
constexpr int CODE_LINES = 119;

constexpr int WS_EPOCH = 0;
constexpr int PAD = 1;
constexpr int QP_B = 256 + PAD, QP_K = 8 * QP_B;
constexpr int RP_C = 32 + PAD;
constexpr int FP_C = 56 + PAD;
constexpr int WS_QP = 1 << 16;
constexpr int WS_OP = WS_QP + 4 * QP_K * 128;
constexpr int WS_LS = WS_OP + (1 << 19);
constexpr int WS_RP = WS_LS + (1 << 15);
constexpr int WS_GH = WS_RP + 128 * RP_C * 128;
constexpr int WS_FP = WS_GH + (1 << 14);
constexpr int WS_END = WS_FP + 128 * FP_C * 128;

constexpr int L_RED = 0;
constexpr int L_WQ = 0;
constexpr int L_Q = 16384;
constexpr int L_MAX = L_Q + 4 * 320;
constexpr int L_SUM = L_MAX + 128;
constexpr int L_FLAG = L_SUM + 128;
constexpr int L_MSC = L_FLAG + 64;
constexpr int L_CS = L_MSC + 128;
constexpr int L_ST = L_CS + 256;
constexpr int L_OS = L_ST + 64;
constexpr int L_HN = L_OS + 8 * 288;
constexpr int L_R3 = L_HN + 8 * 1056;
constexpr int L_HH = L_R3 + 8192;
constexpr int L_W = ((L_HH + 8 * 96 + 255) / 256) * 256;
constexpr int L_GT = L_W, L_UP = L_W + 32768, L_DN = L_W + 65536;
constexpr int LDS_BYTES = L_W + 98304;
static_assert(LDS_BYTES <= 160 * 1024 && LDS_BYTES > 80 * 1024, "one workgroup per CU");

__device__ __forceinline__ float bf_lo(unsigned u) { return __builtin_bit_cast(float, u << 16); }
__device__ __forceinline__ float bf_hi(unsigned u) { return __builtin_bit_cast(float, u & 0xffff0000u); }
__device__ __forceinline__ unsigned pack_bf(float lo, float hi) {
  typedef __bf16 bf2 __attribute__((ext_vector_type(2)));
  bf2 r = {(__bf16)lo, (__bf16)hi};
  return __builtin_bit_cast(unsigned, r);
}
__device__ __forceinline__ float rbf(float f) { return bf_lo(pack_bf(f, f)); }
__device__ __forceinline__ float asf(unsigned u) { return __builtin_bit_cast(float, u); }
__device__ __forceinline__ unsigned asu(float f) { return __builtin_bit_cast(unsigned, f); }
template <int CTRL> __device__ __forceinline__ float dpp(float x) {
  return __builtin_bit_cast(float, __builtin_amdgcn_mov_dpp(__builtin_bit_cast(int, x), CTRL, 0xf, 0xf, true));
}
__device__ __forceinline__ float max3(float a, float b, float c) {
  float r;
  asm("v_max3_f32 %0, %1, %2, %3" : "=v"(r) : "v"(a), "v"(b), "v"(c));
  return r;
}
__device__ __forceinline__ float max2(float a, float b) {
  float r;
  asm("v_max_f32 %0, %1, %2" : "=v"(r) : "v"(a), "v"(b));
  return r;
}
__device__ __forceinline__ float x16_sum(float x) {
  auto s = __builtin_amdgcn_permlane16_swap(asu(x), asu(x), false, false);
  return asf(s[0]) + asf(s[1]);
}
__device__ __forceinline__ float x32_sum(float x) {
  auto s = __builtin_amdgcn_permlane32_swap(asu(x), asu(x), false, false);
  return asf(s[0]) + asf(s[1]);
}
__device__ __forceinline__ float x16_max(float x) {
  auto s = __builtin_amdgcn_permlane16_swap(asu(x), asu(x), false, false);
  return max2(asf(s[0]), asf(s[1]));
}
__device__ __forceinline__ float x32_max(float x) {
  auto s = __builtin_amdgcn_permlane32_swap(asu(x), asu(x), false, false);
  return max2(asf(s[0]), asf(s[1]));
}
__device__ __forceinline__ f32x4 mfma(v4u a, v4u b, f32x4 c) {
  return __builtin_amdgcn_mfma_f32_16x16x32_bf16(__builtin_bit_cast(bf16x8, a), __builtin_bit_cast(bf16x8, b), c, 0, 0, 0);
}
__device__ __forceinline__ v2u tr_read(lds_p p) {
  typedef short v4s __attribute__((ext_vector_type(4)));
  return __builtin_bit_cast(v2u, __builtin_amdgcn_ds_read_tr16_b64_v4i16((LAS v4s*)p));
}
__device__ __forceinline__ v4u tr_frag(lds_p p, int row_stride) {
  const v2u a = tr_read(p), b = tr_read(p + 4 * row_stride);
  return v4u{a.x, a.y, b.x, b.y};
}
__device__ __forceinline__ v4u lds_ld128(lds_p p) { return *(const LAS v4u*)p; }
__device__ __forceinline__ void glds16(const void* gsrc, unsigned lds_dst) {
  unsigned keep;
  asm volatile("s_mov_b32 %0, m0\n\ts_mov_b32 m0, %2\n\ts_nop 0\n\tglobal_load_lds_dwordx4 %1, off nt\n\ts_mov_b32 m0, %0"
               : "=&s"(keep) : "v"(gsrc), "s"(lds_dst) : "memory");
}
#define BAR() do { asm volatile("s_waitcnt lgkmcnt(0)" ::: "memory"); __builtin_amdgcn_s_barrier(); asm volatile("" ::: "memory"); } while (0)
__device__ __forceinline__ void lds_arrive(volatile LAS unsigned* p, int lane) {
  asm volatile("" ::: "memory");
  if (lane == 0) asm volatile("ds_add_u32 %0, %1" :: "v"((unsigned)(size_t)p), "v"(1u) : "memory");
  asm volatile("" ::: "memory");
}
__device__ __forceinline__ void lds_wait(volatile LAS unsigned* p, unsigned n) {
  while (*p < n) { }
  asm volatile("" ::: "memory");
}
typedef unsigned long long mask_t;
__device__ __forceinline__ mask_t bad4(v4u v, unsigned tag) { return __builtin_amdgcn_ballot_w64(v.y != tag) | __builtin_amdgcn_ballot_w64(v.w != tag); }
__device__ __forceinline__ mask_t bad2(v2u v, unsigned tag) { return __builtin_amdgcn_ballot_w64(v.y != tag); }
#define PL4(n_, o_, m_) "s_mov_b64 exec, %" #m_ "\n\tglobal_load_dwordx4 %" #n_ ", %" #o_ ", %[base] sc1\n\t"
#define PL2(n_, o_, m_) "s_mov_b64 exec, %" #m_ "\n\tglobal_load_dwordx2 %" #n_ ", %" #o_ ", %[base] sc1\n\t"
#define PLEND "s_mov_b64 exec, -1\n\ts_waitcnt vmcnt(0)"
__device__ __forceinline__ void poll1x4(const unsigned char* ws, unsigned tag, unsigned o0, v4u& a) {
  a = v4u{0u, ~tag, 0u, ~tag};
  for (unsigned spins = 0; spins <= SPIN_CAP; ++spins) {
    const mask_t m0 = bad4(a, tag);
    if (m0 == 0) break;
    asm volatile(PL4(0, 1, 2) PLEND : "+v"(a) : "v"(o0), "s"(m0), [base] "s"(ws) : "memory");
    __builtin_amdgcn_s_sleep(2);
  }
}
__device__ __forceinline__ void poll2x4(const unsigned char* ws, unsigned tag, unsigned o0, unsigned o1, v4u& a, v4u& b) {
  a = b = v4u{0u, ~tag, 0u, ~tag};
  for (unsigned spins = 0; spins <= SPIN_CAP; ++spins) {
    const mask_t m0 = bad4(a, tag), m1 = bad4(b, tag);
    if ((m0 | m1) == 0) break;
    asm volatile(PL4(0, 2, 4) PL4(1, 3, 5) PLEND : "+v"(a), "+v"(b) : "v"(o0), "v"(o1), "s"(m0), "s"(m1), [base] "s"(ws) : "memory");
  }
}
__device__ __forceinline__ void poll5x2(const unsigned char* ws, unsigned tag, unsigned oa, unsigned ob, v2u (&v)[5]) {
  unsigned o[4];
#pragma unroll
  for (int i = 0; i < 4; ++i) o[i] = oa + i * 512;
#pragma unroll
  for (int i = 0; i < 5; ++i) v[i] = v2u{0u, ~tag};
  for (unsigned spins = 0; spins <= SPIN_CAP; ++spins) {
    mask_t m[5], any = 0;
#pragma unroll
    for (int i = 0; i < 5; ++i) { m[i] = bad2(v[i], tag); any |= m[i]; }
    if (any == 0) break;
    asm volatile(PL2(0, 5, 10) PL2(1, 6, 11) PL2(2, 7, 12) PL2(3, 8, 13) PL2(4, 9, 14) PLEND
                 : "+v"(v[0]), "+v"(v[1]), "+v"(v[2]), "+v"(v[3]), "+v"(v[4])
                 : "v"(o[0]), "v"(o[1]), "v"(o[2]), "v"(o[3]), "v"(ob),
                   "s"(m[0]), "s"(m[1]), "s"(m[2]), "s"(m[3]), "s"(m[4]), [base] "s"(ws) : "memory");
  }
}
template <int N, int STEP>
__device__ __forceinline__ void pollNx4(const unsigned char* ws, unsigned tag, unsigned o0, v4u (&v)[N]) {
  static_assert(N == 4 || N == 7, "");
  unsigned o[N];
#pragma unroll
  for (int i = 0; i < N; ++i) { v[i] = v4u{0u, ~tag, 0u, ~tag}; o[i] = o0 + i * STEP; }
  for (unsigned spins = 0; spins <= SPIN_CAP; ++spins) {
    mask_t m[N], any = 0;
#pragma unroll
    for (int i = 0; i < N; ++i) { m[i] = bad4(v[i], tag); any |= m[i]; }
    if (any == 0) break;
    if constexpr (N == 4) {
      asm volatile(PL4(0, 4, 8) PL4(1, 5, 9) PL4(2, 6, 10) PL4(3, 7, 11) PLEND
                   : "+v"(v[0]), "+v"(v[1]), "+v"(v[2]), "+v"(v[3])
                   : "v"(o[0]), "v"(o[1]), "v"(o[2]), "v"(o[3]), "s"(m[0]), "s"(m[1]), "s"(m[2]), "s"(m[3]), [base] "s"(ws) : "memory");
    } else {
      asm volatile(PL4(0, 7, 14) PL4(1, 8, 15) PL4(2, 9, 16) PL4(3, 10, 17) PL4(4, 11, 18) PL4(5, 12, 19) PL4(6, 13, 20) PLEND
                   : "+v"(v[0]), "+v"(v[1]), "+v"(v[2]), "+v"(v[3]), "+v"(v[4]), "+v"(v[5]), "+v"(v[6])
                   : "v"(o[0]), "v"(o[1]), "v"(o[2]), "v"(o[3]), "v"(o[4]), "v"(o[5]), "v"(o[6]),
                     "s"(m[0]), "s"(m[1]), "s"(m[2]), "s"(m[3]), "s"(m[4]), "s"(m[5]), "s"(m[6]), [base] "s"(ws) : "memory");
    }
  }
}

__device__ __forceinline__ void layernorm8(float (&f)[8]) {
  const float c0 = asf(__builtin_amdgcn_readfirstlane(asu(f[0])));
  float s1 = 0.f, s2 = 0.f;
#pragma unroll
  for (int i = 0; i < 8; ++i) { const float d = f[i] - c0; s1 += d; s2 = fmaf(d, d, s2); }
  s1 += dpp<0xB1>(s1); s2 += dpp<0xB1>(s2);
  s1 += dpp<0x4E>(s1); s2 += dpp<0x4E>(s2);
  s1 += dpp<0x141>(s1); s2 += dpp<0x141>(s2);
  s1 += dpp<0x140>(s1); s2 += dpp<0x140>(s2);
  s1 = x16_sum(s1); s2 = x16_sum(s2);
  s1 = x32_sum(s1) * (1.0f / D); s2 = x32_sum(s2) * (1.0f / D);
  const float var = rbf(fmaxf(fmaf(-s1, s1, s2), 0.f));
  const float den = rbf(__builtin_amdgcn_sqrtf(rbf(var + 1e-5f)));
  const float inv = __builtin_amdgcn_rcpf(den), mb = rbf(c0 + s1);
#pragma unroll
  for (int i = 0; i < 8; ++i) f[i] = rbf(f[i] - mb) * inv;
}

__device__ __forceinline__ void publish_tile(f32x4 d, __amdgpu_buffer_rsrc_t rs, int base, int stride_b, unsigned tag, int lane) {
  const bool t = lane & 8;
  const int src = ((lane >> 3) + 8 * (lane & 1) + 16 * ((lane >> 1) & 3)) * 4;
  const unsigned v0 = (unsigned)__builtin_amdgcn_ds_bpermute(src, (int)asu(t ? d[2] : d[0]));
  const unsigned v1 = (unsigned)__builtin_amdgcn_ds_bpermute(src, (int)asu(t ? d[3] : d[1]));
  __builtin_amdgcn_raw_buffer_store_b128(v4u{v0, tag, v1, tag}, rs, base + (lane >> 3) * stride_b + (lane & 7) * 16, 0, AUX_SC1);
}

__device__ __forceinline__ void final_cell(const unsigned char* ws, unsigned tag, int cell, int lane, unsigned short* out, unsigned* ws_epoch) {
  const int p = lane & 7, row = lane >> 4;
  v4u hv;
  poll1x4(ws, tag, WS_GH + cell * 128 + p * 16, hv);
  float hsel = row == 0 ? bf_lo(hv.x) : row == 1 ? bf_hi(hv.x) : row == 2 ? bf_lo(hv.z) : bf_hi(hv.z);
  unsigned short* po = out + p * D + 4 * cell + row;
  asm volatile("" : "+v"(hsel), "+v"(po));
  v4u v[7];
  pollNx4<7, 8 * 128>(ws, tag, WS_FP + (cell * FP_C + (lane >> 3)) * 128 + p * 16, v);
  float a0 = 0.f, a1 = 0.f, a2 = 0.f, a3 = 0.f;
#pragma unroll
  for (int i = 0; i < 7; ++i) { a0 += bf_lo(v[i].x); a1 += bf_hi(v[i].x); a2 += bf_lo(v[i].z); a3 += bf_hi(v[i].z); }
  auto s01 = __builtin_amdgcn_permlane16_swap(asu(a0), asu(a1), false, false);
  auto s23 = __builtin_amdgcn_permlane16_swap(asu(a2), asu(a3), false, false);
  const float c0 = asf(s01[0]) + asf(s01[1]), c1 = asf(s23[0]) + asf(s23[1]);
  auto sc = __builtin_amdgcn_permlane32_swap(asu(c0), asu(c1), false, false);
  float e = asf(sc[0]) + asf(sc[1]);
  e += dpp<0x128>(e);
  if ((lane & 8) == 0) *po = (unsigned short)pack_bf(hsel + rbf(e), 0.f);
  if (cell == 0 && lane == 0) __hip_atomic_store(ws_epoch, tag, __ATOMIC_RELAXED, __HIP_MEMORY_SCOPE_AGENT);
}

__global__ void __launch_bounds__(NTHR, 3)
fused_decode(const unsigned short* __restrict__ x, const unsigned short* __restrict__ kc, const unsigned short* __restrict__ vc,
             const unsigned short* __restrict__ wq, const unsigned short* __restrict__ wo, const unsigned short* __restrict__ wg,
             const unsigned short* __restrict__ wu, const unsigned short* __restrict__ wd, unsigned short* __restrict__ out,
             unsigned char* __restrict__ ws) {
  unsigned long long code_base;
  asm volatile("s_getpc_b64 %0" : "=s"(code_base));
  code_base &= ~0xffull;
  extern __shared__ __attribute__((aligned(16))) unsigned char smem[];
  const lds_p L = (lds_p)smem;
  const unsigned lds0 = (unsigned)(size_t)L;
  const int tid = threadIdx.x, lane = tid & 63;
  const int wv = __builtin_amdgcn_readfirstlane(tid >> 6);
  const int w = wv - 4;
  const int c = blockIdx.x, j = c & 7, r = c >> 3;
  const int b = r >> 2, s = r & 3;
  const int g = lane >> 4, n16 = lane & 15;
  const bool is_wo = r < 16, is_mlp = (r >= 16) && (j < 7);
  const __amdgpu_buffer_rsrc_t rs = __builtin_amdgcn_make_buffer_rsrc((void*)ws, 0, WS_END, 0x00020000);
  volatile LAS unsigned* flags = (volatile LAS unsigned*)(L + L_FLAG);

  const u64 t_start = __builtin_amdgcn_s_memrealtime();
  const int kq0 = r >> 3, ct8 = r & 7;
  v4u xa, xb; unsigned ep, pf;
  asm volatile("" : "=v"(xa), "=v"(xb), "=v"(ep), "=v"(pf));
  if (wv < 4) {
    const int hw = wv;
    __builtin_amdgcn_s_setprio(2);
    const unsigned zero = 0;
    const unsigned xo = (2 * hw) * (D * 2) + lane * 16;
    asm volatile("global_load_dwordx4 %0, %1, %2" : "+v"(xa) : "v"(xo), "s"(x));
    asm volatile("global_load_dwordx4 %0, %1, %2 offset:1024" : "+v"(xb) : "v"(xo), "s"(x));
    const unsigned char* src = (const unsigned char*)wq + ((size_t)(128 * kq0 + 32 * hw + (lane >> 3)) * (HQ * DH) + 512 * j + 64 * ct8) * 2 + (lane & 7) * 16;
#pragma unroll
    for (int i = 0; i < 4; ++i) glds16(src + (size_t)i * 8 * (HQ * DH) * 2, lds0 + L_WQ + (32 * hw + 8 * i) * 128);
    asm volatile("global_load_dword %0, %1, %2" : "+v"(ep) : "v"(zero), "s"(ws));
    const unsigned po = min((r * 4 + hw) * 2 + (lane & 1), CODE_LINES - 1) * 128;
    asm volatile("global_load_dword %0, %1, %2" : "+v"(pf) : "v"(po), "s"(code_base));
  }
  int wv2 = wv;
  asm volatile("" : "+s"(wv2));

  if (__builtin_expect(wv2 >= 4, 1)) {
    const size_t tile = ((size_t)(b * HKV + j) * SK + (SK - WIN) + 256 * s + 32 * w) * (DH * 2);
    const unsigned char* kb = (const unsigned char*)kc + tile + 4096;
    const unsigned char* vb = (const unsigned char*)vc + tile;
    const unsigned koff = (lane & 7) * 256 + (2 * g + ((lane >> 3) & 1)) * 16;
    const unsigned voff = (16 * (g >> 1) + 4 * (g & 1)) * 256 + n16 * 16;
    v4u K[8], V[8];
#define LDK(i, imm) asm volatile("global_load_dwordx4 %0, %1, %2 offset:" #imm " nt" : "=v"(K[i]) : "v"(koff), "s"(kb))
#define LDV(i, imm) asm volatile("global_load_dwordx4 %0, %1, %2 offset:" #imm " nt" : "=v"(V[i]) : "v"(voff), "s"(vb))
#define THR(n) do { if (n < THRN || __builtin_amdgcn_readfirstlane(flags[6]) < 4) asm volatile("s_waitcnt vmcnt(%0)" :: "i"(PD - 1)); } while (0)
    static_assert(PD == 3, "three loads go out in front of the barrier");
    LDK(0, -4096); LDK(1, -3968); LDK(2, -2048);
    if (w == 0 && lane < 8) flags[lane] = 0;
    BAR();
    THR(3); LDK(3, -1920);
    THR(4); LDK(4, 0); THR(5); LDK(5, 128); THR(6); LDK(6, 2048); THR(7); LDK(7, 2176);
    THR(8); LDV(0, 0); THR(9); LDV(1, 256); THR(10); LDV(2, 512); THR(11); LDV(3, 768);
    THR(12); LDV(4, 2048); THR(13); LDV(5, 2304); THR(14); LDV(6, 2560); THR(15); LDV(7, 2816);
    while (flags[6] < 4u) __builtin_amdgcn_s_sleep(1);
    asm volatile("" ::: "memory");
    const unsigned tag = flags[1];
    const lds_p qa = L + L_Q + (n16 & 3) * 320 + (2 * g + ((n16 >> 2) & 1)) * 16;
    const v4u q0 = lds_ld128(qa), q1 = lds_ld128(qa + 128);
    asm volatile("s_waitcnt vmcnt(8)" : "+v"(K[0]), "+v"(K[1]), "+v"(K[2]), "+v"(K[3]), "+v"(K[4]), "+v"(K[5]), "+v"(K[6]), "+v"(K[7]));
    f32x4 sa[4];
#pragma unroll
    for (int o = 0; o < 4; ++o) {
      sa[o] = mfma(K[2 * o], q0, f32x4{0.f, 0.f, 0.f, 0.f});
      sa[o] = mfma(K[2 * o + 1], q1, sa[o]);
    }
    float sc[8];
    {
      const bool par = lane & 4;
      float raw[8];
#pragma unroll
      for (int p = 0; p < 2; ++p)
#pragma unroll
        for (int i = 0; i < 4; ++i) {
          auto sw = __builtin_amdgcn_permlane32_swap(asu(sa[p][i]), asu(sa[p + 2][i]), false, false);
          const float a = par ? asf(sw[1]) : asf(sw[0]);
          raw[4 * p + i] = a + dpp<0x124>(a);
        }
#pragma unroll
      for (int i = 0; i < 8; i += 2) {
        const unsigned u = pack_bf(raw[i], raw[i + 1]);
        const unsigned t = pack_bf(bf_lo(u) * 0.08838834764831845f, bf_hi(u) * 0.08838834764831845f);
        sc[i] = bf_lo(t); sc[i + 1] = bf_hi(t);
      }
    }
    float m = max3(max3(sc[0], sc[1], sc[2]), max3(sc[3], sc[4], sc[5]), max2(sc[6], sc[7]));
    m = x32_max(x16_max(m));
    float pe[8], ls;
    {
      const float mc = m * 1.4426950408889634f;
#pragma unroll
      for (int i = 0; i < 8; ++i) pe[i] = __builtin_amdgcn_exp2f(fmaf(sc[i], 1.4426950408889634f, -mc));
      ls = ((pe[0] + pe[1]) + (pe[2] + pe[3])) + ((pe[4] + pe[5]) + (pe[6] + pe[7]));
    }
    v4u PA[4];
    {
      const v4u P = {pack_bf(pe[0], pe[1]), pack_bf(pe[2], pe[3]), pack_bf(pe[4], pe[5]), pack_bf(pe[6], pe[7])};
#pragma unroll
      for (int mq = 0; mq < 4; ++mq) {
        const bool on = (n16 >> 2) == mq;
        PA[mq] = v4u{on ? P.x : 0u, on ? P.y : 0u, on ? P.z : 0u, on ? P.w : 0u};
      }
    }
    ls = x32_sum(x16_sum(ls));
    if (lane < 4) {
      ((LAS float*)(L + L_MAX))[w * 4 + lane] = m;
      ((LAS float*)(L + L_SUM))[w * 4 + lane] = ls;
    }
    lds_arrive(flags + 2, lane);
    unsigned T[8][4];
#define XPOSE(n_)                                                                                         \
  _Pragma("unroll") for (int mm = 0; mm < 4; ++mm) {                                                      \
    T[2 * mm][n_] = __builtin_amdgcn_perm(V[2 * n_ + 1][mm], V[2 * n_][mm], 0x05040100u);                 \
    T[2 * mm + 1][n_] = __builtin_amdgcn_perm(V[2 * n_ + 1][mm], V[2 * n_][mm], 0x07060302u);             \
  }                                                                                                       \
  asm volatile("" : "+v"(T[0][n_]), "+v"(T[1][n_]), "+v"(T[2][n_]), "+v"(T[3][n_]), "+v"(T[4][n_]), "+v"(T[5][n_]), "+v"(T[6][n_]), "+v"(T[7][n_]));
    int ooff = WS_OP + ((b * HQ + 4 * j + (w >> 1)) * 4 + s) * 512 + (w & 1) * 256 + n16 * 16;
    const int dr = 64 * (w & 1) + 4 * n16 + g, dw = 8 * n16 + g;
    unsigned redr_a = lds0 + L_RED + ((w >> 1) * 128 + (dr ^ ((dr >> 5) << 1))) * 4;
    unsigned redw_a = lds0 + L_RED + w * 2048 + (dw ^ ((dw >> 5) << 1)) * 4;
    const int h = 4 * j + (r >> 2), ns = r & 3;
    unsigned doff = WS_OP + (w * HQ + h) * 4 * 512 + lane * 8;
    unsigned loff = WS_LS + (w * HKV + j) * 128 + ((h & 3) * 4 + (lane & 3)) * 8;
    int roff = WS_RP + ((32 * ns + 4 * w + g) * RP_C + h) * 128 + (lane & 7) * 16;
    unsigned osw = lds0 + L_OS + w * 288 + lane * 4, osr = lds0 + L_OS + (lane & 7) * 288 + g * 16;
    unsigned wfa = lds0 + L_W + (8 * g + (n16 >> 2)) * 256 + (16 * w + 4 * (lane & 3)) * 2;
    asm volatile("" : "+v"(PA[0]), "+v"(PA[1]), "+v"(PA[2]), "+v"(PA[3]), "+v"(ooff), "+v"(redr_a), "+v"(redw_a));
    asm volatile("" : "+v"(doff), "+v"(loff), "+v"(roff), "+v"(osw), "+v"(osr), "+v"(wfa));
    asm volatile("s_waitcnt vmcnt(6)" : "+v"(V[0]), "+v"(V[1])); XPOSE(0)
    asm volatile("s_waitcnt vmcnt(4)" : "+v"(V[2]), "+v"(V[3])); XPOSE(1)
    asm volatile("s_waitcnt vmcnt(2)" : "+v"(V[4]), "+v"(V[5])); XPOSE(2)
    asm volatile("s_waitcnt vmcnt(0)" : "+v"(V[6]), "+v"(V[7]));
    XPOSE(3)
    {
      f32x4 oa = {0.f, 0.f, 0.f, 0.f}, ob = {0.f, 0.f, 0.f, 0.f};
#pragma unroll
      for (int i = 0; i < 4; ++i) {
        oa = mfma(PA[i], v4u{T[i][0], T[i][1], T[i][2], T[i][3]}, oa);
        ob = mfma(PA[i], v4u{T[i + 4][0], T[i + 4][1], T[i + 4][2], T[i + 4][3]}, ob);
      }
      LAS float* red = (LAS float*)(size_t)redw_a;
#pragma unroll
      for (int hh = 0; hh < 4; ++hh) { red[hh * 128] = oa[hh]; *(LAS float*)(size_t)((redw_a ^ 16) + hh * 512) = ob[hh]; }
    }
    lds_arrive(flags + 4, lane);
    const LAS float* redr = (const LAS float*)(size_t)redr_a;
    lds_wait(flags + 4, 9);
    {
      const f32x4 ma = *(const LAS f32x4*)(L + L_MSC + (w >> 1) * 32), mb = *(const LAS f32x4*)(L + L_MSC + (w >> 1) * 32 + 16);
      float o = redr[0] * ma[0], o2 = redr[512] * ma[1];
      o = fmaf(redr[2 * 512], ma[2], o); o2 = fmaf(redr[3 * 512], ma[3], o2);
      o = fmaf(redr[4 * 512], mb[0], o); o2 = fmaf(redr[5 * 512], mb[1], o2);
      o = fmaf(redr[6 * 512], mb[2], o); o2 = fmaf(redr[7 * 512], mb[3], o2);
      o += o2;
      auto s16 = __builtin_amdgcn_permlane16_swap(asu(o), asu(o), false, false);
      const unsigned pr = pack_bf(asf(s16[0]), asf(s16[1]));
      auto s32 = __builtin_amdgcn_permlane32_swap(pr, pr, false, false);
      if (lane < 16) __builtin_amdgcn_raw_buffer_store_b128(v4u{s32[0], tag, s32[1], tag}, rs, ooff, 0, AUX_SC1);
    }

    if (is_wo) {
      BAR();
      v4u wf[4];
      {
        const lds_p p = (lds_p)(size_t)wfa;
#pragma unroll
        for (int t = 0; t < 4; ++t) wf[t] = tr_frag(p + t * 32 * 256, 256);
      }
      v2u dv[5];
      poll5x2(ws, tag, doff, loff, dv);
      {
        const float lse = asf(dv[4].x);
        float mx = max2(lse, dpp<0xB1>(lse));
        mx = max2(mx, dpp<0x4E>(mx));
        const float e = __builtin_amdgcn_exp2f(lse - mx);
        float es = e + dpp<0xB1>(e);
        es += dpp<0x4E>(es);
        const float wt = e * __builtin_amdgcn_rcpf(es);
        const float e0 = dpp<0x00>(wt), e1 = dpp<0x55>(wt), e2 = dpp<0xAA>(wt), e3 = dpp<0xFF>(wt);
        const float o0 = fmaf(e0, bf_lo(dv[0].x), e1 * bf_lo(dv[1].x)) + fmaf(e2, bf_lo(dv[2].x), e3 * bf_lo(dv[3].x));
        const float o1 = fmaf(e0, bf_hi(dv[0].x), e1 * bf_hi(dv[1].x)) + fmaf(e2, bf_hi(dv[2].x), e3 * bf_hi(dv[3].x));
        *(LAS unsigned*)(size_t)osw = pack_bf(o0, o1);
      }
      BAR();
      {
        const lds_p pa = (lds_p)(size_t)osr;
        f32x4 acc0 = mfma(wf[0], lds_ld128(pa), f32x4{0.f, 0.f, 0.f, 0.f});
        f32x4 acc1 = mfma(wf[1], lds_ld128(pa + 64), f32x4{0.f, 0.f, 0.f, 0.f});
        acc0 = mfma(wf[2], lds_ld128(pa + 128), acc0);
        acc1 = mfma(wf[3], lds_ld128(pa + 192), acc1);
        acc0 += acc1;
        if ((lane & 8) == 0) __builtin_amdgcn_raw_buffer_store_b128(v4u{pack_bf(acc0[0], acc0[1]), tag, pack_bf(acc0[2], acc0[3]), tag}, rs, roff, 0, AUX_SC1);
      }
      if (w == 0) {
        const int rho = 16 * j + r;
        const unsigned xv = *(const unsigned*)(x + (lane & 7) * D + 4 * rho + (g & 2));
        float xsel = (g & 1) ? bf_hi(xv) : bf_lo(xv);
        asm volatile("" : "+v"(xsel));
        v4u v[4];
        pollNx4<4, 8 * 128>(ws, tag, WS_RP + (rho * RP_C + (lane >> 3)) * 128 + (lane & 7) * 16, v);
        const float a0 = (bf_lo(v[0].x) + bf_lo(v[1].x)) + (bf_lo(v[2].x) + bf_lo(v[3].x)), a1 = (bf_hi(v[0].x) + bf_hi(v[1].x)) + (bf_hi(v[2].x) + bf_hi(v[3].x));
        const float a2 = (bf_lo(v[0].z) + bf_lo(v[1].z)) + (bf_lo(v[2].z) + bf_lo(v[3].z)), a3 = (bf_hi(v[0].z) + bf_hi(v[1].z)) + (bf_hi(v[2].z) + bf_hi(v[3].z));
        auto s01 = __builtin_amdgcn_permlane16_swap(asu(a0), asu(a1), false, false);
        auto s23 = __builtin_amdgcn_permlane16_swap(asu(a2), asu(a3), false, false);
        auto sc = __builtin_amdgcn_permlane32_swap(asu(asf(s01[0]) + asf(s01[1])), asu(asf(s23[0]) + asf(s23[1])), false, false);
        float e = asf(sc[0]) + asf(sc[1]);
        e += dpp<0x128>(e);
        const unsigned hb = pack_bf(xsel + rbf(e), 0.f) & 0xffffu;
        auto hp = __builtin_amdgcn_permlane16_swap(hb, hb, false, false);
        const unsigned hq = (unsigned)__builtin_amdgcn_ds_bpermute((((lane >> 1) & 7) + 32 * (lane & 1)) * 4, (int)(hp[0] | (hp[1] << 16)));
        if (lane < 16) __builtin_amdgcn_raw_buffer_store_b64(v2u{hq, tag}, rs, WS_GH + rho * 128 + lane * 8, 0, AUX_SC1);
      } else if (w == 1) {
        final_cell(ws, tag, 16 * j + r, lane, out, (unsigned*)(ws + WS_EPOCH));
      }
    } else if (is_mlp) {
      const int fg = 8 * j + ((r - 16) >> 1), nh = r & 1;
      const int kq = w >> 1, cth = w & 1;
      BAR();
      v4u gf[4], uf[4], df[2];
      {
        const lds_p p = L + L_GT + (128 * kq + 8 * g + (n16 >> 2)) * 64 + (16 * cth + 4 * (lane & 3)) * 2;
#pragma unroll
        for (int t = 0; t < 4; ++t) { gf[t] = tr_frag(p + t * 32 * 64, 64); uf[t] = tr_frag(p + 32768 + t * 32 * 64, 64); }
        const lds_p pd = L + L_DN + (8 * g + (n16 >> 2)) * 512 + (32 * w + 4 * (lane & 3)) * 2;
#pragma unroll
        for (int t = 0; t < 2; ++t) df[t] = tr_frag(pd + t * 32, 512);
      }
      const bool up = lane & 8;
      unsigned r3w = lds0 + L_R3 + ((((kq * 2 + (up ? 1 : 0)) * 2 + cth) * 8 + (lane & 7)) * 16 + 4 * g) * 4;
      {
        const v4u ones = {0x3f803f80u, 0x3f803f80u, 0x3f803f80u, 0x3f803f80u};
        f32x4 cg = {0.f, 0.f, 0.f, 0.f}, cu = {0.f, 0.f, 0.f, 0.f};
#pragma unroll
        for (int t = 0; t < 4; ++t) { cg = mfma(gf[t], ones, cg); cu = mfma(uf[t], ones, cu); }
        *(LAS f32x4*)(size_t)r3w = f32x4{up ? cu[0] : cg[0], up ? cu[1] : cg[1], up ? cu[2] : cg[2], up ? cu[3] : cg[3]};
        BAR();
        if (w == 0) {
          const LAS float* r3 = (const LAS float*)(L + L_R3) + (lane >> 5) * 256 + ((lane >> 4) & 1) * 128 + (lane & 15);
          ((LAS float*)(L + L_CS))[lane] = (r3[0] + r3[512]) + (r3[1024] + r3[1536]);
        }
      }
      unsigned goff = WS_GH + 16 * w * 128 + lane * 16;
      unsigned hnw = lds0 + L_HN + (lane & 7) * 1056 + (16 * w + (lane >> 3)) * 8, hrow = lds0 + L_HN + w * 1056 + lane * 16, hnr = lds0 + L_HN + (lane & 7) * 1056 + (128 * kq + 8 * g) * 2;
      unsigned r3r = lds0 + L_R3 + (((lane >> 4) & 1) * 8 + 2 * (w & 3) + (lane >> 5)) * 64 + (lane & 15) * 4;
      unsigned hhw = lds0 + L_HH + (2 * (w & 3) + (lane >> 5)) * 96 + (lane & 31) * 2, hhr = lds0 + L_HH + (lane & 7) * 96 + g * 16;
      int foff = WS_FP + ((64 * nh + 8 * w + (up ? 4 : 0) + g) * FP_C + fg) * 128 + (lane & 7) * 16;
      asm volatile("" : "+v"(goff), "+v"(hnw), "+v"(hrow), "+v"(hnr), "+v"(r3w), "+v"(r3r), "+v"(hhw), "+v"(hhr), "+v"(foff));
      asm volatile("" : "+v"(gf[0]), "+v"(gf[1]), "+v"(gf[2]), "+v"(gf[3]), "+v"(uf[0]), "+v"(uf[1]), "+v"(uf[2]), "+v"(uf[3]));
      asm volatile("" : "+v"(df[0]), "+v"(df[1]));
      v4u h0, h1;
      poll2x4(ws, tag, goff, goff + 1024, h0, h1);
      *(LAS v2u*)(size_t)hnw = v2u{h0.x, h0.z};
      *(LAS v2u*)(size_t)(hnw + 64) = v2u{h1.x, h1.z};
      BAR();
      {
        const lds_p pa = (lds_p)(size_t)hnr;
        f32x4 ag = {0.f, 0.f, 0.f, 0.f}, au = {0.f, 0.f, 0.f, 0.f};
#pragma unroll
        for (int t = 0; t < 4; ++t) {
          const v4u a = lds_ld128(pa + t * 64);
          ag = mfma(gf[t], a, ag);
          au = mfma(uf[t], a, au);
        }
        {
          const v4u hr = lds_ld128((lds_p)(size_t)hrow);
          const float f[8] = {bf_lo(hr.x), bf_hi(hr.x), bf_lo(hr.y), bf_hi(hr.y), bf_lo(hr.z), bf_hi(hr.z), bf_lo(hr.w), bf_hi(hr.w)};
          const float c0 = asf(__builtin_amdgcn_readfirstlane(asu(f[0])));
          float s1 = 0.f, s2 = 0.f;
#pragma unroll
          for (int i = 0; i < 8; ++i) { const float d = f[i] - c0; s1 += d; s2 = fmaf(d, d, s2); }
          s1 += dpp<0xB1>(s1); s2 += dpp<0xB1>(s2);
          s1 += dpp<0x4E>(s1); s2 += dpp<0x4E>(s2);
          s1 += dpp<0x141>(s1); s2 += dpp<0x141>(s2);
          s1 += dpp<0x140>(s1); s2 += dpp<0x140>(s2);
          auto sw = __builtin_amdgcn_permlane16_swap(asu(s1), asu(s2), false, false);
          const float t = x32_sum(asf(sw[0]) + asf(sw[1])) * (1.0f / D);
          const float m1 = asf(__builtin_amdgcn_readlane(asu(t), 0)), m2 = asf(__builtin_amdgcn_readlane(asu(t), 16));
          const float var = rbf(fmaxf(fmaf(-m1, m1, m2), 0.f));
          const float den = rbf(__builtin_amdgcn_sqrtf(rbf(var + 1e-5f)));
          if (lane == 0) *(LAS v2u*)(L + L_ST + w * 8) = v2u{asu(rbf(c0 + m1)), asu(__builtin_amdgcn_rcpf(den))};
        }
        *(LAS f32x4*)(size_t)r3w = f32x4{up ? au[0] : ag[0], up ? au[1] : ag[1], up ? au[2] : ag[2], up ? au[3] : ag[3]};
      }
      BAR();
      if (w < 4) {
        const int bb = 2 * w + (lane >> 5), n32 = lane & 31;
        const LAS float* r3 = (const LAS float*)(size_t)r3r;
        const v2u stt = *(const LAS v2u*)(L + L_ST + bb * 8);
        const float csg = ((const LAS float*)(L + L_CS))[n32], csu = ((const LAS float*)(L + L_CS))[32 + n32];
        float gg = (r3[0] + r3[512]) + (r3[1024] + r3[1536]);
        float uu = (r3[256] + r3[768]) + (r3[1280] + r3[1792]);
        gg = rbf(fmaf(-asf(stt.x), csg, gg) * asf(stt.y));
        uu = rbf(fmaf(-asf(stt.x), csu, uu) * asf(stt.y));
        const float sg = rbf(__builtin_amdgcn_rcpf(1.0f + __builtin_amdgcn_exp2f(-1.4426950408889634f * gg)));
        *(LAS unsigned short*)(size_t)hhw = (unsigned short)pack_bf(sg * uu, 0.f);
      }
      BAR();
      {
        const v4u a = lds_ld128((lds_p)(size_t)hhr);
        const f32x4 z = {0.f, 0.f, 0.f, 0.f};
        const f32x4 d0 = mfma(df[0], a, z), d1 = mfma(df[1], a, z);
        __builtin_amdgcn_raw_buffer_store_b128(v4u{pack_bf(up ? d1[0] : d0[0], up ? d1[1] : d0[1]), tag, pack_bf(up ? d1[2] : d0[2], up ? d1[3] : d0[3]), tag}, rs, foff, 0, AUX_SC1);
      }
    }
  } else {
    const int hw = wv, kq = kq0;
    BAR();
    asm volatile("s_waitcnt vmcnt(6)" : "+v"(xa), "+v"(xb));
    {
      float fa[8] = {bf_lo(xa.x), bf_hi(xa.x), bf_lo(xa.y), bf_hi(xa.y), bf_lo(xa.z), bf_hi(xa.z), bf_lo(xa.w), bf_hi(xa.w)};
      float fb[8] = {bf_lo(xb.x), bf_hi(xb.x), bf_lo(xb.y), bf_hi(xb.y), bf_lo(xb.z), bf_hi(xb.z), bf_lo(xb.w), bf_hi(xb.w)};
      layernorm8(fa);
      layernorm8(fb);
      if (g == kq) {
        *(LAS v4u*)(L + L_HN + (2 * hw) * 288 + n16 * 16) = v4u{pack_bf(fa[0], fa[1]), pack_bf(fa[2], fa[3]), pack_bf(fa[4], fa[5]), pack_bf(fa[6], fa[7])};
        *(LAS v4u*)(L + L_HN + (2 * hw + 1) * 288 + n16 * 16) = v4u{pack_bf(fb[0], fb[1]), pack_bf(fb[2], fb[3]), pack_bf(fb[4], fb[5]), pack_bf(fb[6], fb[7])};
      }
    }
    asm volatile("s_waitcnt vmcnt(0)" : "+v"(ep), "+v"(pf) :: "memory");
    const unsigned tag = ep + 1;
    lds_arrive(flags + 0, lane);
    lds_wait(flags + 0, 4);
    {
      const lds_p pa = L + L_HN + (lane & 7) * 288 + g * 16;
      const lds_p pb = L + L_WQ + (8 * g + (n16 >> 2)) * 128 + (16 * hw + 4 * (lane & 3)) * 2;
      f32x4 acc0 = mfma(tr_frag(pb, 128), lds_ld128(pa), f32x4{0.f, 0.f, 0.f, 0.f});
      f32x4 acc1 = mfma(tr_frag(pb + 32 * 128, 128), lds_ld128(pa + 64), f32x4{0.f, 0.f, 0.f, 0.f});
      acc0 = mfma(tr_frag(pb + 64 * 128, 128), lds_ld128(pa + 128), acc0);
      acc1 = mfma(tr_frag(pb + 96 * 128, 128), lds_ld128(pa + 192), acc1);
      publish_tile(acc0 + acc1, rs, WS_QP + (kq * QP_K + 32 * j + 4 * ct8 + hw) * 128, QP_B * 128, tag, lane);
    }
    {
      const u64 t_pub = __builtin_amdgcn_s_memrealtime();
      for (;;) {
        const u64 now = __builtin_amdgcn_s_memrealtime();
        if ((unsigned)(now - t_start) >= QDELAY && (unsigned)(now - t_pub) >= QAFTER) break;
        __builtin_amdgcn_s_sleep(1);
      }
    }
    {
      const int off = WS_QP + (b * QP_B + 32 * j + 8 * hw) * 128 + lane * 16;
      v4u v[4];
      pollNx4<4, QP_K * 128>(ws, tag, off, v);
      const float q0 = (asf(v[0].x) + asf(v[1].x)) + (asf(v[2].x) + asf(v[3].x)), q1 = (asf(v[0].z) + asf(v[1].z)) + (asf(v[2].z) + asf(v[3].z));
      *(LAS unsigned*)(L + L_Q + hw * 320 + lane * 4) = pack_bf(q0, q1);
      if (hw == 0 && lane == 0) flags[1] = tag;
    }
    lds_arrive(flags + 6, lane);
    __builtin_amdgcn_s_setprio(0);
    if (hw == 0) {
      while (flags[2] < 8u) __builtin_amdgcn_s_sleep(1);
      asm volatile("" ::: "memory");
      const float mw = ((const LAS float*)(L + L_MAX))[lane & 31], lw = ((const LAS float*)(L + L_SUM))[lane & 31];
      float mx = max2(mw, dpp<0x124>(mw));
      mx = max2(mx, dpp<0x128>(mx));
      mx = x16_max(mx);
      const float f = __builtin_amdgcn_exp2f((mw - mx) * 1.4426950408889634f);
      float l = f * lw;
      l += dpp<0x124>(l);
      l += dpp<0x128>(l);
      l = x16_sum(l);
      if (lane < 32) ((LAS float*)(L + L_MSC))[(lane & 3) * 8 + (lane >> 2)] = f * __builtin_amdgcn_rcpf(l);
      lds_arrive(flags + 4, lane);
      if (lane < 4) {
        const float lse = mx * 1.4426950408889634f + __builtin_amdgcn_logf(l);
        __builtin_amdgcn_raw_buffer_store_b64(v2u{asu(lse), tag}, rs, WS_LS + (b * HKV + j) * 128 + (lane * 4 + s) * 8, 0, AUX_SC1);
      }
    }
    if (is_wo) {
      const int h = 4 * j + (r >> 2), ns = r & 3;
      while (flags[4] < DMAFLAG) __builtin_amdgcn_s_sleep(2);
      const unsigned char* src = (const unsigned char*)wo + ((size_t)(128 * h + 32 * hw + g) * D + 128 * ns) * 2 + n16 * 16;
#pragma unroll
      for (int i = 0; i < 8; ++i) glds16(src + (size_t)i * 4 * D * 2, lds0 + L_W + (32 * hw + 4 * i) * 256);
      asm volatile("s_waitcnt vmcnt(0)" ::: "memory");
      BAR();
    } else if (is_mlp) {
      while (flags[4] < DMAFLAG) __builtin_amdgcn_s_sleep(2);
      const int fg = 8 * j + ((r - 16) >> 1), nh = r & 1;
      const size_t go = ((size_t)(128 * hw + (lane >> 2)) * FF + 32 * fg) * 2 + (lane & 3) * 16;
#pragma unroll
      for (int i = 0; i < 8; ++i) {
        if (i >= 2) asm volatile("s_waitcnt vmcnt(3)" ::: "memory");
        glds16((const unsigned char*)wg + go + (size_t)i * 16 * FF * 2, lds0 + L_GT + (128 * hw + 16 * i) * 64);
        if (i >= 2) asm volatile("s_waitcnt vmcnt(3)" ::: "memory");
        glds16((const unsigned char*)wu + go + (size_t)i * 16 * FF * 2, lds0 + L_UP + (128 * hw + 16 * i) * 64);
      }
      const unsigned char* sd = (const unsigned char*)wd + ((size_t)(32 * fg + 8 * hw + (lane >> 5)) * D + 256 * nh) * 2 + (lane & 31) * 16;
#pragma unroll
      for (int i = 0; i < 4; ++i) {
        asm volatile("s_waitcnt vmcnt(5)" ::: "memory");
        glds16(sd + (size_t)i * 2 * D * 2, lds0 + L_DN + (8 * hw + 2 * i) * 512);
      }
      asm volatile("s_waitcnt vmcnt(0)" ::: "memory");
      BAR();
    }
  }
}

}

extern "C" void kernel_launch(void* const* d_in, const int* in_sizes, int n_in, void* d_out, int out_size, void* d_ws,
                              size_t ws_size, hipStream_t stream) {
  (void)in_sizes; (void)n_in; (void)out_size; (void)ws_size;
  (void)hipFuncSetAttribute((const void*)fused_decode, hipFuncAttributeMaxDynamicSharedMemorySize, LDS_BYTES);
  fused_decode<<<256, NTHR, LDS_BYTES, stream>>>(
      (const unsigned short*)d_in[0], (const unsigned short*)d_in[1], (const unsigned short*)d_in[2], (const unsigned short*)d_in[3],
      (const unsigned short*)d_in[4], (const unsigned short*)d_in[6], (const unsigned short*)d_in[7], (const unsigned short*)d_in[8],
      (unsigned short*)d_out, (unsigned char*)d_ws);
}
```

```cpp
#include <hip/hip_runtime.h>
#include <hip/hip_bf16.h>
#include <stdint.h>

namespace {

typedef unsigned v4u __attribute__((ext_vector_type(4)));
typedef unsigned v2u __attribute__((ext_vector_type(2)));
typedef float f32x4 __attribute__((ext_vector_type(4)));
typedef __bf16 bf16x8 __attribute__((ext_vector_type(8)));
typedef unsigned long long u64;
#define LAS __attribute__((address_space(3)))
typedef LAS unsigned char* lds_p;

constexpr int D = 512, HQ = 32, HKV = 8, SK = 4096, DH = 128, WIN = 1024, FF = 1792;
constexpr int NTHR = 768;
constexpr int AUX_SC1 = 16;
constexpr unsigned SPIN_CAP = 1u << 16;
constexpr int PD = 3;
constexpr int THRN = 10;
constexpr unsigned DMAFLAG = 2;
constexpr unsigned QDELAY = 290;
constexpr unsigned QAFTER = 90;
constexpr int CODE_LINES = 119;

constexpr int WS_EPOCH = 0;
constexpr int PAD = 1;
constexpr int QP_B = 256 + PAD, QP_K = 8 * QP_B;
constexpr int RP_C = 32 + PAD;
constexpr int FP_C = 56 + PAD;
constexpr int WS_QP = 1 << 16;
constexpr int WS_OP = WS_QP + 4 * QP_K * 128;
constexpr int WS_LS = WS_OP + (1 << 19);
constexpr int WS_RP = WS_LS + (1 << 15);
constexpr int WS_GH = WS_RP + 128 * RP_C * 128;
constexpr int WS_FP = WS_GH + (1 << 14);
constexpr int WS_END = WS_FP + 128 * FP_C * 128;

constexpr int L_RED = 0;
constexpr int L_WQ = 0;
constexpr int L_Q = 16384;
constexpr int L_MAX = L_Q + 4 * 320;
constexpr int L_SUM = L_MAX + 128;
constexpr int L_FLAG = L_SUM + 128;
constexpr int L_MSC = L_FLAG + 64;
constexpr int L_CS = L_MSC + 128;
constexpr int L_ST = L_CS + 256;
constexpr int L_OS = L_ST + 64;
constexpr int L_HN = L_OS + 8 * 288;
constexpr int L_R3 = L_HN + 8 * 1056;
constexpr int L_HH = L_R3 + 8192;
constexpr int L_W = ((L_HH + 8 * 96 + 255) / 256) * 256;
constexpr int L_GT = L_W, L_UP = L_W + 32768, L_DN = L_W + 65536;
constexpr int LDS_BYTES = L_W + 98304;
static_assert(LDS_BYTES <= 160 * 1024 && LDS_BYTES > 80 * 1024, "one workgroup per CU");

__device__ __forceinline__ float bf_lo(unsigned u) { return __builtin_bit_cast(float, u << 16); }
__device__ __forceinline__ float bf_hi(unsigned u) { return __builtin_bit_cast(float, u & 0xffff0000u); }
__device__ __forceinline__ unsigned pack_bf(float lo, float hi) {
  typedef __bf16 bf2 __attribute__((ext_vector_type(2)));
  bf2 r = {(__bf16)lo, (__bf16)hi};
  return __builtin_bit_cast(unsigned, r);
}
__device__ __forceinline__ float rbf(float f) { return bf_lo(pack_bf(f, f)); }
__device__ __forceinline__ float asf(unsigned u) { return __builtin_bit_cast(float, u); }
__device__ __forceinline__ unsigned asu(float f) { return __builtin_bit_cast(unsigned, f); }
template <int CTRL> __device__ __forceinline__ float dpp(float x) {
  return __builtin_bit_cast(float, __builtin_amdgcn_mov_dpp(__builtin_bit_cast(int, x), CTRL, 0xf, 0xf, true));
}
__device__ __forceinline__ float max3(float a, float b, float c) {
  float r;
  asm("v_max3_f32 %0, %1, %2, %3" : "=v"(r) : "v"(a), "v"(b), "v"(c));
  return r;
}
__device__ __forceinline__ float max2(float a, float b) {
  float r;
  asm("v_max_f32 %0, %1, %2" : "=v"(r) : "v"(a), "v"(b));
  return r;
}
__device__ __forceinline__ float x16_sum(float x) {
  auto s = __builtin_amdgcn_permlane16_swap(asu(x), asu(x), false, false);
  return asf(s[0]) + asf(s[1]);
}
__device__ __forceinline__ float x32_sum(float x) {
  auto s = __builtin_amdgcn_permlane32_swap(asu(x), asu(x), false, false);
  return asf(s[0]) + asf(s[1]);
}
__device__ __forceinline__ float x16_max(float x) {
  auto s = __builtin_amdgcn_permlane16_swap(asu(x), asu(x), false, false);
  return max2(asf(s[0]), asf(s[1]));
}
__device__ __forceinline__ float x32_max(float x) {
  auto s = __builtin_amdgcn_permlane32_swap(asu(x), asu(x), false, false);
  return max2(asf(s[0]), asf(s[1]));
}
__device__ __forceinline__ f32x4 mfma(v4u a, v4u b, f32x4 c) {
  return __builtin_amdgcn_mfma_f32_16x16x32_bf16(__builtin_bit_cast(bf16x8, a), __builtin_bit_cast(bf16x8, b), c, 0, 0, 0);
}
__device__ __forceinline__ v2u tr_read(lds_p p) {
  typedef short v4s __attribute__((ext_vector_type(4)));
  return __builtin_bit_cast(v2u, __builtin_amdgcn_ds_read_tr16_b64_v4i16((LAS v4s*)p));
}
__device__ __forceinline__ v4u tr_frag(lds_p p, int row_stride) {
  const v2u a = tr_read(p), b = tr_read(p + 4 * row_stride);
  return v4u{a.x, a.y, b.x, b.y};
}
__device__ __forceinline__ v4u lds_ld128(lds_p p) { return *(const LAS v4u*)p; }
__device__ __forceinline__ void glds16(const void* gsrc, unsigned lds_dst) {
  unsigned keep;
  asm volatile("s_mov_b32 %0, m0\n\ts_mov_b32 m0, %2\n\ts_nop 0\n\tglobal_load_lds_dwordx4 %1, off nt\n\ts_mov_b32 m0, %0"
               : "=&s"(keep) : "v"(gsrc), "s"(lds_dst) : "memory");
}
#define BAR() do { asm volatile("s_waitcnt lgkmcnt(0)" ::: "memory"); __builtin_amdgcn_s_barrier(); asm volatile("" ::: "memory"); } while (0)
__device__ __forceinline__ void lds_arrive(volatile LAS unsigned* p, int lane) {
  asm volatile("" ::: "memory");
  if (lane == 0) asm volatile("ds_add_u32 %0, %1" :: "v"((unsigned)(size_t)p), "v"(1u) : "memory");
  asm volatile("" ::: "memory");
}
__device__ __forceinline__ void lds_wait(volatile LAS unsigned* p, unsigned n) {
  while (*p < n) { }
  asm volatile("" ::: "memory");
}
typedef unsigned long long mask_t;
__device__ __forceinline__ mask_t bad4(v4u v, unsigned tag) { return __builtin_amdgcn_ballot_w64(v.y != tag) | __builtin_amdgcn_ballot_w64(v.w != tag); }
__device__ __forceinline__ mask_t bad2(v2u v, unsigned tag) { return __builtin_amdgcn_ballot_w64(v.y != tag); }
#define PL4(n_, o_, m_) "s_mov_b64 exec, %" #m_ "\n\tglobal_load_dwordx4 %" #n_ ", %" #o_ ", %[base] sc1\n\t"
#define PL2(n_, o_, m_) "s_mov_b64 exec, %" #m_ "\n\tglobal_load_dwordx2 %" #n_ ", %" #o_ ", %[base] sc1\n\t"
#define PLEND "s_mov_b64 exec, -1\n\ts_waitcnt vmcnt(0)"
__device__ __forceinline__ void poll1x4(const unsigned char* ws, unsigned tag, unsigned o0, v4u& a) {
  a = v4u{0u, ~tag, 0u, ~tag};
  for (unsigned spins = 0; spins <= SPIN_CAP; ++spins) {
    const mask_t m0 = bad4(a, tag);
    if (m0 == 0) break;
    asm volatile(PL4(0, 1, 2) PLEND : "+v"(a) : "v"(o0), "s"(m0), [base] "s"(ws) : "memory");
    __builtin_amdgcn_s_sleep(2);
  }
}
__device__ __forceinline__ void poll2x4(const unsigned char* ws, unsigned tag, unsigned o0, unsigned o1, v4u& a, v4u& b) {
  a = b = v4u{0u, ~tag, 0u, ~tag};
  for (unsigned spins = 0; spins <= SPIN_CAP; ++spins) {
    const mask_t m0 = bad4(a, tag), m1 = bad4(b, tag);
    if ((m0 | m1) == 0) break;
    asm volatile(PL4(0, 2, 4) PL4(1, 3, 5) PLEND : "+v"(a), "+v"(b) : "v"(o0), "v"(o1), "s"(m0), "s"(m1), [base] "s"(ws) : "memory");
  }
}
__device__ __forceinline__ void poll5x2(const unsigned char* ws, unsigned tag, unsigned oa, unsigned ob, v2u (&v)[5]) {
  unsigned o[4];
#pragma unroll
  for (int i = 0; i < 4; ++i) o[i] = oa + i * 512;
#pragma unroll
  for (int i = 0; i < 5; ++i) v[i] = v2u{0u, ~tag};
  for (unsigned spins = 0; spins <= SPIN_CAP; ++spins) {
    mask_t m[5], any = 0;
#pragma unroll
    for (int i = 0; i < 5; ++i) { m[i] = bad2(v[i], tag); any |= m[i]; }
    if (any == 0) break;
    asm volatile(PL2(0, 5, 10) PL2(1, 6, 11) PL2(2, 7, 12) PL2(3, 8, 13) PL2(4, 9, 14) PLEND
                 : "+v"(v[0]), "+v"(v[1]), "+v"(v[2]), "+v"(v[3]), "+v"(v[4])
                 : "v"(o[0]), "v"(o[1]), "v"(o[2]), "v"(o[3]), "v"(ob),
                   "s"(m[0]), "s"(m[1]), "s"(m[2]), "s"(m[3]), "s"(m[4]), [base] "s"(ws) : "memory");
  }
}
template <int N, int STEP>
__device__ __forceinline__ void pollNx4(const unsigned char* ws, unsigned tag, unsigned o0, v4u (&v)[N]) {
  static_assert(N == 4 || N == 7, "");
  unsigned o[N];
#pragma unroll
  for (int i = 0; i < N; ++i) { v[i] = v4u{0u, ~tag, 0u, ~tag}; o[i] = o0 + i * STEP; }
  for (unsigned spins = 0; spins <= SPIN_CAP; ++spins) {
    mask_t m[N], any = 0;
#pragma unroll
    for (int i = 0; i < N; ++i) { m[i] = bad4(v[i], tag); any |= m[i]; }
    if (any == 0) break;
    if constexpr (N == 4) {
      asm volatile(PL4(0, 4, 8) PL4(1, 5, 9) PL4(2, 6, 10) PL4(3, 7, 11) PLEND
                   : "+v"(v[0]), "+v"(v[1]), "+v"(v[2]), "+v"(v[3])
                   : "v"(o[0]), "v"(o[1]), "v"(o[2]), "v"(o[3]), "s"(m[0]), "s"(m[1]), "s"(m[2]), "s"(m[3]), [base] "s"(ws) : "memory");
    } else {
      asm volatile(PL4(0, 7, 14) PL4(1, 8, 15) PL4(2, 9, 16) PL4(3, 10, 17) PL4(4, 11, 18) PL4(5, 12, 19) PL4(6, 13, 20) PLEND
                   : "+v"(v[0]), "+v"(v[1]), "+v"(v[2]), "+v"(v[3]), "+v"(v[4]), "+v"(v[5]), "+v"(v[6])
                   : "v"(o[0]), "v"(o[1]), "v"(o[2]), "v"(o[3]), "v"(o[4]), "v"(o[5]), "v"(o[6]),
                     "s"(m[0]), "s"(m[1]), "s"(m[2]), "s"(m[3]), "s"(m[4]), "s"(m[5]), "s"(m[6]), [base] "s"(ws) : "memory");
    }
  }
}

__device__ __forceinline__ void layernorm8(float (&f)[8]) {
  const float c0 = asf(__builtin_amdgcn_readfirstlane(asu(f[0])));
  float s1 = 0.f, s2 = 0.f;
#pragma unroll
  for (int i = 0; i < 8; ++i) { const float d = f[i] - c0; s1 += d; s2 = fmaf(d, d, s2); }
  s1 += dpp<0xB1>(s1); s2 += dpp<0xB1>(s2);
  s1 += dpp<0x4E>(s1); s2 += dpp<0x4E>(s2);
  s1 += dpp<0x141>(s1); s2 += dpp<0x141>(s2);
  s1 += dpp<0x140>(s1); s2 += dpp<0x140>(s2);
  s1 = x16_sum(s1); s2 = x16_sum(s2);
  s1 = x32_sum(s1) * (1.0f / D); s2 = x32_sum(s2) * (1.0f / D);
  const float var = rbf(fmaxf(fmaf(-s1, s1, s2), 0.f));
  const float den = rbf(__builtin_amdgcn_sqrtf(rbf(var + 1e-5f)));
  const float inv = __builtin_amdgcn_rcpf(den), mb = rbf(c0 + s1);
#pragma unroll
  for (int i = 0; i < 8; ++i) f[i] = rbf(f[i] - mb) * inv;
}

__device__ __forceinline__ void publish_tile(f32x4 d, __amdgpu_buffer_rsrc_t rs, int base, int stride_b, unsigned tag, int lane) {
  const bool t = lane & 8;
  const int src = ((lane >> 3) + 8 * (lane & 1) + 16 * ((lane >> 1) & 3)) * 4;
  const unsigned v0 = (unsigned)__builtin_amdgcn_ds_bpermute(src, (int)asu(t ? d[2] : d[0]));
  const unsigned v1 = (unsigned)__builtin_amdgcn_ds_bpermute(src, (int)asu(t ? d[3] : d[1]));
  __builtin_amdgcn_raw_buffer_store_b128(v4u{v0, tag, v1, tag}, rs, base + (lane >> 3) * stride_b + (lane & 7) * 16, 0, AUX_SC1);
}

__device__ __forceinline__ void final_cell(const unsigned char* ws, unsigned tag, int cell, int lane, unsigned short* out, unsigned* ws_epoch) {
  const int p = lane & 7, row = lane >> 4;
  v4u hv;
  poll1x4(ws, tag, WS_GH + cell * 128 + p * 16, hv);
  float hsel = row == 0 ? bf_lo(hv.x) : row == 1 ? bf_hi(hv.x) : row == 2 ? bf_lo(hv.z) : bf_hi(hv.z);
  unsigned oo = (p * D + 4 * cell + (row & 2)) * 2;
  asm volatile("" : "+v"(hsel), "+v"(oo));
  v4u v[7];
  pollNx4<7, 8 * 128>(ws, tag, WS_FP + (cell * FP_C + (lane >> 3)) * 128 + p * 16, v);
  float a0 = 0.f, a1 = 0.f, a2 = 0.f, a3 = 0.f;
#pragma unroll
  for (int i = 0; i < 7; ++i) { a0 += bf_lo(v[i].x); a1 += bf_hi(v[i].x); a2 += bf_lo(v[i].z); a3 += bf_hi(v[i].z); }
  auto s01 = __builtin_amdgcn_permlane16_swap(asu(a0), asu(a1), false, false);
  auto s23 = __builtin_amdgcn_permlane16_swap(asu(a2), asu(a3), false, false);
  const float c0 = asf(s01[0]) + asf(s01[1]), c1 = asf(s23[0]) + asf(s23[1]);
  auto sc = __builtin_amdgcn_permlane32_swap(asu(c0), asu(c1), false, false);
  float e = asf(sc[0]) + asf(sc[1]);
  e += dpp<0x128>(e);
  const unsigned ob = pack_bf(hsel + rbf(e), 0.f) & 0xffffu;
  auto op = __builtin_amdgcn_permlane16_swap(ob, ob, false, false);
  if ((lane & 24) == 0) *(__attribute__((address_space(1))) unsigned*)((unsigned char*)out + oo) = op[0] | (op[1] << 16);
  if (cell == 0 && lane == 0) __hip_atomic_store(ws_epoch, tag, __ATOMIC_RELAXED, __HIP_MEMORY_SCOPE_AGENT);
}

__global__ void __launch_bounds__(NTHR, 3)
fused_decode(const unsigned short* __restrict__ x, const unsigned short* __restrict__ kc, const unsigned short* __restrict__ vc,
             const unsigned short* __restrict__ wq, const unsigned short* __restrict__ wo, const unsigned short* __restrict__ wg,
             const unsigned short* __restrict__ wu, const unsigned short* __restrict__ wd, unsigned short* __restrict__ out,
             unsigned char* __restrict__ ws) {
  unsigned long long code_base;
  asm volatile("s_getpc_b64 %0" : "=s"(code_base));
  code_base &= ~0xffull;
  extern __shared__ __attribute__((aligned(16))) unsigned char smem[];
  const lds_p L = (lds_p)smem;
  const unsigned lds0 = (unsigned)(size_t)L;
  const int tid = threadIdx.x, lane = tid & 63;
  const int wv = __builtin_amdgcn_readfirstlane(tid >> 6);
  const int w = wv - 4;
  const int c = blockIdx.x, j = c & 7, r = c >> 3;
  const int b = r >> 2, s = r & 3;
  const int g = lane >> 4, n16 = lane & 15;
  const bool is_wo = r < 16, is_mlp = (r >= 16) && (j < 7);
  const __amdgpu_buffer_rsrc_t rs = __builtin_amdgcn_make_buffer_rsrc((void*)ws, 0, WS_END, 0x00020000);
  volatile LAS unsigned* flags = (volatile LAS unsigned*)(L + L_FLAG);

  const u64 t_start = __builtin_amdgcn_s_memrealtime();
  const int kq0 = r >> 3, ct8 = r & 7;
  v4u xa, xb; unsigned ep, pf;
  asm volatile("" : "=v"(xa), "=v"(xb), "=v"(ep), "=v"(pf));
  if (wv < 4) {
    const int hw = wv;
    __builtin_amdgcn_s_setprio(2);
    const unsigned zero = 0;
    const unsigned xo = (2 * hw) * (D * 2) + lane * 16;
    asm volatile("global_load_dwordx4 %0, %1, %2" : "+v"(xa) : "v"(xo), "s"(x));
    asm volatile("global_load_dwordx4 %0, %1, %2 offset:1024" : "+v"(xb) : "v"(xo), "s"(x));
    const unsigned char* src = (const unsigned char*)wq + ((size_t)(128 * kq0 + 32 * hw + (lane >> 3)) * (HQ * DH) + 512 * j + 64 * ct8) * 2 + (lane & 7) * 16;
#pragma unroll
    for (int i = 0; i < 4; ++i) glds16(src + (size_t)i * 8 * (HQ * DH) * 2, lds0 + L_WQ + (32 * hw + 8 * i) * 128);
    asm volatile("global_load_dword %0, %1, %2" : "+v"(ep) : "v"(zero), "s"(ws));
    const unsigned po = min((r * 4 + hw) * 2 + (lane & 1), CODE_LINES - 1) * 128;
    asm volatile("global_load_dword %0, %1, %2" : "+v"(pf) : "v"(po), "s"(code_base));
  }
  int wv2 = wv;
  asm volatile("" : "+s"(wv2));

  if (__builtin_expect(wv2 >= 4, 1)) {
    const size_t tile = ((size_t)(b * HKV + j) * SK + (SK - WIN) + 256 * s + 32 * w) * (DH * 2);
    const unsigned char* kb = (const unsigned char*)kc + tile + 4096;
    const unsigned char* vb = (const unsigned char*)vc + tile;
    const unsigned koff = (lane & 7) * 256 + (2 * g + ((lane >> 3) & 1)) * 16;
    const unsigned voff = (16 * (g >> 1) + 4 * (g & 1)) * 256 + n16 * 16;
    v4u K[8], V[8];
#define LDK(i, imm) asm volatile("global_load_dwordx4 %0, %1, %2 offset:" #imm " nt" : "=v"(K[i]) : "v"(koff), "s"(kb))
#define LDV(i, imm) asm volatile("global_load_dwordx4 %0, %1, %2 offset:" #imm " nt" : "=v"(V[i]) : "v"(voff), "s"(vb))
#define THR(n) do { if (n < THRN || __builtin_amdgcn_readfirstlane(flags[6]) < 4) asm volatile("s_waitcnt vmcnt(%0)" :: "i"(PD - 1)); } while (0)
    static_assert(PD == 3, "three loads go out in front of the barrier");
    LDK(0, -4096); LDK(1, -3968); LDK(2, -2048);
    if (w == 0 && lane < 8) flags[lane] = 0;
    BAR();
    THR(3); LDK(3, -1920);
    THR(4); LDK(4, 0); THR(5); LDK(5, 128); THR(6); LDK(6, 2048); THR(7); LDK(7, 2176);
    THR(8); LDV(0, 0); THR(9); LDV(1, 256); THR(10); LDV(2, 512); THR(11); LDV(3, 768);
    THR(12); LDV(4, 2048); THR(13); LDV(5, 2304); THR(14); LDV(6, 2560); THR(15); LDV(7, 2816);
    while (flags[6] < 4u) __builtin_amdgcn_s_sleep(1);
    asm volatile("" ::: "memory");
    const unsigned tag = flags[1];
    const lds_p qa = L + L_Q + (n16 & 3) * 320 + (2 * g + ((n16 >> 2) & 1)) * 16;
    const v4u q0 = lds_ld128(qa), q1 = lds_ld128(qa + 128);
    asm volatile("s_waitcnt vmcnt(8)" : "+v"(K[0]), "+v"(K[1]), "+v"(K[2]), "+v"(K[3]), "+v"(K[4]), "+v"(K[5]), "+v"(K[6]), "+v"(K[7]));
    f32x4 sa[4];
#pragma unroll
    for (int o = 0; o < 4; ++o) {
      sa[o] = mfma(K[2 * o], q0, f32x4{0.f, 0.f, 0.f, 0.f});
      sa[o] = mfma(K[2 * o + 1], q1, sa[o]);
    }
    float sc[8];
    {
      const bool par = lane & 4;
      float raw[8];
#pragma unroll
      for (int p = 0; p < 2; ++p)
#pragma unroll
        for (int i = 0; i < 4; ++i) {
          auto sw = __builtin_amdgcn_permlane32_swap(asu(sa[p][i]), asu(sa[p + 2][i]), false, false);
          const float a = par ? asf(sw[1]) : asf(sw[0]);
          raw[4 * p + i] = a + dpp<0x124>(a);
        }
#pragma unroll
      for (int i = 0; i < 8; i += 2) {
        const unsigned u = pack_bf(raw[i], raw[i + 1]);
        const unsigned t = pack_bf(bf_lo(u) * 0.08838834764831845f, bf_hi(u) * 0.08838834764831845f);
        sc[i] = bf_lo(t); sc[i + 1] = bf_hi(t);
      }
    }
    float m = max3(max3(sc[0], sc[1], sc[2]), max3(sc[3], sc[4], sc[5]), max2(sc[6], sc[7]));
    m = x32_max(x16_max(m));
    float pe[8], ls;
    {
      const float mc = m * 1.4426950408889634f;
#pragma unroll
      for (int i = 0; i < 8; ++i) pe[i] = __builtin_amdgcn_exp2f(fmaf(sc[i], 1.4426950408889634f, -mc));
      ls = ((pe[0] + pe[1]) + (pe[2] + pe[3])) + ((pe[4] + pe[5]) + (pe[6] + pe[7]));
    }
    v4u PA[4];
    {
      const v4u P = {pack_bf(pe[0], pe[1]), pack_bf(pe[2], pe[3]), pack_bf(pe[4], pe[5]), pack_bf(pe[6], pe[7])};
#pragma unroll
      for (int mq = 0; mq < 4; ++mq) {
        const bool on = (n16 >> 2) == mq;
        PA[mq] = v4u{on ? P.x : 0u, on ? P.y : 0u, on ? P.z : 0u, on ? P.w : 0u};
      }
    }
    ls = x32_sum(x16_sum(ls));
    if (lane < 4) {
      ((LAS float*)(L + L_MAX))[w * 4 + lane] = m;
      ((LAS float*)(L + L_SUM))[w * 4 + lane] = ls;
    }
    lds_arrive(flags + 2, lane);
    unsigned T[8][4];
#define XPOSE(n_)                                                                                         \
  _Pragma("unroll") for (int mm = 0; mm < 4; ++mm) {                                                      \
    T[2 * mm][n_] = __builtin_amdgcn_perm(V[2 * n_ + 1][mm], V[2 * n_][mm], 0x05040100u);                 \
    T[2 * mm + 1][n_] = __builtin_amdgcn_perm(V[2 * n_ + 1][mm], V[2 * n_][mm], 0x07060302u);             \
  }                                                                                                       \
  asm volatile("" : "+v"(T[0][n_]), "+v"(T[1][n_]), "+v"(T[2][n_]), "+v"(T[3][n_]), "+v"(T[4][n_]), "+v"(T[5][n_]), "+v"(T[6][n_]), "+v"(T[7][n_]));
    int ooff = WS_OP + ((b * HQ + 4 * j + (w >> 1)) * 4 + s) * 512 + (w & 1) * 256 + n16 * 16;
    const int dr = 64 * (w & 1) + 4 * n16 + g, dw = 8 * n16 + g;
    unsigned redr_a = lds0 + L_RED + ((w >> 1) * 128 + (dr ^ ((dr >> 5) << 1))) * 4;
    unsigned redw_a = lds0 + L_RED + w * 2048 + (dw ^ ((dw >> 5) << 1)) * 4;
    const int h = 4 * j + (r >> 2), ns = r & 3;
    unsigned doff = WS_OP + (w * HQ + h) * 4 * 512 + lane * 8;
    unsigned loff = WS_LS + (w * HKV + j) * 128 + ((h & 3) * 4 + (lane & 3)) * 8;
    int roff = WS_RP + ((32 * ns + 4 * w + g) * RP_C + h) * 128 + (lane & 7) * 16;
    unsigned osw = lds0 + L_OS + w * 288 + lane * 4, osr = lds0 + L_OS + (lane & 7) * 288 + g * 16;
    unsigned wfa = lds0 + L_W + (8 * g + (n16 >> 2)) * 256 + (16 * w + 4 * (lane & 3)) * 2;
    asm volatile("" : "+v"(PA[0]), "+v"(PA[1]), "+v"(PA[2]), "+v"(PA[3]), "+v"(ooff), "+v"(redr_a), "+v"(redw_a));
    asm volatile("" : "+v"(doff), "+v"(loff), "+v"(roff), "+v"(osw), "+v"(osr), "+v"(wfa));
    asm volatile("s_waitcnt vmcnt(6)" : "+v"(V[0]), "+v"(V[1])); XPOSE(0)
    asm volatile("s_waitcnt vmcnt(4)" : "+v"(V[2]), "+v"(V[3])); XPOSE(1)
    asm volatile("s_waitcnt vmcnt(2)" : "+v"(V[4]), "+v"(V[5])); XPOSE(2)
    asm volatile("s_waitcnt vmcnt(0)" : "+v"(V[6]), "+v"(V[7]));
    XPOSE(3)
    {
      f32x4 oa = {0.f, 0.f, 0.f, 0.f}, ob = {0.f, 0.f, 0.f, 0.f};
#pragma unroll
      for (int i = 0; i < 4; ++i) {
        oa = mfma(PA[i], v4u{T[i][0], T[i][1], T[i][2], T[i][3]}, oa);
        ob = mfma(PA[i], v4u{T[i + 4][0], T[i + 4][1], T[i + 4][2], T[i + 4][3]}, ob);
      }
      LAS float* red = (LAS float*)(size_t)redw_a;
#pragma unroll
      for (int hh = 0; hh < 4; ++hh) { red[hh * 128] = oa[hh]; *(LAS float*)(size_t)((redw_a ^ 16) + hh * 512) = ob[hh]; }
    }
    lds_arrive(flags + 4, lane);
    const LAS float* redr = (const LAS float*)(size_t)redr_a;
    lds_wait(flags + 4, 9);
    {
      const f32x4 ma = *(const LAS f32x4*)(L + L_MSC + (w >> 1) * 32), mb = *(const LAS f32x4*)(L + L_MSC + (w >> 1) * 32 + 16);
      float o = redr[0] * ma[0], o2 = redr[512] * ma[1];
      o = fmaf(redr[2 * 512], ma[2], o); o2 = fmaf(redr[3 * 512], ma[3], o2);
      o = fmaf(redr[4 * 512], mb[0], o); o2 = fmaf(redr[5 * 512], mb[1], o2);
      o = fmaf(redr[6 * 512], mb[2], o); o2 = fmaf(redr[7 * 512], mb[3], o2);
      o += o2;
      auto s16 = __builtin_amdgcn_permlane16_swap(asu(o), asu(o), false, false);
      const unsigned pr = pack_bf(asf(s16[0]), asf(s16[1]));
      auto s32 = __builtin_amdgcn_permlane32_swap(pr, pr, false, false);
      if (lane < 16) __builtin_amdgcn_raw_buffer_store_b128(v4u{s32[0], tag, s32[1], tag}, rs, ooff, 0, AUX_SC1);
    }

    if (is_wo) {
      BAR();
      v4u wf[4];
      {
        const lds_p p = (lds_p)(size_t)wfa;
#pragma unroll
        for (int t = 0; t < 4; ++t) wf[t] = tr_frag(p + t * 32 * 256, 256);
      }
      v2u dv[5];
      poll5x2(ws, tag, doff, loff, dv);
      {
        const float lse = asf(dv[4].x);
        float mx = max2(lse, dpp<0xB1>(lse));
        mx = max2(mx, dpp<0x4E>(mx));
        const float e = __builtin_amdgcn_exp2f(lse - mx);
        float es = e + dpp<0xB1>(e);
        es += dpp<0x4E>(es);
        const float wt = e * __builtin_amdgcn_rcpf(es);
        const float e0 = dpp<0x00>(wt), e1 = dpp<0x55>(wt), e2 = dpp<0xAA>(wt), e3 = dpp<0xFF>(wt);
        const float o0 = fmaf(e0, bf_lo(dv[0].x), e1 * bf_lo(dv[1].x)) + fmaf(e2, bf_lo(dv[2].x), e3 * bf_lo(dv[3].x));
        const float o1 = fmaf(e0, bf_hi(dv[0].x), e1 * bf_hi(dv[1].x)) + fmaf(e2, bf_hi(dv[2].x), e3 * bf_hi(dv[3].x));
        *(LAS unsigned*)(size_t)osw = pack_bf(o0, o1);
      }
      BAR();
      {
        const lds_p pa = (lds_p)(size_t)osr;
        f32x4 acc0 = mfma(wf[0], lds_ld128(pa), f32x4{0.f, 0.f, 0.f, 0.f});
        f32x4 acc1 = mfma(wf[1], lds_ld128(pa + 64), f32x4{0.f, 0.f, 0.f, 0.f});
        acc0 = mfma(wf[2], lds_ld128(pa + 128), acc0);
        acc1 = mfma(wf[3], lds_ld128(pa + 192), acc1);
        acc0 += acc1;
        if ((lane & 8) == 0) __builtin_amdgcn_raw_buffer_store_b128(v4u{pack_bf(acc0[0], acc0[1]), tag, pack_bf(acc0[2], acc0[3]), tag}, rs, roff, 0, AUX_SC1);
      }
      if (w == 0) {
        const int rho = 16 * j + r;
        const unsigned xv = *(const unsigned*)(x + (lane & 7) * D + 4 * rho + (g & 2));
        float xsel = (g & 1) ? bf_hi(xv) : bf_lo(xv);
        asm volatile("" : "+v"(xsel));
        v4u v[4];
        pollNx4<4, 8 * 128>(ws, tag, WS_RP + (rho * RP_C + (lane >> 3)) * 128 + (lane & 7) * 16, v);
        const float a0 = (bf_lo(v[0].x) + bf_lo(v[1].x)) + (bf_lo(v[2].x) + bf_lo(v[3].x)), a1 = (bf_hi(v[0].x) + bf_hi(v[1].x)) + (bf_hi(v[2].x) + bf_hi(v[3].x));
        const float a2 = (bf_lo(v[0].z) + bf_lo(v[1].z)) + (bf_lo(v[2].z) + bf_lo(v[3].z)), a3 = (bf_hi(v[0].z) + bf_hi(v[1].z)) + (bf_hi(v[2].z) + bf_hi(v[3].z));
        auto s01 = __builtin_amdgcn_permlane16_swap(asu(a0), asu(a1), false, false);
        auto s23 = __builtin_amdgcn_permlane16_swap(asu(a2), asu(a3), false, false);
        auto sc = __builtin_amdgcn_permlane32_swap(asu(asf(s01[0]) + asf(s01[1])), asu(asf(s23[0]) + asf(s23[1])), false, false);
        float e = asf(sc[0]) + asf(sc[1]);
        e += dpp<0x128>(e);
        const unsigned hb = pack_bf(xsel + rbf(e), 0.f) & 0xffffu;
        auto hp = __builtin_amdgcn_permlane16_swap(hb, hb, false, false);
        const unsigned hq = (unsigned)__builtin_amdgcn_ds_bpermute((((lane >> 1) & 7) + 32 * (lane & 1)) * 4, (int)(hp[0] | (hp[1] << 16)));
        if (lane < 16) __builtin_amdgcn_raw_buffer_store_b64(v2u{hq, tag}, rs, WS_GH + rho * 128 + lane * 8, 0, AUX_SC1);
      } else if (w == 1) {
        final_cell(ws, tag, 16 * j + r, lane, out, (unsigned*)(ws + WS_EPOCH));
      }
    } else if (is_mlp) {
      const int fg = 8 * j + ((r - 16) >> 1), nh = r & 1;
      const int kq = w >> 1, cth = w & 1;
      BAR();
      v4u gf[4], uf[4], df[2];
      {
        const lds_p p = L + L_GT + (128 * kq + 8 * g + (n16 >> 2)) * 64 + (16 * cth + 4 * (lane & 3)) * 2;
#pragma unroll
        for (int t = 0; t < 4; ++t) { gf[t] = tr_frag(p + t * 32 * 64, 64); uf[t] = tr_frag(p + 32768 + t * 32 * 64, 64); }
        const lds_p pd = L + L_DN + (8 * g + (n16 >> 2)) * 512 + (32 * w + 4 * (lane & 3)) * 2;
#pragma unroll
        for (int t = 0; t < 2; ++t) df[t] = tr_frag(pd + t * 32, 512);
      }
      const bool up = lane & 8;
      unsigned r3w = lds0 + L_R3 + ((((kq * 2 + (up ? 1 : 0)) * 2 + cth) * 8 + (lane & 7)) * 16 + 4 * g) * 4;
      {
        const v4u ones = {0x3f803f80u, 0x3f803f80u, 0x3f803f80u, 0x3f803f80u};
        f32x4 cg = {0.f, 0.f, 0.f, 0.f}, cu = {0.f, 0.f, 0.f, 0.f};
#pragma unroll
        for (int t = 0; t < 4; ++t) { cg = mfma(gf[t], ones, cg); cu = mfma(uf[t], ones, cu); }
        *(LAS f32x4*)(size_t)r3w = f32x4{up ? cu[0] : cg[0], up ? cu[1] : cg[1], up ? cu[2] : cg[2], up ? cu[3] : cg[3]};
        BAR();
        if (w == 0) {
          const LAS float* r3 = (const LAS float*)(L + L_R3) + (lane >> 5) * 256 + ((lane >> 4) & 1) * 128 + (lane & 15);
          ((LAS float*)(L + L_CS))[lane] = (r3[0] + r3[512]) + (r3[1024] + r3[1536]);
        }
      }
      unsigned goff = WS_GH + 16 * w * 128 + lane * 16;
      unsigned hnw = lds0 + L_HN + (lane & 7) * 1056 + (16 * w + (lane >> 3)) * 8, hrow = lds0 + L_HN + w * 1056 + lane * 16, hnr = lds0 + L_HN + (lane & 7) * 1056 + (128 * kq + 8 * g) * 2;
      unsigned r3r = lds0 + L_R3 + (((lane >> 4) & 1) * 8 + 2 * (w & 3) + (lane >> 5)) * 64 + (lane & 15) * 4;
      unsigned hhw = lds0 + L_HH + (2 * (w & 3) + (lane >> 5)) * 96 + (lane & 31) * 2, hhr = lds0 + L_HH + (lane & 7) * 96 + g * 16;
      int foff = WS_FP + ((64 * nh + 8 * w + (up ? 4 : 0) + g) * FP_C + fg) * 128 + (lane & 7) * 16;
      asm volatile("" : "+v"(goff), "+v"(hnw), "+v"(hrow), "+v"(hnr), "+v"(r3w), "+v"(r3r), "+v"(hhw), "+v"(hhr), "+v"(foff));
      asm volatile("" : "+v"(gf[0]), "+v"(gf[1]), "+v"(gf[2]), "+v"(gf[3]), "+v"(uf[0]), "+v"(uf[1]), "+v"(uf[2]), "+v"(uf[3]));
      asm volatile("" : "+v"(df[0]), "+v"(df[1]));
      v4u h0, h1;
      poll2x4(ws, tag, goff, goff + 1024, h0, h1);
      *(LAS v2u*)(size_t)hnw = v2u{h0.x, h0.z};
      *(LAS v2u*)(size_t)(hnw + 64) = v2u{h1.x, h1.z};
      BAR();
      {
        const lds_p pa = (lds_p)(size_t)hnr;
        f32x4 ag = {0.f, 0.f, 0.f, 0.f}, au = {0.f, 0.f, 0.f, 0.f};
#pragma unroll
        for (int t = 0; t < 4; ++t) {
          const v4u a = lds_ld128(pa + t * 64);
          ag = mfma(gf[t], a, ag);
          au = mfma(uf[t], a, au);
        }
        {
          const v4u hr = lds_ld128((lds_p)(size_t)hrow);
          const float f[8] = {bf_lo(hr.x), bf_hi(hr.x), bf_lo(hr.y), bf_hi(hr.y), bf_lo(hr.z), bf_hi(hr.z), bf_lo(hr.w), bf_hi(hr.w)};
          const float c0 = asf(__builtin_amdgcn_readfirstlane(asu(f[0])));
          float s1 = 0.f, s2 = 0.f;
#pragma unroll
          for (int i = 0; i < 8; ++i) { const float d = f[i] - c0; s1 += d; s2 = fmaf(d, d, s2); }
          s1 += dpp<0xB1>(s1); s2 += dpp<0xB1>(s2);
          s1 += dpp<0x4E>(s1); s2 += dpp<0x4E>(s2);
          s1 += dpp<0x141>(s1); s2 += dpp<0x141>(s2);
          s1 += dpp<0x140>(s1); s2 += dpp<0x140>(s2);
          auto sw = __builtin_amdgcn_permlane16_swap(asu(s1), asu(s2), false, false);
          const float t = x32_sum(asf(sw[0]) + asf(sw[1])) * (1.0f / D);
          const float m1 = asf(__builtin_amdgcn_readlane(asu(t), 0)), m2 = asf(__builtin_amdgcn_readlane(asu(t), 16));
          const float var = rbf(fmaxf(fmaf(-m1, m1, m2), 0.f));
          const float den = rbf(__builtin_amdgcn_sqrtf(rbf(var + 1e-5f)));
          if (lane == 0) *(LAS v2u*)(L + L_ST + w * 8) = v2u{asu(rbf(c0 + m1)), asu(__builtin_amdgcn_rcpf(den))};
        }
        *(LAS f32x4*)(size_t)r3w = f32x4{up ? au[0] : ag[0], up ? au[1] : ag[1], up ? au[2] : ag[2], up ? au[3] : ag[3]};
      }
      BAR();
      if (w < 4) {
        const int bb = 2 * w + (lane >> 5), n32 = lane & 31;
        const LAS float* r3 = (const LAS float*)(size_t)r3r;
        const v2u stt = *(const LAS v2u*)(L + L_ST + bb * 8);
        const float csg = ((const LAS float*)(L + L_CS))[n32], csu = ((const LAS float*)(L + L_CS))[32 + n32];
        float gg = (r3[0] + r3[512]) + (r3[1024] + r3[1536]);
        float uu = (r3[256] + r3[768]) + (r3[1280] + r3[1792]);
        gg = rbf(fmaf(-asf(stt.x), csg, gg) * asf(stt.y));
        uu = rbf(fmaf(-asf(stt.x), csu, uu) * asf(stt.y));
        const float sg = rbf(__builtin_amdgcn_rcpf(1.0f + __builtin_amdgcn_exp2f(-1.4426950408889634f * gg)));
        *(LAS unsigned short*)(size_t)hhw = (unsigned short)pack_bf(sg * uu, 0.f);
      }
      BAR();
      {
        const v4u a = lds_ld128((lds_p)(size_t)hhr);
        const f32x4 z = {0.f, 0.f, 0.f, 0.f};
        const f32x4 d0 = mfma(df[0], a, z), d1 = mfma(df[1], a, z);
        __builtin_amdgcn_raw_buffer_store_b128(v4u{pack_bf(up ? d1[0] : d0[0], up ? d1[1] : d0[1]), tag, pack_bf(up ? d1[2] : d0[2], up ? d1[3] : d0[3]), tag}, rs, foff, 0, AUX_SC1);
      }
    }
  } else {
    const int hw = wv, kq = kq0;
    BAR();
    asm volatile("s_waitcnt vmcnt(6)" : "+v"(xa), "+v"(xb));
    {
      float fa[8] = {bf_lo(xa.x), bf_hi(xa.x), bf_lo(xa.y), bf_hi(xa.y), bf_lo(xa.z), bf_hi(xa.z), bf_lo(xa.w), bf_hi(xa.w)};
      float fb[8] = {bf_lo(xb.x), bf_hi(xb.x), bf_lo(xb.y), bf_hi(xb.y), bf_lo(xb.z), bf_hi(xb.z), bf_lo(xb.w), bf_hi(xb.w)};
      layernorm8(fa);
      layernorm8(fb);
      if (g == kq) {
        *(LAS v4u*)(L + L_HN + (2 * hw) * 288 + n16 * 16) = v4u{pack_bf(fa[0], fa[1]), pack_bf(fa[2], fa[3]), pack_bf(fa[4], fa[5]), pack_bf(fa[6], fa[7])};
        *(LAS v4u*)(L + L_HN + (2 * hw + 1) * 288 + n16 * 16) = v4u{pack_bf(fb[0], fb[1]), pack_bf(fb[2], fb[3]), pack_bf(fb[4], fb[5]), pack_bf(fb[6], fb[7])};
      }
    }
    asm volatile("s_waitcnt vmcnt(0)" : "+v"(ep), "+v"(pf) :: "memory");
    const unsigned tag = ep + 1;
    lds_arrive(flags + 0, lane);
    lds_wait(flags + 0, 4);
    {
      const lds_p pa = L + L_HN + (lane & 7) * 288 + g * 16;
      const lds_p pb = L + L_WQ + (8 * g + (n16 >> 2)) * 128 + (16 * hw + 4 * (lane & 3)) * 2;
      f32x4 acc0 = mfma(tr_frag(pb, 128), lds_ld128(pa), f32x4{0.f, 0.f, 0.f, 0.f});
      f32x4 acc1 = mfma(tr_frag(pb + 32 * 128, 128), lds_ld128(pa + 64), f32x4{0.f, 0.f, 0.f, 0.f});
      acc0 = mfma(tr_frag(pb + 64 * 128, 128), lds_ld128(pa + 128), acc0);
      acc1 = mfma(tr_frag(pb + 96 * 128, 128), lds_ld128(pa + 192), acc1);
      publish_tile(acc0 + acc1, rs, WS_QP + (kq * QP_K + 32 * j + 4 * ct8 + hw) * 128, QP_B * 128, tag, lane);
    }
    {
      const u64 t_pub = __builtin_amdgcn_s_memrealtime();
      for (;;) {
        const u64 now = __builtin_amdgcn_s_memrealtime();
        if ((unsigned)(now - t_start) >= QDELAY && (unsigned)(now - t_pub) >= QAFTER) break;
        __builtin_amdgcn_s_sleep(1);
      }
    }
    {
      const int off = WS_QP + (b * QP_B + 32 * j + 8 * hw) * 128 + lane * 16;
      v4u v[4];
      pollNx4<4, QP_K * 128>(ws, tag, off, v);
      const float q0 = (asf(v[0].x) + asf(v[1].x)) + (asf(v[2].x) + asf(v[3].x)), q1 = (asf(v[0].z) + asf(v[1].z)) + (asf(v[2].z) + asf(v[3].z));
      *(LAS unsigned*)(L + L_Q + hw * 320 + lane * 4) = pack_bf(q0, q1);
      if (hw == 0 && lane == 0) flags[1] = tag;
    }
    lds_arrive(flags + 6, lane);
    __builtin_amdgcn_s_setprio(0);
    if (hw == 0) {
      while (flags[2] < 8u) __builtin_amdgcn_s_sleep(1);
      asm volatile("" ::: "memory");
      const float mw = ((const LAS float*)(L + L_MAX))[lane & 31], lw = ((const LAS float*)(L + L_SUM))[lane & 31];
      float mx = max2(mw, dpp<0x124>(mw));
      mx = max2(mx, dpp<0x128>(mx));
      mx = x16_max(mx);
      const float f = __builtin_amdgcn_exp2f((mw - mx) * 1.4426950408889634f);
      float l = f * lw;
      l += dpp<0x124>(l);
      l += dpp<0x128>(l);
      l = x16_sum(l);
      if (lane < 32) ((LAS float*)(L + L_MSC))[(lane & 3) * 8 + (lane >> 2)] = f * __builtin_amdgcn_rcpf(l);
      lds_arrive(flags + 4, lane);
      if (lane < 4) {
        const float lse = mx * 1.4426950408889634f + __builtin_amdgcn_logf(l);
        __builtin_amdgcn_raw_buffer_store_b64(v2u{asu(lse), tag}, rs, WS_LS + (b * HKV + j) * 128 + (lane * 4 + s) * 8, 0, AUX_SC1);
      }
    }
    if (is_wo) {
      const int h = 4 * j + (r >> 2), ns = r & 3;
      while (flags[4] < DMAFLAG) __builtin_amdgcn_s_sleep(2);
      const unsigned char* src = (const unsigned char*)wo + ((size_t)(128 * h + 32 * hw + g) * D + 128 * ns) * 2 + n16 * 16;
#pragma unroll
      for (int i = 0; i < 8; ++i) glds16(src + (size_t)i * 4 * D * 2, lds0 + L_W + (32 * hw + 4 * i) * 256);
      asm volatile("s_waitcnt vmcnt(0)" ::: "memory");
      BAR();
    } else if (is_mlp) {
      while (flags[4] < DMAFLAG) __builtin_amdgcn_s_sleep(2);
      const int fg = 8 * j + ((r - 16) >> 1), nh = r & 1;
      const size_t go = ((size_t)(128 * hw + (lane >> 2)) * FF + 32 * fg) * 2 + (lane & 3) * 16;
#pragma unroll
      for (int i = 0; i < 8; ++i) {
        if (i >= 2) asm volatile("s_waitcnt vmcnt(3)" ::: "memory");
        glds16((const unsigned char*)wg + go + (size_t)i * 16 * FF * 2, lds0 + L_GT + (128 * hw + 16 * i) * 64);
        if (i >= 2) asm volatile("s_waitcnt vmcnt(3)" ::: "memory");
        glds16((const unsigned char*)wu + go + (size_t)i * 16 * FF * 2, lds0 + L_UP + (128 * hw + 16 * i) * 64);
      }
      const unsigned char* sd = (const unsigned char*)wd + ((size_t)(32 * fg + 8 * hw + (lane >> 5)) * D + 256 * nh) * 2 + (lane & 31) * 16;
#pragma unroll
      for (int i = 0; i < 4; ++i) {
        asm volatile("s_waitcnt vmcnt(5)" ::: "memory");
        glds16(sd + (size_t)i * 2 * D * 2, lds0 + L_DN + (8 * hw + 2 * i) * 512);
      }
      asm volatile("s_waitcnt vmcnt(0)" ::: "memory");
      BAR();
    }
  }
}

}

extern "C" void kernel_launch(void* const* d_in, const int* in_sizes, int n_in, void* d_out, int out_size, void* d_ws,
                              size_t ws_size, hipStream_t stream) {
  (void)in_sizes; (void)n_in; (void)out_size; (void)ws_size;
  (void)hipFuncSetAttribute((const void*)fused_decode, hipFuncAttributeMaxDynamicSharedMemorySize, LDS_BYTES);
  fused_decode<<<256, NTHR, LDS_BYTES, stream>>>(
      (const unsigned short*)d_in[0], (const unsigned short*)d_in[1], (const unsigned short*)d_in[2], (const unsigned short*)d_in[3],
      (const unsigned short*)d_in[4], (const unsigned short*)d_in[6], (const unsigned short*)d_in[7], (const unsigned short*)d_in[8],
      (unsigned short*)d_out, (unsigned char*)d_ws);
}
```

```cpp
#include <hip/hip_runtime.h>
#include <hip/hip_bf16.h>
#include <stdint.h>

namespace {

typedef unsigned v4u __attribute__((ext_vector_type(4)));
typedef unsigned v2u __attribute__((ext_vector_type(2)));
typedef float f32x4 __attribute__((ext_vector_type(4)));
typedef __bf16 bf16x8 __attribute__((ext_vector_type(8)));
typedef unsigned long long u64;
#define LAS __attribute__((address_space(3)))
typedef LAS unsigned char* lds_p;

constexpr int D = 512, HQ = 32, HKV = 8, SK = 4096, DH = 128, WIN = 1024, FF = 1792;
constexpr int NTHR = 768;
constexpr int AUX_SC1 = 16;
constexpr unsigned SPIN_CAP = 1u << 16;
constexpr int PD = 3;
constexpr int THRN = 10;
constexpr unsigned DMAFLAG = 2;
constexpr unsigned QDELAY = 290;
constexpr unsigned QAFTER = 60;
constexpr int CODE_LINES = 119;

constexpr int WS_EPOCH = 0;
constexpr int PAD = 1;
constexpr int QP_B = 256 + PAD, QP_K = 8 * QP_B;
constexpr int RP_C = 32 + PAD;
constexpr int FP_C = 56 + PAD;
constexpr int WS_QP = 1 << 16;
constexpr int WS_OP = WS_QP + 2 * QP_K * 128;
constexpr int WS_LS = WS_OP + (1 << 19);
constexpr int WS_QP2 = 2 << 20;
constexpr int WS_RP = WS_QP2 + 2 * QP_K * 128;
constexpr int WS_GH = WS_RP + 128 * RP_C * 128;
constexpr int WS_FP = WS_GH + (1 << 14);
constexpr int WS_END = WS_FP + 128 * FP_C * 128;
static_assert(WS_LS + 8192 <= WS_QP2 && WS_END <= (4 << 20), "");

constexpr int L_RED = 0;
constexpr int L_WQ = 0;
constexpr int L_Q = 16384;
constexpr int L_MAX = L_Q + 4 * 320;
constexpr int L_SUM = L_MAX + 128;
constexpr int L_FLAG = L_SUM + 128;
constexpr int L_MSC = L_FLAG + 64;
constexpr int L_CS = L_MSC + 128;
constexpr int L_ST = L_CS + 256;
constexpr int L_OS = L_ST + 64;
constexpr int L_HN = L_OS + 8 * 288;
constexpr int L_R3 = L_HN + 8 * 1056;
constexpr int L_HH = L_R3 + 8192;
constexpr int L_W = ((L_HH + 8 * 96 + 255) / 256) * 256;
constexpr int L_GT = L_W, L_UP = L_W + 32768, L_DN = L_W + 65536;
constexpr int LDS_BYTES = L_W + 98304;
static_assert(LDS_BYTES <= 160 * 1024 && LDS_BYTES > 80 * 1024, "one workgroup per CU");

__device__ __forceinline__ float bf_lo(unsigned u) { return __builtin_bit_cast(float, u << 16); }
__device__ __forceinline__ float bf_hi(unsigned u) { return __builtin_bit_cast(float, u & 0xffff0000u); }
__device__ __forceinline__ unsigned pack_bf(float lo, float hi) {
  typedef __bf16 bf2 __attribute__((ext_vector_type(2)));
  bf2 r = {(__bf16)lo, (__bf16)hi};
  return __builtin_bit_cast(unsigned, r);
}
__device__ __forceinline__ float rbf(float f) { return bf_lo(pack_bf(f, f)); }
__device__ __forceinline__ float asf(unsigned u) { return __builtin_bit_cast(float, u); }
__device__ __forceinline__ unsigned asu(float f) { return __builtin_bit_cast(unsigned, f); }
template <int CTRL> __device__ __forceinline__ float dpp(float x) {
  return __builtin_bit_cast(float, __builtin_amdgcn_mov_dpp(__builtin_bit_cast(int, x), CTRL, 0xf, 0xf, true));
}
__device__ __forceinline__ float max3(float a, float b, float c) {
  float r;
  asm("v_max3_f32 %0, %1, %2, %3" : "=v"(r) : "v"(a), "v"(b), "v"(c));
  return r;
}
__device__ __forceinline__ float max2(float a, float b) {
  float r;
  asm("v_max_f32 %0, %1, %2" : "=v"(r) : "v"(a), "v"(b));
  return r;
}
__device__ __forceinline__ float x16_sum(float x) {
  auto s = __builtin_amdgcn_permlane16_swap(asu(x), asu(x), false, false);
  return asf(s[0]) + asf(s[1]);
}
__device__ __forceinline__ float x32_sum(float x) {
  auto s = __builtin_amdgcn_permlane32_swap(asu(x), asu(x), false, false);
  return asf(s[0]) + asf(s[1]);
}
__device__ __forceinline__ float x16_max(float x) {
  auto s = __builtin_amdgcn_permlane16_swap(asu(x), asu(x), false, false);
  return max2(asf(s[0]), asf(s[1]));
}
__device__ __forceinline__ float x32_max(float x) {
  auto s = __builtin_amdgcn_permlane32_swap(asu(x), asu(x), false, false);
  return max2(asf(s[0]), asf(s[1]));
}
__device__ __forceinline__ f32x4 mfma(v4u a, v4u b, f32x4 c) {
  return __builtin_amdgcn_mfma_f32_16x16x32_bf16(__builtin_bit_cast(bf16x8, a), __builtin_bit_cast(bf16x8, b), c, 0, 0, 0);
}
__device__ __forceinline__ v2u tr_read(lds_p p) {
  typedef short v4s __attribute__((ext_vector_type(4)));
  return __builtin_bit_cast(v2u, __builtin_amdgcn_ds_read_tr16_b64_v4i16((LAS v4s*)p));
}
__device__ __forceinline__ v4u tr_frag(lds_p p, int row_stride) {
  const v2u a = tr_read(p), b = tr_read(p + 4 * row_stride);
  return v4u{a.x, a.y, b.x, b.y};
}
__device__ __forceinline__ v4u lds_ld128(lds_p p) { return *(const LAS v4u*)p; }
__device__ __forceinline__ void glds16(const void* gsrc, unsigned lds_dst) {
  unsigned keep;
  asm volatile("s_mov_b32 %0, m0\n\ts_mov_b32 m0, %2\n\ts_nop 0\n\tglobal_load_lds_dwordx4 %1, off nt\n\ts_mov_b32 m0, %0"
               : "=&s"(keep) : "v"(gsrc), "s"(lds_dst) : "memory");
}
#define BAR() do { asm volatile("s_waitcnt lgkmcnt(0)" ::: "memory"); __builtin_amdgcn_s_barrier(); asm volatile("" ::: "memory"); } while (0)
__device__ __forceinline__ void lds_arrive(volatile LAS unsigned* p, int lane) {
  asm volatile("" ::: "memory");
  if (lane == 0) asm volatile("ds_add_u32 %0, %1" :: "v"((unsigned)(size_t)p), "v"(1u) : "memory");
  asm volatile("" ::: "memory");
}
__device__ __forceinline__ void lds_wait(volatile LAS unsigned* p, unsigned n) {
  while (*p < n) { }
  asm volatile("" ::: "memory");
}
typedef unsigned long long mask_t;
__device__ __forceinline__ mask_t bad4(v4u v, unsigned tag) { return __builtin_amdgcn_ballot_w64(v.y != tag) | __builtin_amdgcn_ballot_w64(v.w != tag); }
__device__ __forceinline__ mask_t bad2(v2u v, unsigned tag) { return __builtin_amdgcn_ballot_w64(v.y != tag); }
#define PL4(n_, o_, m_) "s_mov_b64 exec, %" #m_ "\n\tglobal_load_dwordx4 %" #n_ ", %" #o_ ", %[base] sc1\n\t"
#define PL2(n_, o_, m_) "s_mov_b64 exec, %" #m_ "\n\tglobal_load_dwordx2 %" #n_ ", %" #o_ ", %[base] sc1\n\t"
#define PLEND "s_mov_b64 exec, -1\n\ts_waitcnt vmcnt(0)"
__device__ __forceinline__ void poll1x4(const unsigned char* ws, unsigned tag, unsigned o0, v4u& a) {
  a = v4u{0u, ~tag, 0u, ~tag};
  for (unsigned spins = 0; spins <= SPIN_CAP; ++spins) {
    const mask_t m0 = bad4(a, tag);
    if (m0 == 0) break;
    asm volatile(PL4(0, 1, 2) PLEND : "+v"(a) : "v"(o0), "s"(m0), [base] "s"(ws) : "memory");
    __builtin_amdgcn_s_sleep(2);
  }
}
__device__ __forceinline__ void poll2x4(const unsigned char* ws, unsigned tag, unsigned o0, unsigned o1, v4u& a, v4u& b) {
  a = b = v4u{0u, ~tag, 0u, ~tag};
  for (unsigned spins = 0; spins <= SPIN_CAP; ++spins) {
    const mask_t m0 = bad4(a, tag), m1 = bad4(b, tag);
    if ((m0 | m1) == 0) break;
    asm volatile(PL4(0, 2, 4) PL4(1, 3, 5) PLEND : "+v"(a), "+v"(b) : "v"(o0), "v"(o1), "s"(m0), "s"(m1), [base] "s"(ws) : "memory");
  }
}
__device__ __forceinline__ void poll5x2(const unsigned char* ws, unsigned tag, unsigned oa, unsigned ob, v2u (&v)[5]) {
  unsigned o[4];
#pragma unroll
  for (int i = 0; i < 4; ++i) o[i] = oa + i * 512;
#pragma unroll
  for (int i = 0; i < 5; ++i) v[i] = v2u{0u, ~tag};
  for (unsigned spins = 0; spins <= SPIN_CAP; ++spins) {
    mask_t m[5], any = 0;
#pragma unroll
    for (int i = 0; i < 5; ++i) { m[i] = bad2(v[i], tag); any |= m[i]; }
    if (any == 0) break;
    asm volatile(PL2(0, 5, 10) PL2(1, 6, 11) PL2(2, 7, 12) PL2(3, 8, 13) PL2(4, 9, 14) PLEND
                 : "+v"(v[0]), "+v"(v[1]), "+v"(v[2]), "+v"(v[3]), "+v"(v[4])
                 : "v"(o[0]), "v"(o[1]), "v"(o[2]), "v"(o[3]), "v"(ob),
                   "s"(m[0]), "s"(m[1]), "s"(m[2]), "s"(m[3]), "s"(m[4]), [base] "s"(ws) : "memory");
  }
}
template <int N, int STEP, int JUMP = 2 * STEP>
__device__ __forceinline__ void pollNx4(const unsigned char* ws, unsigned tag, unsigned o0, v4u (&v)[N]) {
  static_assert(N == 4 || N == 7, "");
  unsigned o[N];
#pragma unroll
  for (int i = 0; i < N; ++i) { v[i] = v4u{0u, ~tag, 0u, ~tag}; o[i] = o0 + (i & 1) * STEP + (i >> 1) * JUMP; }
  for (unsigned spins = 0; spins <= SPIN_CAP; ++spins) {
    mask_t m[N], any = 0;
#pragma unroll
    for (int i = 0; i < N; ++i) { m[i] = bad4(v[i], tag); any |= m[i]; }
    if (any == 0) break;
    if constexpr (N == 4) {
      asm volatile(PL4(0, 4, 8) PL4(1, 5, 9) PL4(2, 6, 10) PL4(3, 7, 11) PLEND
                   : "+v"(v[0]), "+v"(v[1]), "+v"(v[2]), "+v"(v[3])
                   : "v"(o[0]), "v"(o[1]), "v"(o[2]), "v"(o[3]), "s"(m[0]), "s"(m[1]), "s"(m[2]), "s"(m[3]), [base] "s"(ws) : "memory");
    } else {
      asm volatile(PL4(0, 7, 14) PL4(1, 8, 15) PL4(2, 9, 16) PL4(3, 10, 17) PL4(4, 11, 18) PL4(5, 12, 19) PL4(6, 13, 20) PLEND
                   : "+v"(v[0]), "+v"(v[1]), "+v"(v[2]), "+v"(v[3]), "+v"(v[4]), "+v"(v[5]), "+v"(v[6])
                   : "v"(o[0]), "v"(o[1]), "v"(o[2]), "v"(o[3]), "v"(o[4]), "v"(o[5]), "v"(o[6]),
                     "s"(m[0]), "s"(m[1]), "s"(m[2]), "s"(m[3]), "s"(m[4]), "s"(m[5]), "s"(m[6]), [base] "s"(ws) : "memory");
    }
  }
}

__device__ __forceinline__ void layernorm8(float (&f)[8]) {
  const float c0 = asf(__builtin_amdgcn_readfirstlane(asu(f[0])));
  float s1 = 0.f, s2 = 0.f;
#pragma unroll
  for (int i = 0; i < 8; ++i) { const float d = f[i] - c0; s1 += d; s2 = fmaf(d, d, s2); }
  s1 += dpp<0xB1>(s1); s2 += dpp<0xB1>(s2);
  s1 += dpp<0x4E>(s1); s2 += dpp<0x4E>(s2);
  s1 += dpp<0x141>(s1); s2 += dpp<0x141>(s2);
  s1 += dpp<0x140>(s1); s2 += dpp<0x140>(s2);
  s1 = x16_sum(s1); s2 = x16_sum(s2);
  s1 = x32_sum(s1) * (1.0f / D); s2 = x32_sum(s2) * (1.0f / D);
  const float var = rbf(fmaxf(fmaf(-s1, s1, s2), 0.f));
  const float den = rbf(__builtin_amdgcn_sqrtf(rbf(var + 1e-5f)));
  const float inv = __builtin_amdgcn_rcpf(den), mb = rbf(c0 + s1);
#pragma unroll
  for (int i = 0; i < 8; ++i) f[i] = rbf(f[i] - mb) * inv;
}

__device__ __forceinline__ void publish_tile(f32x4 d, __amdgpu_buffer_rsrc_t rs, int base, int stride_b, unsigned tag, int lane) {
  const bool t = lane & 8;
  const int src = ((lane >> 3) + 8 * (lane & 1) + 16 * ((lane >> 1) & 3)) * 4;
  const unsigned v0 = (unsigned)__builtin_amdgcn_ds_bpermute(src, (int)asu(t ? d[2] : d[0]));
  const unsigned v1 = (unsigned)__builtin_amdgcn_ds_bpermute(src, (int)asu(t ? d[3] : d[1]));
  __builtin_amdgcn_raw_buffer_store_b128(v4u{v0, tag, v1, tag}, rs, base + (lane >> 3) * stride_b + (lane & 7) * 16, 0, AUX_SC1);
}

__device__ __forceinline__ void final_cell(const unsigned char* ws, unsigned tag, int cell, int lane, unsigned short* out, unsigned* ws_epoch) {
  const int p = lane & 7, row = lane >> 4;
  v4u hv;
  poll1x4(ws, tag, WS_GH + cell * 128 + p * 16, hv);
  float hsel = row == 0 ? bf_lo(hv.x) : row == 1 ? bf_hi(hv.x) : row == 2 ? bf_lo(hv.z) : bf_hi(hv.z);
  unsigned oo = (p * D + 4 * cell + (row & 2)) * 2;
  asm volatile("" : "+v"(hsel), "+v"(oo));
  v4u v[7];
  pollNx4<7, 8 * 128>(ws, tag, WS_FP + (cell * FP_C + (lane >> 3)) * 128 + p * 16, v);
  float a0 = 0.f, a1 = 0.f, a2 = 0.f, a3 = 0.f;
#pragma unroll
  for (int i = 0; i < 7; ++i) { a0 += bf_lo(v[i].x); a1 += bf_hi(v[i].x); a2 += bf_lo(v[i].z); a3 += bf_hi(v[i].z); }
  auto s01 = __builtin_amdgcn_permlane16_swap(asu(a0), asu(a1), false, false);
  auto s23 = __builtin_amdgcn_permlane16_swap(asu(a2), asu(a3), false, false);
  const float c0 = asf(s01[0]) + asf(s01[1]), c1 = asf(s23[0]) + asf(s23[1]);
  auto sc = __builtin_amdgcn_permlane32_swap(asu(c0), asu(c1), false, false);
  float e = asf(sc[0]) + asf(sc[1]);
  e += dpp<0x128>(e);
  const unsigned ob = pack_bf(hsel + rbf(e), 0.f) & 0xffffu;
  auto op = __builtin_amdgcn_permlane16_swap(ob, ob, false, false);
  if ((lane & 24) == 0) *(__attribute__((address_space(1))) unsigned*)((unsigned char*)out + oo) = op[0] | (op[1] << 16);
  if (cell == 0 && lane == 0) __hip_atomic_store(ws_epoch, tag, __ATOMIC_RELAXED, __HIP_MEMORY_SCOPE_AGENT);
}

__global__ void __launch_bounds__(NTHR, 3)
fused_decode(const unsigned short* __restrict__ x, const unsigned short* __restrict__ kc, const unsigned short* __restrict__ vc,
             const unsigned short* __restrict__ wq, const unsigned short* __restrict__ wo, const unsigned short* __restrict__ wg,
             const unsigned short* __restrict__ wu, const unsigned short* __restrict__ wd, unsigned short* __restrict__ out,
             unsigned char* __restrict__ ws) {
  unsigned long long code_base;
  asm volatile("s_getpc_b64 %0" : "=s"(code_base));
  code_base &= ~0xffull;
  extern __shared__ __attribute__((aligned(16))) unsigned char smem[];
  const lds_p L = (lds_p)smem;
  const unsigned lds0 = (unsigned)(size_t)L;
  const int tid = threadIdx.x, lane = tid & 63;
  const int wv = __builtin_amdgcn_readfirstlane(tid >> 6);
  const int w = wv - 4;
  const int c = blockIdx.x, j = c & 7, r = c >> 3;
  const int b = r >> 2, s = r & 3;
  const int g = lane >> 4, n16 = lane & 15;
  const bool is_wo = r < 16, is_mlp = (r >= 16) && (j < 7);
  const __amdgpu_buffer_rsrc_t rs = __builtin_amdgcn_make_buffer_rsrc((void*)ws, 0, WS_END, 0x00020000);
  volatile LAS unsigned* flags = (volatile LAS unsigned*)(L + L_FLAG);

  const u64 t_start = __builtin_amdgcn_s_memrealtime();
  const int kq0 = r >> 3, ct8 = r & 7;
  v4u xa, xb; unsigned ep, pf, pt;
  asm volatile("" : "=v"(xa), "=v"(xb), "=v"(ep), "=v"(pf), "=v"(pt));
  if (wv < 4) {
    const int hw = wv;
    __builtin_amdgcn_s_setprio(2);
    const unsigned zero = 0;
    const unsigned xo = (2 * hw) * (D * 2) + lane * 16;
    asm volatile("global_load_dwordx4 %0, %1, %2" : "+v"(xa) : "v"(xo), "s"(x));
    asm volatile("global_load_dwordx4 %0, %1, %2 offset:1024" : "+v"(xb) : "v"(xo), "s"(x));
    const unsigned char* src = (const unsigned char*)wq + ((size_t)(128 * kq0 + 32 * hw + (lane >> 3)) * (HQ * DH) + 512 * j + 64 * ct8) * 2 + (lane & 7) * 16;
#pragma unroll
    for (int i = 0; i < 4; ++i) glds16(src + (size_t)i * 8 * (HQ * DH) * 2, lds0 + L_WQ + (32 * hw + 8 * i) * 128);
    asm volatile("global_load_dword %0, %1, %2" : "+v"(ep) : "v"(zero), "s"(ws));
    const unsigned po = min((r * 4 + hw) * 2 + (lane & 1), CODE_LINES - 1) * 128;
    asm volatile("global_load_dword %0, %1, %2" : "+v"(pf) : "v"(po), "s"(code_base));
    const unsigned char* tp = hw == 0 ? ((r & 1) ? (const unsigned char*)out : (const unsigned char*)wo + (size_t)(128 * (4 * j + ((r >> 2) & 3))) * D * 2)
                                      : (hw == 1 ? (const unsigned char*)wg : hw == 2 ? (const unsigned char*)wu : (const unsigned char*)wd) + (r & 1) * (D * FF * 2 - 128);
    asm volatile("global_load_dword %0, %1, %2" : "+v"(pt) : "v"(zero), "s"(tp));
  }
  int wv2 = wv;
  asm volatile("" : "+s"(wv2));

  if (__builtin_expect(wv2 >= 4, 1)) {
    const size_t tile = ((size_t)(b * HKV + j) * SK + (SK - WIN) + 256 * s + 32 * w) * (DH * 2);
    const unsigned char* kb = (const unsigned char*)kc + tile + 4096;
    const unsigned char* vb = (const unsigned char*)vc + tile;
    const unsigned koff = (lane & 7) * 256 + (2 * g + ((lane >> 3) & 1)) * 16;
    const unsigned voff = (16 * (g >> 1) + 4 * (g & 1)) * 256 + n16 * 16;
    v4u K[8], V[8];
#define LDK(i, imm) asm volatile("global_load_dwordx4 %0, %1, %2 offset:" #imm " nt" : "=v"(K[i]) : "v"(koff), "s"(kb))
#define LDV(i, imm) asm volatile("global_load_dwordx4 %0, %1, %2 offset:" #imm " nt" : "=v"(V[i]) : "v"(voff), "s"(vb))
#define THR(n) do { if (n < THRN || __builtin_amdgcn_readfirstlane(flags[6]) < 4) asm volatile("s_waitcnt vmcnt(%0)" :: "i"(PD - 1)); } while (0)
    static_assert(PD == 3, "three loads go out in front of the barrier");
    LDK(0, -4096); LDK(1, -3968); LDV(0, 0);
    if (w == 0 && lane < 8) flags[lane] = 0;
    BAR();
    THR(3); LDK(2, -2048); THR(4); LDK(3, -1920); THR(5); LDK(4, 0); THR(6); LDK(5, 128); THR(7); LDK(6, 2048); THR(8); LDK(7, 2176);
    THR(9); LDV(1, 256); THR(10); LDV(2, 512); THR(11); LDV(3, 768);
    THR(12); LDV(4, 2048); THR(13); LDV(5, 2304); THR(14); LDV(6, 2560); THR(15); LDV(7, 2816);
    while (flags[6] < 4u) __builtin_amdgcn_s_sleep(1);
    asm volatile("" ::: "memory");
    const unsigned tag = flags[1];
    const lds_p qa = L + L_Q + (n16 & 3) * 320 + (2 * g + ((n16 >> 2) & 1)) * 16;
    const v4u q0 = lds_ld128(qa), q1 = lds_ld128(qa + 128);
    asm volatile("s_waitcnt vmcnt(7)" : "+v"(K[0]), "+v"(K[1]), "+v"(K[2]), "+v"(K[3]), "+v"(K[4]), "+v"(K[5]), "+v"(K[6]), "+v"(K[7]));
    f32x4 sa[4];
#pragma unroll
    for (int o = 0; o < 4; ++o) {
      sa[o] = mfma(K[2 * o], q0, f32x4{0.f, 0.f, 0.f, 0.f});
      sa[o] = mfma(K[2 * o + 1], q1, sa[o]);
    }
    float sc[8];
    {
      const bool par = lane & 4;
      float raw[8];
#pragma unroll
      for (int p = 0; p < 2; ++p)
#pragma unroll
        for (int i = 0; i < 4; ++i) {
          auto sw = __builtin_amdgcn_permlane32_swap(asu(sa[p][i]), asu(sa[p + 2][i]), false, false);
          const float a = par ? asf(sw[1]) : asf(sw[0]);
          raw[4 * p + i] = a + dpp<0x124>(a);
        }
#pragma unroll
      for (int i = 0; i < 8; i += 2) {
        const unsigned u = pack_bf(raw[i], raw[i + 1]);
        const unsigned t = pack_bf(bf_lo(u) * 0.08838834764831845f, bf_hi(u) * 0.08838834764831845f);
        sc[i] = bf_lo(t); sc[i + 1] = bf_hi(t);
      }
    }
    float m = max3(max3(sc[0], sc[1], sc[2]), max3(sc[3], sc[4], sc[5]), max2(sc[6], sc[7]));
    m = x32_max(x16_max(m));
    float pe[8], ls;
    {
      const float mc = m * 1.4426950408889634f;
#pragma unroll
      for (int i = 0; i < 8; ++i) pe[i] = __builtin_amdgcn_exp2f(fmaf(sc[i], 1.4426950408889634f, -mc));
      ls = ((pe[0] + pe[1]) + (pe[2] + pe[3])) + ((pe[4] + pe[5]) + (pe[6] + pe[7]));
    }
    v4u PA[4];
    {
      const v4u P = {pack_bf(pe[0], pe[1]), pack_bf(pe[2], pe[3]), pack_bf(pe[4], pe[5]), pack_bf(pe[6], pe[7])};
#pragma unroll
      for (int mq = 0; mq < 4; ++mq) {
        const bool on = (n16 >> 2) == mq;
        PA[mq] = v4u{on ? P.x : 0u, on ? P.y : 0u, on ? P.z : 0u, on ? P.w : 0u};
      }
    }
    ls = x32_sum(x16_sum(ls));
    if (lane < 4) {
      ((LAS float*)(L + L_MAX))[w * 4 + lane] = m;
      ((LAS float*)(L + L_SUM))[w * 4 + lane] = ls;
    }
    lds_arrive(flags + 2, lane);
    unsigned T[8][4];
#define XPOSE(n_)                                                                                         \
  _Pragma("unroll") for (int mm = 0; mm < 4; ++mm) {                                                      \
    T[2 * mm][n_] = __builtin_amdgcn_perm(V[2 * n_ + 1][mm], V[2 * n_][mm], 0x05040100u);                 \
    T[2 * mm + 1][n_] = __builtin_amdgcn_perm(V[2 * n_ + 1][mm], V[2 * n_][mm], 0x07060302u);             \
  }                                                                                                       \
  asm volatile("" : "+v"(T[0][n_]), "+v"(T[1][n_]), "+v"(T[2][n_]), "+v"(T[3][n_]), "+v"(T[4][n_]), "+v"(T[5][n_]), "+v"(T[6][n_]), "+v"(T[7][n_]));
    int ooff = WS_OP + ((b * HQ + 4 * j + (w >> 1)) * 4 + s) * 512 + (w & 1) * 256 + n16 * 16;
    const int dr = 64 * (w & 1) + 4 * n16 + g, dw = 8 * n16 + g;
    unsigned redr_a = lds0 + L_RED + ((w >> 1) * 128 + (dr ^ ((dr >> 5) << 1))) * 4;
    unsigned redw_a = lds0 + L_RED + w * 2048 + (dw ^ ((dw >> 5) << 1)) * 4;
    const int h = 4 * j + (r >> 2), ns = r & 3;
    unsigned doff = WS_OP + (w * HQ + h) * 4 * 512 + lane * 8;
    unsigned loff = WS_LS + (w * HKV + j) * 128 + ((h & 3) * 4 + (lane & 3)) * 8;
    int roff = WS_RP + ((32 * ns + 4 * w + g) * RP_C + h) * 128 + (lane & 7) * 16;
    unsigned osw = lds0 + L_OS + w * 288 + lane * 4, osr = lds0 + L_OS + (lane & 7) * 288 + g * 16;
    unsigned wfa = lds0 + L_W + (8 * g + (n16 >> 2)) * 256 + (16 * w + 4 * (lane & 3)) * 2;
    asm volatile("" : "+v"(PA[0]), "+v"(PA[1]), "+v"(PA[2]), "+v"(PA[3]), "+v"(ooff), "+v"(redr_a), "+v"(redw_a));
    asm volatile("" : "+v"(doff), "+v"(loff), "+v"(roff), "+v"(osw), "+v"(osr), "+v"(wfa));
    asm volatile("s_waitcnt vmcnt(6)" : "+v"(V[0]), "+v"(V[1])); XPOSE(0)
    asm volatile("s_waitcnt vmcnt(4)" : "+v"(V[2]), "+v"(V[3])); XPOSE(1)
    asm volatile("s_waitcnt vmcnt(2)" : "+v"(V[4]), "+v"(V[5])); XPOSE(2)
    asm volatile("s_waitcnt vmcnt(0)" : "+v"(V[6]), "+v"(V[7]));
    XPOSE(3)
    {
      f32x4 oa = {0.f, 0.f, 0.f, 0.f}, ob = {0.f, 0.f, 0.f, 0.f};
#pragma unroll
      for (int i = 0; i < 4; ++i) {
        oa = mfma(PA[i], v4u{T[i][0], T[i][1], T[i][2], T[i][3]}, oa);
        ob = mfma(PA[i], v4u{T[i + 4][0], T[i + 4][1], T[i + 4][2], T[i + 4][3]}, ob);
      }
      LAS float* red = (LAS float*)(size_t)redw_a;
#pragma unroll
      for (int hh = 0; hh < 4; ++hh) { red[hh * 128] = oa[hh]; *(LAS float*)(size_t)((redw_a ^ 16) + hh * 512) = ob[hh]; }
    }
    lds_arrive(flags + 4, lane);
    const LAS float* redr = (const LAS float*)(size_t)redr_a;
    lds_wait(flags + 4, 9);
    {
      const f32x4 ma = *(const LAS f32x4*)(L + L_MSC + (w >> 1) * 32), mb = *(const LAS f32x4*)(L + L_MSC + (w >> 1) * 32 + 16);
      float o = redr[0] * ma[0], o2 = redr[512] * ma[1];
      o = fmaf(redr[2 * 512], ma[2], o); o2 = fmaf(redr[3 * 512], ma[3], o2);
      o = fmaf(redr[4 * 512], mb[0], o); o2 = fmaf(redr[5 * 512], mb[1], o2);
      o = fmaf(redr[6 * 512], mb[2], o); o2 = fmaf(redr[7 * 512], mb[3], o2);
      o += o2;
      auto s16 = __builtin_amdgcn_permlane16_swap(asu(o), asu(o), false, false);
      const unsigned pr = pack_bf(asf(s16[0]), asf(s16[1]));
      auto s32 = __builtin_amdgcn_permlane32_swap(pr, pr, false, false);
      if (lane < 16) __builtin_amdgcn_raw_buffer_store_b128(v4u{s32[0], tag, s32[1], tag}, rs, ooff, 0, AUX_SC1);
    }

    if (is_wo) {
      BAR();
      v4u wf[4];
      {
        const lds_p p = (lds_p)(size_t)wfa;
#pragma unroll
        for (int t = 0; t < 4; ++t) wf[t] = tr_frag(p + t * 32 * 256, 256);
      }
      v2u dv[5];
      poll5x2(ws, tag, doff, loff, dv);
      {
        const float lse = asf(dv[4].x);
        float mx = max2(lse, dpp<0xB1>(lse));
        mx = max2(mx, dpp<0x4E>(mx));
        const float e = __builtin_amdgcn_exp2f(lse - mx);
        float es = e + dpp<0xB1>(e);
        es += dpp<0x4E>(es);
        const float wt = e * __builtin_amdgcn_rcpf(es);
        const float e0 = dpp<0x00>(wt), e1 = dpp<0x55>(wt), e2 = dpp<0xAA>(wt), e3 = dpp<0xFF>(wt);
        const float o0 = fmaf(e0, bf_lo(dv[0].x), e1 * bf_lo(dv[1].x)) + fmaf(e2, bf_lo(dv[2].x), e3 * bf_lo(dv[3].x));
        const float o1 = fmaf(e0, bf_hi(dv[0].x), e1 * bf_hi(dv[1].x)) + fmaf(e2, bf_hi(dv[2].x), e3 * bf_hi(dv[3].x));
        *(LAS unsigned*)(size_t)osw = pack_bf(o0, o1);
      }
      BAR();
      {
        const lds_p pa = (lds_p)(size_t)osr;
        f32x4 acc0 = mfma(wf[0], lds_ld128(pa), f32x4{0.f, 0.f, 0.f, 0.f});
        f32x4 acc1 = mfma(wf[1], lds_ld128(pa + 64), f32x4{0.f, 0.f, 0.f, 0.f});
        acc0 = mfma(wf[2], lds_ld128(pa + 128), acc0);
        acc1 = mfma(wf[3], lds_ld128(pa + 192), acc1);
        acc0 += acc1;
        if ((lane & 8) == 0) __builtin_amdgcn_raw_buffer_store_b128(v4u{pack_bf(acc0[0], acc0[1]), tag, pack_bf(acc0[2], acc0[3]), tag}, rs, roff, 0, AUX_SC1);
      }
      if (w == 0) {
        const int rho = 16 * j + r;
        const unsigned xv = *(const unsigned*)(x + (lane & 7) * D + 4 * rho + (g & 2));
        float xsel = (g & 1) ? bf_hi(xv) : bf_lo(xv);
        asm volatile("" : "+v"(xsel));
        v4u v[4];
        pollNx4<4, 8 * 128>(ws, tag, WS_RP + (rho * RP_C + (lane >> 3)) * 128 + (lane & 7) * 16, v);
        const float a0 = (bf_lo(v[0].x) + bf_lo(v[1].x)) + (bf_lo(v[2].x) + bf_lo(v[3].x)), a1 = (bf_hi(v[0].x) + bf_hi(v[1].x)) + (bf_hi(v[2].x) + bf_hi(v[3].x));
        const float a2 = (bf_lo(v[0].z) + bf_lo(v[1].z)) + (bf_lo(v[2].z) + bf_lo(v[3].z)), a3 = (bf_hi(v[0].z) + bf_hi(v[1].z)) + (bf_hi(v[2].z) + bf_hi(v[3].z));
        auto s01 = __builtin_amdgcn_permlane16_swap(asu(a0), asu(a1), false, false);
        auto s23 = __builtin_amdgcn_permlane16_swap(asu(a2), asu(a3), false, false);
        auto sc = __builtin_amdgcn_permlane32_swap(asu(asf(s01[0]) + asf(s01[1])), asu(asf(s23[0]) + asf(s23[1])), false, false);
        float e = asf(sc[0]) + asf(sc[1]);
        e += dpp<0x128>(e);
        const unsigned hb = pack_bf(xsel + rbf(e), 0.f) & 0xffffu;
        auto hp = __builtin_amdgcn_permlane16_swap(hb, hb, false, false);
        const unsigned hq = (unsigned)__builtin_amdgcn_ds_bpermute((((lane >> 1) & 7) + 32 * (lane & 1)) * 4, (int)(hp[0] | (hp[1] << 16)));
        if (lane < 16) __builtin_amdgcn_raw_buffer_store_b64(v2u{hq, tag}, rs, WS_GH + rho * 128 + lane * 8, 0, AUX_SC1);
      } else if (w == 1) {
        final_cell(ws, tag, 16 * j + r, lane, out, (unsigned*)(ws + WS_EPOCH));
      }
    } else if (is_mlp) {
      const int fg = 8 * j + ((r - 16) >> 1), nh = r & 1;
      const int kq = w >> 1, cth = w & 1;
      BAR();
      v4u gf[4], uf[4], df[2];
      {
        const lds_p p = L + L_GT + (128 * kq + 8 * g + (n16 >> 2)) * 64 + (16 * cth + 4 * (lane & 3)) * 2;
#pragma unroll
        for (int t = 0; t < 4; ++t) { gf[t] = tr_frag(p + t * 32 * 64, 64); uf[t] = tr_frag(p + 32768 + t * 32 * 64, 64); }
        const lds_p pd = L + L_DN + (8 * g + (n16 >> 2)) * 512 + (32 * w + 4 * (lane & 3)) * 2;
#pragma unroll
        for (int t = 0; t < 2; ++t) df[t] = tr_frag(pd + t * 32, 512);
      }
      const bool up = lane & 8;
      unsigned r3w = lds0 + L_R3 + ((((kq * 2 + (up ? 1 : 0)) * 2 + cth) * 8 + (lane & 7)) * 16 + 4 * g) * 4;
      {
        const v4u ones = {0x3f803f80u, 0x3f803f80u, 0x3f803f80u, 0x3f803f80u};
        f32x4 cg = {0.f, 0.f, 0.f, 0.f}, cu = {0.f, 0.f, 0.f, 0.f};
#pragma unroll
        for (int t = 0; t < 4; ++t) { cg = mfma(gf[t], ones, cg); cu = mfma(uf[t], ones, cu); }
        *(LAS f32x4*)(size_t)r3w = f32x4{up ? cu[0] : cg[0], up ? cu[1] : cg[1], up ? cu[2] : cg[2], up ? cu[3] : cg[3]};
        BAR();
        if (w == 0) {
          const LAS float* r3 = (const LAS float*)(L + L_R3) + (lane >> 5) * 256 + ((lane >> 4) & 1) * 128 + (lane & 15);
          ((LAS float*)(L + L_CS))[lane] = (r3[0] + r3[512]) + (r3[1024] + r3[1536]);
        }
      }
      unsigned goff = WS_GH + 16 * w * 128 + lane * 16;
      unsigned hnw = lds0 + L_HN + (lane & 7) * 1056 + (16 * w + (lane >> 3)) * 8, hrow = lds0 + L_HN + w * 1056 + lane * 16, hnr = lds0 + L_HN + (lane & 7) * 1056 + (128 * kq + 8 * g) * 2;
      unsigned r3r = lds0 + L_R3 + (((lane >> 4) & 1) * 8 + 2 * (w & 3) + (lane >> 5)) * 64 + (lane & 15) * 4;
      unsigned hhw = lds0 + L_HH + (2 * (w & 3) + (lane >> 5)) * 96 + (lane & 31) * 2, hhr = lds0 + L_HH + (lane & 7) * 96 + g * 16;
      int foff = WS_FP + ((64 * nh + 8 * w + (up ? 4 : 0) + g) * FP_C + fg) * 128 + (lane & 7) * 16;
      asm volatile("" : "+v"(goff), "+v"(hnw), "+v"(hrow), "+v"(hnr), "+v"(r3w), "+v"(r3r), "+v"(hhw), "+v"(hhr), "+v"(foff));
      asm volatile("" : "+v"(gf[0]), "+v"(gf[1]), "+v"(gf[2]), "+v"(gf[3]), "+v"(uf[0]), "+v"(uf[1]), "+v"(uf[2]), "+v"(uf[3]));
      asm volatile("" : "+v"(df[0]), "+v"(df[1]));
      v4u h0, h1;
      poll2x4(ws, tag, goff, goff + 1024, h0, h1);
      *(LAS v2u*)(size_t)hnw = v2u{h0.x, h0.z};
      *(LAS v2u*)(size_t)(hnw + 64) = v2u{h1.x, h1.z};
      BAR();
      {
        const lds_p pa = (lds_p)(size_t)hnr;
        f32x4 ag = {0.f, 0.f, 0.f, 0.f}, au = {0.f, 0.f, 0.f, 0.f};
#pragma unroll
        for (int t = 0; t < 4; ++t) {
          const v4u a = lds_ld128(pa + t * 64);
          ag = mfma(gf[t], a, ag);
          au = mfma(uf[t], a, au);
        }
        {
          const v4u hr = lds_ld128((lds_p)(size_t)hrow);
          const float f[8] = {bf_lo(hr.x), bf_hi(hr.x), bf_lo(hr.y), bf_hi(hr.y), bf_lo(hr.z), bf_hi(hr.z), bf_lo(hr.w), bf_hi(hr.w)};
          const float c0 = asf(__builtin_amdgcn_readfirstlane(asu(f[0])));
          float s1 = 0.f, s2 = 0.f;
#pragma unroll
          for (int i = 0; i < 8; ++i) { const float d = f[i] - c0; s1 += d; s2 = fmaf(d, d, s2); }
          s1 += dpp<0xB1>(s1); s2 += dpp<0xB1>(s2);
          s1 += dpp<0x4E>(s1); s2 += dpp<0x4E>(s2);
          s1 += dpp<0x141>(s1); s2 += dpp<0x141>(s2);
          s1 += dpp<0x140>(s1); s2 += dpp<0x140>(s2);
          auto sw = __builtin_amdgcn_permlane16_swap(asu(s1), asu(s2), false, false);
          const float t = x32_sum(asf(sw[0]) + asf(sw[1])) * (1.0f / D);
          const float m1 = asf(__builtin_amdgcn_readlane(asu(t), 0)), m2 = asf(__builtin_amdgcn_readlane(asu(t), 16));
          const float var = rbf(fmaxf(fmaf(-m1, m1, m2), 0.f));
          const float den = rbf(__builtin_amdgcn_sqrtf(rbf(var + 1e-5f)));
          if (lane == 0) *(LAS v2u*)(L + L_ST + w * 8) = v2u{asu(rbf(c0 + m1)), asu(__builtin_amdgcn_rcpf(den))};
        }
        *(LAS f32x4*)(size_t)r3w = f32x4{up ? au[0] : ag[0], up ? au[1] : ag[1], up ? au[2] : ag[2], up ? au[3] : ag[3]};
      }
      BAR();
      if (w < 4) {
        const int bb = 2 * w + (lane >> 5), n32 = lane & 31;
        const LAS float* r3 = (const LAS float*)(size_t)r3r;
        const v2u stt = *(const LAS v2u*)(L + L_ST + bb * 8);
        const float csg = ((const LAS float*)(L + L_CS))[n32], csu = ((const LAS float*)(L + L_CS))[32 + n32];
        float gg = (r3[0] + r3[512]) + (r3[1024] + r3[1536]);
        float uu = (r3[256] + r3[768]) + (r3[1280] + r3[1792]);
        gg = rbf(fmaf(-asf(stt.x), csg, gg) * asf(stt.y));
        uu = rbf(fmaf(-asf(stt.x), csu, uu) * asf(stt.y));
        const float sg = rbf(__builtin_amdgcn_rcpf(1.0f + __builtin_amdgcn_exp2f(-1.4426950408889634f * gg)));
        *(LAS unsigned short*)(size_t)hhw = (unsigned short)pack_bf(sg * uu, 0.f);
      }
      BAR();
      {
        const v4u a = lds_ld128((lds_p)(size_t)hhr);
        const f32x4 z = {0.f, 0.f, 0.f, 0.f};
        const f32x4 d0 = mfma(df[0], a, z), d1 = mfma(df[1], a, z);
        __builtin_amdgcn_raw_buffer_store_b128(v4u{pack_bf(up ? d1[0] : d0[0], up ? d1[1] : d0[1]), tag, pack_bf(up ? d1[2] : d0[2], up ? d1[3] : d0[3]), tag}, rs, foff, 0, AUX_SC1);
      }
    }
  } else {
    const int hw = wv, kq = kq0;
    BAR();
    asm volatile("s_waitcnt vmcnt(7)" : "+v"(xa), "+v"(xb));
    {
      float fa[8] = {bf_lo(xa.x), bf_hi(xa.x), bf_lo(xa.y), bf_hi(xa.y), bf_lo(xa.z), bf_hi(xa.z), bf_lo(xa.w), bf_hi(xa.w)};
      float fb[8] = {bf_lo(xb.x), bf_hi(xb.x), bf_lo(xb.y), bf_hi(xb.y), bf_lo(xb.z), bf_hi(xb.z), bf_lo(xb.w), bf_hi(xb.w)};
      layernorm8(fa);
      layernorm8(fb);
      if (g == kq) {
        *(LAS v4u*)(L + L_HN + (2 * hw) * 288 + n16 * 16) = v4u{pack_bf(fa[0], fa[1]), pack_bf(fa[2], fa[3]), pack_bf(fa[4], fa[5]), pack_bf(fa[6], fa[7])};
        *(LAS v4u*)(L + L_HN + (2 * hw + 1) * 288 + n16 * 16) = v4u{pack_bf(fb[0], fb[1]), pack_bf(fb[2], fb[3]), pack_bf(fb[4], fb[5]), pack_bf(fb[6], fb[7])};
      }
    }
    asm volatile("s_waitcnt vmcnt(1)" : "+v"(ep), "+v"(pf) :: "memory");
    const unsigned tag = ep + 1;
    lds_arrive(flags + 0, lane);
    lds_wait(flags + 0, 4);
    {
      const lds_p pa = L + L_HN + (lane & 7) * 288 + g * 16;
      const lds_p pb = L + L_WQ + (8 * g + (n16 >> 2)) * 128 + (16 * hw + 4 * (lane & 3)) * 2;
      f32x4 acc0 = mfma(tr_frag(pb, 128), lds_ld128(pa), f32x4{0.f, 0.f, 0.f, 0.f});
      f32x4 acc1 = mfma(tr_frag(pb + 32 * 128, 128), lds_ld128(pa + 64), f32x4{0.f, 0.f, 0.f, 0.f});
      acc0 = mfma(tr_frag(pb + 64 * 128, 128), lds_ld128(pa + 128), acc0);
      acc1 = mfma(tr_frag(pb + 96 * 128, 128), lds_ld128(pa + 192), acc1);
      publish_tile(acc0 + acc1, rs, (kq < 2 ? WS_QP : WS_QP2) + ((kq & 1) * QP_K + 32 * j + 4 * ct8 + hw) * 128, QP_B * 128, tag, lane);
    }
    {
      const u64 t_pub = __builtin_amdgcn_s_memrealtime();
      for (;;) {
        const u64 now = __builtin_amdgcn_s_memrealtime();
        if ((unsigned)(now - t_start) >= QDELAY && (unsigned)(now - t_pub) >= QAFTER) break;
        __builtin_amdgcn_s_sleep(1);
      }
    }
    {
      const int off = WS_QP + (b * QP_B + 32 * j + 8 * hw) * 128 + lane * 16;
      v4u v[4];
      pollNx4<4, QP_K * 128, WS_QP2 - WS_QP>(ws, tag, off, v);
      const float q0 = (asf(v[0].x) + asf(v[1].x)) + (asf(v[2].x) + asf(v[3].x)), q1 = (asf(v[0].z) + asf(v[1].z)) + (asf(v[2].z) + asf(v[3].z));
      asm volatile("" : "+v"(pt));
      *(LAS unsigned*)(L + L_Q + hw * 320 + lane * 4) = pack_bf(q0, q1);
      if (hw == 0 && lane == 0) flags[1] = tag;
    }
    lds_arrive(flags + 6, lane);
    __builtin_amdgcn_s_setprio(0);
    if (hw == 0) {
      while (flags[2] < 8u) __builtin_amdgcn_s_sleep(1);
      asm volatile("" ::: "memory");
      const float mw = ((const LAS float*)(L + L_MAX))[lane & 31], lw = ((const LAS float*)(L + L_SUM))[lane & 31];
      float mx = max2(mw, dpp<0x124>(mw));
      mx = max2(mx, dpp<0x128>(mx));
      mx = x16_max(mx);
      const float f = __builtin_amdgcn_exp2f((mw - mx) * 1.4426950408889634f);
      float l = f * lw;
      l += dpp<0x124>(l);
      l += dpp<0x128>(l);
      l = x16_sum(l);
      if (lane < 32) ((LAS float*)(L + L_MSC))[(lane & 3) * 8 + (lane >> 2)] = f * __builtin_amdgcn_rcpf(l);
      lds_arrive(flags + 4, lane);
      if (lane < 4) {
        const float lse = mx * 1.4426950408889634f + __builtin_amdgcn_logf(l);
        __builtin_amdgcn_raw_buffer_store_b64(v2u{asu(lse), tag}, rs, WS_LS + (b * HKV + j) * 128 + (lane * 4 + s) * 8, 0, AUX_SC1);
      }
    }
    if (is_wo) {
      const int h = 4 * j + (r >> 2), ns = r & 3;
      while (flags[4] < DMAFLAG) __builtin_amdgcn_s_sleep(2);
      const unsigned char* src = (const unsigned char*)wo + ((size_t)(128 * h + 32 * hw + g) * D + 128 * ns) * 2 + n16 * 16;
#pragma unroll
      for (int i = 0; i < 8; ++i) glds16(src + (size_t)i * 4 * D * 2, lds0 + L_W + (32 * hw + 4 * i) * 256);
      asm volatile("s_waitcnt vmcnt(0)" ::: "memory");
      BAR();
    } else if (is_mlp) {
      while (flags[4] < DMAFLAG) __builtin_amdgcn_s_sleep(2);
      const int fg = 8 * j + ((r - 16) >> 1), nh = r & 1;
      const size_t go = ((size_t)(128 * hw + (lane >> 2)) * FF + 32 * fg) * 2 + (lane & 3) * 16;
#pragma unroll
      for (int i = 0; i < 8; ++i) {
        if (i >= 2) asm volatile("s_waitcnt vmcnt(3)" ::: "memory");
        glds16((const unsigned char*)wg + go + (size_t)i * 16 * FF * 2, lds0 + L_GT + (128 * hw + 16 * i) * 64);
        if (i >= 2) asm volatile("s_waitcnt vmcnt(3)" ::: "memory");
        glds16((const unsigned char*)wu + go + (size_t)i * 16 * FF * 2, lds0 + L_UP + (128 * hw + 16 * i) * 64);
      }
      const unsigned char* sd = (const unsigned char*)wd + ((size_t)(32 * fg + 8 * hw + (lane >> 5)) * D + 256 * nh) * 2 + (lane & 31) * 16;
#pragma unroll
      for (int i = 0; i < 4; ++i) {
        asm volatile("s_waitcnt vmcnt(5)" ::: "memory");
        glds16(sd + (size_t)i * 2 * D * 2, lds0 + L_DN + (8 * hw + 2 * i) * 512);
      }
      asm volatile("s_waitcnt vmcnt(0)" ::: "memory");
      BAR();
    }
  }
}

}

extern "C" void kernel_launch(void* const* d_in, const int* in_sizes, int n_in, void* d_out, int out_size, void* d_ws,
                              size_t ws_size, hipStream_t stream) {
  (void)in_sizes; (void)n_in; (void)out_size; (void)ws_size;
  (void)hipFuncSetAttribute((const void*)fused_decode, hipFuncAttributeMaxDynamicSharedMemorySize, LDS_BYTES);
  fused_decode<<<256, NTHR, LDS_BYTES, stream>>>(
      (const unsigned short*)d_in[0], (const unsigned short*)d_in[1], (const unsigned short*)d_in[2], (const unsigned short*)d_in[3],
      (const unsigned short*)d_in[4], (const unsigned short*)d_in[6], (const unsigned short*)d_in[7], (const unsigned short*)d_in[8],
      (unsigned short*)d_out, (unsigned char*)d_ws);
}
```

```cpp
#define DIAG 101
#include <hip/hip_runtime.h>
#include <hip/hip_bf16.h>
#include <stdint.h>

namespace {

typedef unsigned v4u __attribute__((ext_vector_type(4)));
typedef unsigned v2u __attribute__((ext_vector_type(2)));
typedef float f32x4 __attribute__((ext_vector_type(4)));
typedef __bf16 bf16x8 __attribute__((ext_vector_type(8)));
typedef unsigned long long u64;
#define LAS __attribute__((address_space(3)))
typedef LAS unsigned char* lds_p;

constexpr int D = 512, HQ = 32, HKV = 8, SK = 4096, DH = 128, WIN = 1024, FF = 1792;
constexpr int NTHR = 768;
constexpr int AUX_SC1 = 16;
constexpr unsigned SPIN_CAP = 1u << 16;
#define PD 3
#define THRN 10
#define DMAFLAG 2
#define ABL_NOQG 0
#define ABL_NOQP 0
#define ABL_NOW 0
#define WS_TOP 0
#define LSELATE 9
#define QBUF 0
#define ARR3 0
#define CODE_LINES 116
#define FDELAY 0
#define RDELAY 0
#define GSENT 0
#define TDELAY 0
#define UNTHR 0
#define SELFTRIG 0
#define WODELAY 0
#define RBF 1
#define DMAFLAG_MLP 2
#define MLPSLEEP 0
#define ABL_NOWM 0
#define QAFTER 60
#define HOLDQ 1
#define SLOWSTART 0
#define SLOWMASK 0
#define PRETOUCH 0
#define VFIRST 1
#define QDELAY 290

#define HPRIO 2

constexpr int WS_EPOCH = 0;
#define PAD 1
constexpr int QP_B = 256 + PAD, QP_K = 8 * QP_B;
constexpr int RP_C = 32 + PAD;
constexpr int FP_C = 56 + PAD;
constexpr int WS_QP = 1 << 16;
constexpr int WS_OP = WS_QP + 4 * QP_K * 128;
constexpr int WS_LS = WS_OP + (1 << 19);
constexpr int WS_RP = WS_LS + (1 << 15);
constexpr int WS_GH = WS_RP + 128 * RP_C * 128;
constexpr int WS_FP = WS_GH + (1 << 14);
constexpr int WS_END = WS_FP + 128 * FP_C * 128;
constexpr long long WS_STAMP = 64ll << 20;

constexpr int L_RED = 0;
constexpr int L_WQ = 0;
constexpr int L_Q = 16384;
constexpr int L_MAX = L_Q + 4 * 320;
constexpr int L_SUM = L_MAX + 128;
constexpr int L_FLAG = L_SUM + 128;
constexpr int L_MSC = L_FLAG + 64;
constexpr int L_LW = L_MSC + 128;
constexpr int L_CS = L_LW + 128;
constexpr int L_ST = L_CS + 256;
constexpr int L_OS = L_ST + 64;
constexpr int L_HN = L_OS + 8 * 288;
constexpr int L_R3 = L_HN + 8 * 1056;
constexpr int L_HH = L_R3 + 8192;
constexpr int L_W = ((L_HH + 8 * 96 + 255) / 256) * 256;
constexpr int L_GT = L_W, L_UP = L_W + 32768, L_DN = L_W + 65536;
constexpr int LDS_BYTES = L_W + 98304;
static_assert(LDS_BYTES <= 160 * 1024 && LDS_BYTES > 80 * 1024, "one workgroup per CU");

__device__ __forceinline__ float bf_lo(unsigned u) { return __builtin_bit_cast(float, u << 16); }
__device__ __forceinline__ float bf_hi(unsigned u) { return __builtin_bit_cast(float, u & 0xffff0000u); }
__device__ __forceinline__ unsigned pack_bf(float lo, float hi) {
  typedef __bf16 bf2 __attribute__((ext_vector_type(2)));
  bf2 r = {(__bf16)lo, (__bf16)hi};
  return __builtin_bit_cast(unsigned, r);
}
__device__ __forceinline__ float rbf(float f) { return bf_lo(pack_bf(f, f)); }
__device__ __forceinline__ float asf(unsigned u) { return __builtin_bit_cast(float, u); }
__device__ __forceinline__ unsigned asu(float f) { return __builtin_bit_cast(unsigned, f); }
template <int CTRL> __device__ __forceinline__ float dpp(float x) {
  return __builtin_bit_cast(float, __builtin_amdgcn_mov_dpp(__builtin_bit_cast(int, x), CTRL, 0xf, 0xf, true));
}
__device__ __forceinline__ float max3(float a, float b, float c) {
  float r;
  asm("v_max3_f32 %0, %1, %2, %3" : "=v"(r) : "v"(a), "v"(b), "v"(c));
  return r;
}
__device__ __forceinline__ float max2(float a, float b) {
  float r;
  asm("v_max_f32 %0, %1, %2" : "=v"(r) : "v"(a), "v"(b));
  return r;
}
__device__ __forceinline__ float x16_sum(float x) {
  auto s = __builtin_amdgcn_permlane16_swap(asu(x), asu(x), false, false);
  return asf(s[0]) + asf(s[1]);
}
__device__ __forceinline__ float x32_sum(float x) {
  auto s = __builtin_amdgcn_permlane32_swap(asu(x), asu(x), false, false);
  return asf(s[0]) + asf(s[1]);
}
__device__ __forceinline__ float x16_max(float x) {
  auto s = __builtin_amdgcn_permlane16_swap(asu(x), asu(x), false, false);
  return max2(asf(s[0]), asf(s[1]));
}
__device__ __forceinline__ float x32_max(float x) {
  auto s = __builtin_amdgcn_permlane32_swap(asu(x), asu(x), false, false);
  return max2(asf(s[0]), asf(s[1]));
}
__device__ __forceinline__ f32x4 mfma(v4u a, v4u b, f32x4 c) {
  return __builtin_amdgcn_mfma_f32_16x16x32_bf16(__builtin_bit_cast(bf16x8, a), __builtin_bit_cast(bf16x8, b), c, 0, 0, 0);
}
__device__ __forceinline__ v2u tr_read(lds_p p) {
  typedef short v4s __attribute__((ext_vector_type(4)));
  return __builtin_bit_cast(v2u, __builtin_amdgcn_ds_read_tr16_b64_v4i16((LAS v4s*)p));
}
__device__ __forceinline__ v4u tr_frag(lds_p p, int row_stride) {
  const v2u a = tr_read(p), b = tr_read(p + 4 * row_stride);
  return v4u{a.x, a.y, b.x, b.y};
}
__device__ __forceinline__ v4u lds_ld128(lds_p p) { return *(const LAS v4u*)p; }
__device__ __forceinline__ void glds16(const void* gsrc, unsigned lds_dst) {
  unsigned keep;
  asm volatile("s_mov_b32 %0, m0\n\ts_mov_b32 m0, %2\n\ts_nop 0\n\tglobal_load_lds_dwordx4 %1, off nt\n\ts_mov_b32 m0, %0"
               : "=&s"(keep) : "v"(gsrc), "s"(lds_dst) : "memory");
}
#define BAR() do { asm volatile("s_waitcnt lgkmcnt(0)" ::: "memory"); __builtin_amdgcn_s_barrier(); asm volatile("" ::: "memory"); } while (0)
__device__ __forceinline__ void lds_arrive(volatile LAS unsigned* p, int lane) {
  asm volatile("" ::: "memory");
  if (lane == 0) asm volatile("ds_add_u32 %0, %1" :: "v"((unsigned)(size_t)p), "v"(1u) : "memory");
  asm volatile("" ::: "memory");
}
__device__ __forceinline__ void lds_wait(volatile LAS unsigned* p, unsigned n) {
  while (*p < n) { }
  asm volatile("" ::: "memory");
}
typedef unsigned long long mask_t;
#define STICKY 1
#define MK(m) (STICKY ? (m) : ~0ull)
__device__ __forceinline__ mask_t bad4(v4u v, unsigned tag) { return __builtin_amdgcn_ballot_w64(v.y != tag) | __builtin_amdgcn_ballot_w64(v.w != tag); }
__device__ __forceinline__ mask_t bad2(v2u v, unsigned tag) { return __builtin_amdgcn_ballot_w64(v.y != tag); }
#define PL4(n_, o_, m_) "s_mov_b64 exec, %" #m_ "\n\tglobal_load_dwordx4 %" #n_ ", %" #o_ ", %[base] sc1\n\t"
#define PL2(n_, o_, m_) "s_mov_b64 exec, %" #m_ "\n\tglobal_load_dwordx2 %" #n_ ", %" #o_ ", %[base] sc1\n\t"
#define PLEND "s_mov_b64 exec, -1\n\ts_waitcnt vmcnt(0)"
__device__ __forceinline__ void poll1x4(const unsigned char* ws, unsigned tag, unsigned o0, v4u& a, bool nap) {
  a = v4u{0u, ~tag, 0u, ~tag};
  for (unsigned spins = 0; spins <= SPIN_CAP; ++spins) {
    const mask_t m0 = bad4(a, tag);
    if (m0 == 0) break;
    asm volatile(PL4(0, 1, 2) PLEND : "+v"(a) : "v"(o0), "s"(MK(m0)), [base] "s"(ws) : "memory");
    if (nap) __builtin_amdgcn_s_sleep(2);
  }
}
__device__ __forceinline__ void poll1x2(const unsigned char* ws, unsigned tag, unsigned o0, v2u& a, bool nap) {
  a = v2u{0u, ~tag};
  for (unsigned spins = 0; spins <= SPIN_CAP; ++spins) {
    const mask_t m0 = bad2(a, tag);
    if (m0 == 0) break;
    asm volatile(PL2(0, 1, 2) PLEND : "+v"(a) : "v"(o0), "s"(MK(m0)), [base] "s"(ws) : "memory");
    if (nap) __builtin_amdgcn_s_sleep(2);
  }
}
__device__ __forceinline__ void poll2x4(const unsigned char* ws, unsigned tag, unsigned o0, unsigned o1, v4u& a, v4u& b) {
  a = b = v4u{0u, ~tag, 0u, ~tag};
  for (unsigned spins = 0; spins <= SPIN_CAP; ++spins) {
    const mask_t m0 = bad4(a, tag), m1 = bad4(b, tag);
    if ((m0 | m1) == 0) break;
    asm volatile(PL4(0, 2, 4) PL4(1, 3, 5) PLEND : "+v"(a), "+v"(b) : "v"(o0), "v"(o1), "s"(MK(m0)), "s"(MK(m1)), [base] "s"(ws) : "memory");
  }
}
__device__ __forceinline__ void poll4x2(const unsigned char* ws, unsigned tag, unsigned o0, unsigned o1, unsigned o2, unsigned o3, v2u (&v)[4]) {
#pragma unroll
  for (int i = 0; i < 4; ++i) v[i] = v2u{0u, ~tag};
  for (unsigned spins = 0; spins <= SPIN_CAP; ++spins) {
    const mask_t m0 = bad2(v[0], tag), m1 = bad2(v[1], tag), m2 = bad2(v[2], tag), m3 = bad2(v[3], tag);
    if ((m0 | m1 | m2 | m3) == 0) break;
    asm volatile(PL2(0, 4, 8) PL2(1, 5, 9) PL2(2, 6, 10) PL2(3, 7, 11) PLEND
                 : "+v"(v[0]), "+v"(v[1]), "+v"(v[2]), "+v"(v[3])
                 : "v"(o0), "v"(o1), "v"(o2), "v"(o3), "s"(MK(m0)), "s"(MK(m1)), "s"(MK(m2)), "s"(MK(m3)), [base] "s"(ws) : "memory");
  }
}
__device__ __forceinline__ void poll5x2(const unsigned char* ws, unsigned tag, unsigned oa, unsigned ob, v2u (&v)[5]) {
  unsigned o[4];
#pragma unroll
  for (int i = 0; i < 4; ++i) o[i] = oa + i * 512;
#pragma unroll
  for (int i = 0; i < 5; ++i) v[i] = v2u{0u, ~tag};
  for (unsigned spins = 0; spins <= SPIN_CAP; ++spins) {
    mask_t m[5], any = 0;
#pragma unroll
    for (int i = 0; i < 5; ++i) { m[i] = bad2(v[i], tag); any |= m[i]; }
    if (any == 0) break;
    asm volatile(PL2(0, 5, 10) PL2(1, 6, 11) PL2(2, 7, 12) PL2(3, 8, 13) PL2(4, 9, 14) PLEND
                 : "+v"(v[0]), "+v"(v[1]), "+v"(v[2]), "+v"(v[3]), "+v"(v[4])
                 : "v"(o[0]), "v"(o[1]), "v"(o[2]), "v"(o[3]), "v"(ob),
                   "s"(MK(m[0])), "s"(MK(m[1])), "s"(MK(m[2])), "s"(MK(m[3])), "s"(MK(m[4])), [base] "s"(ws) : "memory");
  }
}
template <int N, int STEP>
__device__ __forceinline__ void pollNx4(const unsigned char* ws, unsigned tag, unsigned o0, v4u (&v)[8]) {
  unsigned o[8];
#pragma unroll
  for (int i = 0; i < 8; ++i) { v[i] = i < N ? v4u{0u, ~tag, 0u, ~tag} : v4u{0u, tag, 0u, tag}; o[i] = o0 + (i < N ? i : 0) * STEP; }
  for (unsigned spins = 0; spins <= SPIN_CAP; ++spins) {
    mask_t m[8], any = 0;
#pragma unroll
    for (int i = 0; i < 8; ++i) { m[i] = i < N ? bad4(v[i], tag) : 0; any |= m[i]; }
    if (any == 0) break;
    if constexpr (N == 4) {
      asm volatile(PL4(0, 4, 8) PL4(1, 5, 9) PL4(2, 6, 10) PL4(3, 7, 11) PLEND
                   : "+v"(v[0]), "+v"(v[1]), "+v"(v[2]), "+v"(v[3])
                   : "v"(o[0]), "v"(o[1]), "v"(o[2]), "v"(o[3]), "s"(MK(m[0])), "s"(MK(m[1])), "s"(MK(m[2])), "s"(MK(m[3])), [base] "s"(ws) : "memory");
    } else if constexpr (N == 7) {
      asm volatile(PL4(0, 7, 14) PL4(1, 8, 15) PL4(2, 9, 16) PL4(3, 10, 17) PL4(4, 11, 18) PL4(5, 12, 19) PL4(6, 13, 20) PLEND
                   : "+v"(v[0]), "+v"(v[1]), "+v"(v[2]), "+v"(v[3]), "+v"(v[4]), "+v"(v[5]), "+v"(v[6])
                   : "v"(o[0]), "v"(o[1]), "v"(o[2]), "v"(o[3]), "v"(o[4]), "v"(o[5]), "v"(o[6]),
                     "s"(MK(m[0])), "s"(MK(m[1])), "s"(MK(m[2])), "s"(MK(m[3])), "s"(MK(m[4])), "s"(MK(m[5])), "s"(MK(m[6])), [base] "s"(ws) : "memory");
    } else {
      asm volatile(PL4(0, 8, 16) PL4(1, 9, 17) PL4(2, 10, 18) PL4(3, 11, 19) PL4(4, 12, 20) PL4(5, 13, 21) PL4(6, 14, 22) PL4(7, 15, 23) PLEND
                   : "+v"(v[0]), "+v"(v[1]), "+v"(v[2]), "+v"(v[3]), "+v"(v[4]), "+v"(v[5]), "+v"(v[6]), "+v"(v[7])
                   : "v"(o[0]), "v"(o[1]), "v"(o[2]), "v"(o[3]), "v"(o[4]), "v"(o[5]), "v"(o[6]), "v"(o[7]),
                     "s"(MK(m[0])), "s"(MK(m[1])), "s"(MK(m[2])), "s"(MK(m[3])), "s"(MK(m[4])), "s"(MK(m[5])), "s"(MK(m[6])), "s"(MK(m[7])), [base] "s"(ws) : "memory");
    }
  }
}

__device__ __forceinline__ void layernorm8(float (&f)[8]) {
  const float c0 = asf(__builtin_amdgcn_readfirstlane(asu(f[0])));
  float s1 = 0.f, s2 = 0.f;
#pragma unroll
  for (int i = 0; i < 8; ++i) { const float d = f[i] - c0; s1 += d; s2 = fmaf(d, d, s2); }
  s1 += dpp<0xB1>(s1); s2 += dpp<0xB1>(s2);
  s1 += dpp<0x4E>(s1); s2 += dpp<0x4E>(s2);
  s1 += dpp<0x141>(s1); s2 += dpp<0x141>(s2);
  s1 += dpp<0x140>(s1); s2 += dpp<0x140>(s2);
  s1 = x16_sum(s1); s2 = x16_sum(s2);
  s1 = x32_sum(s1) * (1.0f / D); s2 = x32_sum(s2) * (1.0f / D);
  const float var = rbf(fmaxf(fmaf(-s1, s1, s2), 0.f));
  const float den = rbf(__builtin_amdgcn_sqrtf(rbf(var + 1e-5f)));
  const float inv = __builtin_amdgcn_rcpf(den), mb = rbf(c0 + s1);
#pragma unroll
  for (int i = 0; i < 8; ++i) f[i] = rbf(f[i] - mb) * inv;
}

__device__ __forceinline__ void publish_tile(f32x4 d, __amdgpu_buffer_rsrc_t rs, int base, int stride_b, unsigned tag, int lane) {
  const bool t = lane & 8;
  const int src = ((lane >> 3) + 8 * (lane & 1) + 16 * ((lane >> 1) & 3)) * 4;
  const unsigned v0 = (unsigned)__builtin_amdgcn_ds_bpermute(src, (int)asu(t ? d[2] : d[0]));
  const unsigned v1 = (unsigned)__builtin_amdgcn_ds_bpermute(src, (int)asu(t ? d[3] : d[1]));
  __builtin_amdgcn_raw_buffer_store_b128(v4u{v0, tag, v1, tag}, rs, base + (lane >> 3) * stride_b + (lane & 7) * 16, 0, AUX_SC1);
}

#define STAMP(i) do { if (i == 0) dg0 = __builtin_amdgcn_s_memrealtime(); if (wv >= 4 && i == DIAG) dg1 = __builtin_amdgcn_s_memrealtime(); if (wv == 0 && i + 100 == DIAG && (tid & 63) == 0) flags[9] = (unsigned)(__builtin_amdgcn_s_memrealtime() - dg0); } while (0)

#define FSTAMP
__device__ __forceinline__ void final_cell(const unsigned char* ws, unsigned tag, int cell, int lane, unsigned short* out, unsigned* ws_epoch, u64* tf) {
  const int p = lane & 7, row = lane >> 4;
  v4u hv;
  poll1x4(ws, tag, WS_GH + cell * 128 + p * 16, hv, true);
  float hsel = row == 0 ? bf_lo(hv.x) : row == 1 ? bf_hi(hv.x) : row == 2 ? bf_lo(hv.z) : bf_hi(hv.z);
  unsigned short* po = out + p * D + 4 * cell + row;
  asm volatile("" : "+v"(hsel), "+v"(po));
  if (FDELAY) __builtin_amdgcn_s_sleep(FDELAY);
  v4u v[8];
  pollNx4<7, 8 * 128>(ws, tag, WS_FP + (cell * FP_C + (lane >> 3)) * 128 + p * 16, v);
  FSTAMP;
  float a0 = 0.f, a1 = 0.f, a2 = 0.f, a3 = 0.f;
#pragma unroll
  for (int i = 0; i < 7; ++i) { a0 += bf_lo(v[i].x); a1 += bf_hi(v[i].x); a2 += bf_lo(v[i].z); a3 += bf_hi(v[i].z); }
  auto s01 = __builtin_amdgcn_permlane16_swap(asu(a0), asu(a1), false, false);
  auto s23 = __builtin_amdgcn_permlane16_swap(asu(a2), asu(a3), false, false);
  const float c0 = asf(s01[0]) + asf(s01[1]), c1 = asf(s23[0]) + asf(s23[1]);
  auto sc = __builtin_amdgcn_permlane32_swap(asu(c0), asu(c1), false, false);
  float e = asf(sc[0]) + asf(sc[1]);
  e += dpp<0x128>(e);
  if ((lane & 8) == 0) *po = (unsigned short)pack_bf(hsel + rbf(e), 0.f);
  if (cell == 0 && lane == 0) __hip_atomic_store(ws_epoch, tag, __ATOMIC_RELAXED, __HIP_MEMORY_SCOPE_AGENT);
}

__global__ void __launch_bounds__(NTHR, 3)
fused_decode(const unsigned short* __restrict__ x, const unsigned short* __restrict__ kc, const unsigned short* __restrict__ vc,
             const unsigned short* __restrict__ wq, const unsigned short* __restrict__ wo, const unsigned short* __restrict__ wg,
             const unsigned short* __restrict__ wu, const unsigned short* __restrict__ wd, unsigned short* __restrict__ out,
             unsigned char* __restrict__ ws) {
  unsigned long long code_base;
  asm volatile("s_getpc_b64 %0" : "=s"(code_base));
  code_base &= ~0xffull;
  extern __shared__ __attribute__((aligned(16))) unsigned char smem[];
  const lds_p L = (lds_p)smem;
  const unsigned lds0 = (unsigned)(size_t)L;
  const int tid = threadIdx.x, lane = tid & 63;
  const int wv = __builtin_amdgcn_readfirstlane(tid >> 6);
  const int w = wv - 4;
  const int c = blockIdx.x, j = c & 7, r = c >> 3;
  const int b = r >> 2, s = r & 3;
  const int g = lane >> 4, n16 = lane & 15;
  const bool is_wo = r < 16, is_mlp = (r >= 16) && (j < 7);
  const __amdgpu_buffer_rsrc_t rs = __builtin_amdgcn_make_buffer_rsrc((void*)ws, 0, WS_END, 0x00020000);
  volatile LAS unsigned* flags = (volatile LAS unsigned*)(L + L_FLAG);
  const int fcell = is_wo ? 16 * j + r : -1;
  u64 dg0 = 0, dg1 = 0;
  STAMP(0);

  const u64 t_start = __builtin_amdgcn_s_memrealtime();
  const int kq0 = r >> 3, ct8 = r & 7;
  v4u xa, xb; unsigned ep, pf, pt;
  asm volatile("" : "=v"(xa), "=v"(xb), "=v"(ep), "=v"(pf), "=v"(pt));
  if (wv < 4) {
    const int hw = wv;
    __builtin_amdgcn_s_setprio(HPRIO);
    const unsigned zero = 0;
    const unsigned xo = (2 * hw) * (D * 2) + lane * 16;
    asm volatile("global_load_dwordx4 %0, %1, %2" : "+v"(xa) : "v"(xo), "s"(x));
    asm volatile("global_load_dwordx4 %0, %1, %2 offset:1024" : "+v"(xb) : "v"(xo), "s"(x));
    const unsigned char* src = (const unsigned char*)wq + ((size_t)(128 * kq0 + 32 * hw + (lane >> 3)) * (HQ * DH) + 512 * j + 64 * ct8) * 2 + (lane & 7) * 16;
#pragma unroll
    for (int i = 0; i < 4; ++i) glds16(src + (size_t)i * 8 * (HQ * DH) * 2, lds0 + L_WQ + (32 * hw + 8 * i) * 128);
    asm volatile("global_load_dword %0, %1, %2" : "+v"(ep) : "v"(zero), "s"(ws));
    const unsigned po = min((r * 4 + hw) * 2 + (lane & 1), CODE_LINES - 1) * 128;
    asm volatile("global_load_dword %0, %1, %2" : "+v"(pf) : "v"(po), "s"(code_base));
  }
  int wv2 = wv;
  asm volatile("" : "+s"(wv2));

  if (__builtin_expect(wv2 >= 4, 1)) {
    const size_t tile = ((size_t)(b * HKV + j) * SK + (SK - WIN) + 256 * s + 32 * w) * (DH * 2);
    const unsigned char* kb = (const unsigned char*)kc + tile + 4096;
    const unsigned char* vb = (const unsigned char*)vc + tile;
    const unsigned koff = (lane & 7) * 256 + (2 * g + ((lane >> 3) & 1)) * 16;
    const unsigned voff = (16 * (g >> 1) + 4 * (g & 1)) * 256 + n16 * 16;
    v4u K[8], V[8];
#define LDK(i, imm) asm volatile("global_load_dwordx4 %0, %1, %2 offset:" #imm " nt" : "=v"(K[i]) : "v"(koff), "s"(kb))
#define LDV(i, imm) asm volatile("global_load_dwordx4 %0, %1, %2 offset:" #imm " nt" : "=v"(V[i]) : "v"(voff), "s"(vb))
#define THR(n) do { if (n >= PD && (n < THRN || (HOLDQ && __builtin_amdgcn_readfirstlane(flags[6]) < 4))) asm volatile("s_waitcnt vmcnt(%0)" :: "i"(PD - 1)); } while (0)
    LDK(0, -4096); LDK(1, -3968); LDV(0, 0);
    if (w == 0 && lane < 8) flags[lane] = 0;
    BAR();
    THR(3); LDK(2, -2048); THR(4); LDK(3, -1920); THR(5); LDK(4, 0); THR(6); LDK(5, 128); THR(7); LDK(6, 2048); THR(8); LDK(7, 2176);
    THR(9); LDV(1, 256); THR(10); LDV(2, 512); THR(11); LDV(3, 768);
    THR(12); LDV(4, 2048); THR(13); LDV(5, 2304); THR(14); LDV(6, 2560); THR(15); LDV(7, 2816);
    if (WODELAY) lds_arrive(flags + 3, lane);
    STAMP(1);
    while (flags[6] < 4u) __builtin_amdgcn_s_sleep(1);
    asm volatile("" ::: "memory");
    STAMP(2);
    const unsigned tag = flags[1];
    const lds_p qa = L + L_Q + (n16 & 3) * 320 + (2 * g + ((n16 >> 2) & 1)) * 16;
    const v4u q0 = lds_ld128(qa), q1 = lds_ld128(qa + 128);
    asm volatile("s_waitcnt vmcnt(%8)" : "+v"(K[0]), "+v"(K[1]), "+v"(K[2]), "+v"(K[3]), "+v"(K[4]), "+v"(K[5]), "+v"(K[6]), "+v"(K[7]) : "i"(VFIRST ? 7 : 8));
    f32x4 sa[4];
#pragma unroll
    for (int o = 0; o < 4; ++o) {
      sa[o] = mfma(K[2 * o], q0, f32x4{0.f, 0.f, 0.f, 0.f});
      sa[o] = mfma(K[2 * o + 1], q1, sa[o]);
    }
    float sc[8];
    {
      const bool par = lane & 4;
      float raw[8];
#pragma unroll
      for (int p = 0; p < 2; ++p)
#pragma unroll
        for (int i = 0; i < 4; ++i) {
          auto sw = __builtin_amdgcn_permlane32_swap(asu(sa[p][i]), asu(sa[p + 2][i]), false, false);
          const float a = par ? asf(sw[1]) : asf(sw[0]);
          raw[4 * p + i] = a + dpp<0x124>(a);
        }
#pragma unroll
      for (int i = 0; i < 8; i += 2) {
        const unsigned u = pack_bf(raw[i], raw[i + 1]);
        const unsigned t = pack_bf(bf_lo(u) * 0.08838834764831845f, bf_hi(u) * 0.08838834764831845f);
        sc[i] = bf_lo(t); sc[i + 1] = bf_hi(t);
      }
    }
    float m = max3(max3(sc[0], sc[1], sc[2]), max3(sc[3], sc[4], sc[5]), max2(sc[6], sc[7]));
    m = x32_max(x16_max(m));
    float pe[8], ls;
    {
      const float mc = m * 1.4426950408889634f;
#pragma unroll
      for (int i = 0; i < 8; ++i) pe[i] = __builtin_amdgcn_exp2f(fmaf(sc[i], 1.4426950408889634f, -mc));
      ls = ((pe[0] + pe[1]) + (pe[2] + pe[3])) + ((pe[4] + pe[5]) + (pe[6] + pe[7]));
    }
    v4u PA[4];
    {
      const v4u P = {pack_bf(pe[0], pe[1]), pack_bf(pe[2], pe[3]), pack_bf(pe[4], pe[5]), pack_bf(pe[6], pe[7])};
#pragma unroll
      for (int mq = 0; mq < 4; ++mq) {
        const bool on = (n16 >> 2) == mq;
        PA[mq] = v4u{on ? P.x : 0u, on ? P.y : 0u, on ? P.z : 0u, on ? P.w : 0u};
      }
    }
    ls = x32_sum(x16_sum(ls));
    if (lane < 4) {
      ((LAS float*)(L + L_MAX))[w * 4 + lane] = m;
      ((LAS float*)(L + L_SUM))[w * 4 + lane] = ls;
    }
    lds_arrive(flags + 2, lane);
    STAMP(4);
    unsigned T[8][4];
#define XPOSE(n_)                                                                                         \
  _Pragma("unroll") for (int mm = 0; mm < 4; ++mm) {                                                      \
    T[2 * mm][n_] = __builtin_amdgcn_perm(V[2 * n_ + 1][mm], V[2 * n_][mm], 0x05040100u);                 \
    T[2 * mm + 1][n_] = __builtin_amdgcn_perm(V[2 * n_ + 1][mm], V[2 * n_][mm], 0x07060302u);             \
  }                                                                                                       \
  asm volatile("" : "+v"(T[0][n_]), "+v"(T[1][n_]), "+v"(T[2][n_]), "+v"(T[3][n_]), "+v"(T[4][n_]), "+v"(T[5][n_]), "+v"(T[6][n_]), "+v"(T[7][n_]));
    int ooff = WS_OP + ((b * HQ + 4 * j + (w >> 1)) * 4 + s) * 512 + (w & 1) * 256 + n16 * 16;
    const int dr = 64 * (w & 1) + 4 * n16 + g, dw = 8 * n16 + g;
    unsigned redr_a = lds0 + L_RED + ((w >> 1) * 128 + (dr ^ ((dr >> 5) << 1))) * 4;
    unsigned redw_a = lds0 + L_RED + w * 2048 + (dw ^ ((dw >> 5) << 1)) * 4;
    const int h = 4 * j + (r >> 2), ns = r & 3;
    unsigned doff = WS_OP + (w * HQ + h) * 4 * 512 + lane * 8;
    unsigned loff = WS_LS + (w * HKV + j) * 128 + ((h & 3) * 4 + (lane & 3)) * 8;
    int roff = WS_RP + ((32 * ns + 4 * w + g) * RP_C + h) * 128 + (lane & 7) * 16;
    unsigned osw = lds0 + L_OS + w * 288 + lane * 4, osr = lds0 + L_OS + (lane & 7) * 288 + g * 16;
    unsigned wfa = lds0 + L_W + (8 * g + (n16 >> 2)) * 256 + (16 * w + 4 * (lane & 3)) * 2;
    asm volatile("" : "+v"(PA[0]), "+v"(PA[1]), "+v"(PA[2]), "+v"(PA[3]), "+v"(ooff), "+v"(redr_a), "+v"(redw_a));
    asm volatile("" : "+v"(doff), "+v"(loff), "+v"(roff), "+v"(osw), "+v"(osr), "+v"(wfa));
    asm volatile("s_waitcnt vmcnt(6)" : "+v"(V[0]), "+v"(V[1])); XPOSE(0)
    asm volatile("s_waitcnt vmcnt(4)" : "+v"(V[2]), "+v"(V[3])); XPOSE(1)
    asm volatile("s_waitcnt vmcnt(2)" : "+v"(V[4]), "+v"(V[5])); XPOSE(2)
    asm volatile("s_waitcnt vmcnt(0)" : "+v"(V[6]), "+v"(V[7]));
    STAMP(5);
    XPOSE(3)
    {
      f32x4 oa = {0.f, 0.f, 0.f, 0.f}, ob = {0.f, 0.f, 0.f, 0.f};
#pragma unroll
      for (int i = 0; i < 4; ++i) {
        oa = mfma(PA[i], v4u{T[i][0], T[i][1], T[i][2], T[i][3]}, oa);
        ob = mfma(PA[i], v4u{T[i + 4][0], T[i + 4][1], T[i + 4][2], T[i + 4][3]}, ob);
      }
      LAS float* red = (LAS float*)(size_t)redw_a;
#pragma unroll
      for (int hh = 0; hh < 4; ++hh) { red[hh * 128] = oa[hh]; *(LAS float*)(size_t)((redw_a ^ 16) + hh * 512) = ob[hh]; }
    }
    lds_arrive(flags + 4, lane);
    STAMP(6);
    const LAS float* redr = (const LAS float*)(size_t)redr_a;
    lds_wait(flags + 4, 9);
    {
      const f32x4 ma = *(const LAS f32x4*)(L + L_MSC + (w >> 1) * 32), mb = *(const LAS f32x4*)(L + L_MSC + (w >> 1) * 32 + 16);
      float o = redr[0] * ma[0], o2 = redr[512] * ma[1];
      o = fmaf(redr[2 * 512], ma[2], o); o2 = fmaf(redr[3 * 512], ma[3], o2);
      o = fmaf(redr[4 * 512], mb[0], o); o2 = fmaf(redr[5 * 512], mb[1], o2);
      o = fmaf(redr[6 * 512], mb[2], o); o2 = fmaf(redr[7 * 512], mb[3], o2);
      o += o2;
      auto s16 = __builtin_amdgcn_permlane16_swap(asu(o), asu(o), false, false);
      const unsigned pr = pack_bf(asf(s16[0]), asf(s16[1]));
      auto s32 = __builtin_amdgcn_permlane32_swap(pr, pr, false, false);
      if (lane < 16) __builtin_amdgcn_raw_buffer_store_b128(v4u{s32[0], tag, s32[1], tag}, rs, ooff, 0, AUX_SC1);
    }
    STAMP(7);

    if (is_wo) {
      BAR();
      v4u wf[4];
      {
        const lds_p p = (lds_p)(size_t)wfa;
#pragma unroll
        for (int t = 0; t < 4; ++t) wf[t] = tr_frag(p + t * 32 * 256, 256);
      }
      if (TDELAY) __builtin_amdgcn_s_sleep(TDELAY);
      v2u dv[5];
      poll5x2(ws, tag, doff, loff, dv);
      STAMP(8);
      {
        const float lse = asf(dv[4].x);
        float mx = max2(lse, dpp<0xB1>(lse));
        mx = max2(mx, dpp<0x4E>(mx));
        const float e = __builtin_amdgcn_exp2f(lse - mx);
        float es = e + dpp<0xB1>(e);
        es += dpp<0x4E>(es);
        const float wt = e * __builtin_amdgcn_rcpf(es);
        const float e0 = dpp<0x00>(wt), e1 = dpp<0x55>(wt), e2 = dpp<0xAA>(wt), e3 = dpp<0xFF>(wt);
        const float o0 = fmaf(e0, bf_lo(dv[0].x), e1 * bf_lo(dv[1].x)) + fmaf(e2, bf_lo(dv[2].x), e3 * bf_lo(dv[3].x));
        const float o1 = fmaf(e0, bf_hi(dv[0].x), e1 * bf_hi(dv[1].x)) + fmaf(e2, bf_hi(dv[2].x), e3 * bf_hi(dv[3].x));
        *(LAS unsigned*)(size_t)osw = pack_bf(o0, o1);
      }
      BAR();
      {
        const lds_p pa = (lds_p)(size_t)osr;
        f32x4 acc0 = mfma(wf[0], lds_ld128(pa), f32x4{0.f, 0.f, 0.f, 0.f});
        f32x4 acc1 = mfma(wf[1], lds_ld128(pa + 64), f32x4{0.f, 0.f, 0.f, 0.f});
        acc0 = mfma(wf[2], lds_ld128(pa + 128), acc0);
        acc1 = mfma(wf[3], lds_ld128(pa + 192), acc1);
        acc0 += acc1;
        if ((lane & 8) == 0) __builtin_amdgcn_raw_buffer_store_b128(v4u{pack_bf(acc0[0], acc0[1]), tag, pack_bf(acc0[2], acc0[3]), tag}, rs, roff, 0, AUX_SC1);
      }
      STAMP(9);
      if (w == 0) {
        const int rho = 16 * j + r;
        const unsigned xv = *(const unsigned*)(x + (lane & 7) * D + 4 * rho + (g & 2));
        float xsel = (g & 1) ? bf_hi(xv) : bf_lo(xv);
        asm volatile("" : "+v"(xsel));
        if (RDELAY) __builtin_amdgcn_s_sleep(RDELAY);
        v4u v[8];
        pollNx4<4, 8 * 128>(ws, tag, WS_RP + (rho * RP_C + (lane >> 3)) * 128 + (lane & 7) * 16, v);
        STAMP(10);
        const float a0 = (bf_lo(v[0].x) + bf_lo(v[1].x)) + (bf_lo(v[2].x) + bf_lo(v[3].x)), a1 = (bf_hi(v[0].x) + bf_hi(v[1].x)) + (bf_hi(v[2].x) + bf_hi(v[3].x));
        const float a2 = (bf_lo(v[0].z) + bf_lo(v[1].z)) + (bf_lo(v[2].z) + bf_lo(v[3].z)), a3 = (bf_hi(v[0].z) + bf_hi(v[1].z)) + (bf_hi(v[2].z) + bf_hi(v[3].z));
        auto s01 = __builtin_amdgcn_permlane16_swap(asu(a0), asu(a1), false, false);
        auto s23 = __builtin_amdgcn_permlane16_swap(asu(a2), asu(a3), false, false);
        auto sc = __builtin_amdgcn_permlane32_swap(asu(asf(s01[0]) + asf(s01[1])), asu(asf(s23[0]) + asf(s23[1])), false, false);
        float e = asf(sc[0]) + asf(sc[1]);
        e += dpp<0x128>(e);
        const unsigned hb = pack_bf(xsel + rbf(e), 0.f) & 0xffffu;
        auto hp = __builtin_amdgcn_permlane16_swap(hb, hb, false, false);
        const unsigned hq = (unsigned)__builtin_amdgcn_ds_bpermute((((lane >> 1) & 7) + 32 * (lane & 1)) * 4, (int)(hp[0] | (hp[1] << 16)));
        if (lane < 16) __builtin_amdgcn_raw_buffer_store_b64(v2u{hq, tag}, rs, WS_GH + rho * 128 + lane * 8, 0, AUX_SC1);
        STAMP(11);
      } else if (w == 1) {
        u64 tf_ = 0;
        final_cell(ws, tag, fcell, lane, out, (unsigned*)(ws + WS_EPOCH), &tf_);
        STAMP(15);
        if (c == 0) {
          const u64 tend = __builtin_amdgcn_s_memrealtime();
          const u64 dlt = DIAG >= 100 ? (u64)flags[9] : dg1 - dg0;
          const u64 until = tend + 4 * dlt;
          for (int sp = 0; sp < 100000 && __builtin_amdgcn_s_memrealtime() < until; ++sp) __builtin_amdgcn_s_sleep(1);
        }
      }
    } else if (is_mlp) {
      const int fg = 8 * j + ((r - 16) >> 1), nh = r & 1;
      const int kq = w >> 1, cth = w & 1;
      BAR();
      STAMP(8);
      v4u gf[4], uf[4], df[2];
      {
        const lds_p p = L + L_GT + (128 * kq + 8 * g + (n16 >> 2)) * 64 + (16 * cth + 4 * (lane & 3)) * 2;
#pragma unroll
        for (int t = 0; t < 4; ++t) { gf[t] = tr_frag(p + t * 32 * 64, 64); uf[t] = tr_frag(p + 32768 + t * 32 * 64, 64); }
        const lds_p pd = L + L_DN + (8 * g + (n16 >> 2)) * 512 + (32 * w + 4 * (lane & 3)) * 2;
#pragma unroll
        for (int t = 0; t < 2; ++t) df[t] = tr_frag(pd + t * 32, 512);
      }
      const bool up = lane & 8;
      unsigned r3w = lds0 + L_R3 + ((((kq * 2 + (up ? 1 : 0)) * 2 + cth) * 8 + (lane & 7)) * 16 + 4 * g) * 4;
      {
        const v4u ones = {0x3f803f80u, 0x3f803f80u, 0x3f803f80u, 0x3f803f80u};
        f32x4 cg = {0.f, 0.f, 0.f, 0.f}, cu = {0.f, 0.f, 0.f, 0.f};
#pragma unroll
        for (int t = 0; t < 4; ++t) { cg = mfma(gf[t], ones, cg); cu = mfma(uf[t], ones, cu); }
        *(LAS f32x4*)(size_t)r3w = f32x4{up ? cu[0] : cg[0], up ? cu[1] : cg[1], up ? cu[2] : cg[2], up ? cu[3] : cg[3]};
        BAR();
        if (w == 0) {
          const LAS float* r3 = (const LAS float*)(L + L_R3) + (lane >> 5) * 256 + ((lane >> 4) & 1) * 128 + (lane & 15);
          ((LAS float*)(L + L_CS))[lane] = (r3[0] + r3[512]) + (r3[1024] + r3[1536]);
        }
      }
      unsigned goff = WS_GH + 16 * w * 128 + lane * 16;
      unsigned hnw = lds0 + L_HN + (lane & 7) * 1056 + (16 * w + (lane >> 3)) * 8, hrow = lds0 + L_HN + w * 1056 + lane * 16, hnr = lds0 + L_HN + (lane & 7) * 1056 + (128 * kq + 8 * g) * 2;
      unsigned r3r = lds0 + L_R3 + (((lane >> 4) & 1) * 8 + 2 * (w & 3) + (lane >> 5)) * 64 + (lane & 15) * 4;
      unsigned hhw = lds0 + L_HH + (2 * (w & 3) + (lane >> 5)) * 96 + (lane & 31) * 2, hhr = lds0 + L_HH + (lane & 7) * 96 + g * 16;
      int foff = WS_FP + ((64 * nh + 8 * w + (up ? 4 : 0) + g) * FP_C + fg) * 128 + (lane & 7) * 16;
      asm volatile("" : "+v"(goff), "+v"(hnw), "+v"(hrow), "+v"(hnr), "+v"(r3w), "+v"(r3r), "+v"(hhw), "+v"(hhr), "+v"(foff));
      asm volatile("" : "+v"(gf[0]), "+v"(gf[1]), "+v"(gf[2]), "+v"(gf[3]), "+v"(uf[0]), "+v"(uf[1]), "+v"(uf[2]), "+v"(uf[3]));
      asm volatile("" : "+v"(df[0]), "+v"(df[1]));
      v4u h0, h1;
      if (GSENT) {
        v2u sv;
        poll1x2(ws, tag, WS_GH + (16 * w + (fg & 15)) * 128, sv, true);
      }
      poll2x4(ws, tag, goff, goff + 1024, h0, h1);
      STAMP(9);
      *(LAS v2u*)(size_t)hnw = v2u{h0.x, h0.z};
      *(LAS v2u*)(size_t)(hnw + 64) = v2u{h1.x, h1.z};
      BAR();
      {
        const lds_p pa = (lds_p)(size_t)hnr;
        f32x4 ag = {0.f, 0.f, 0.f, 0.f}, au = {0.f, 0.f, 0.f, 0.f};
#pragma unroll
        for (int t = 0; t < 4; ++t) {
          const v4u a = lds_ld128(pa + t * 64);
          ag = mfma(gf[t], a, ag);
          au = mfma(uf[t], a, au);
        }
        {
          const v4u hr = lds_ld128((lds_p)(size_t)hrow);
          const float f[8] = {bf_lo(hr.x), bf_hi(hr.x), bf_lo(hr.y), bf_hi(hr.y), bf_lo(hr.z), bf_hi(hr.z), bf_lo(hr.w), bf_hi(hr.w)};
          const float c0 = asf(__builtin_amdgcn_readfirstlane(asu(f[0])));
          float s1 = 0.f, s2 = 0.f;
#pragma unroll
          for (int i = 0; i < 8; ++i) { const float d = f[i] - c0; s1 += d; s2 = fmaf(d, d, s2); }
          s1 += dpp<0xB1>(s1); s2 += dpp<0xB1>(s2);
          s1 += dpp<0x4E>(s1); s2 += dpp<0x4E>(s2);
          s1 += dpp<0x141>(s1); s2 += dpp<0x141>(s2);
          s1 += dpp<0x140>(s1); s2 += dpp<0x140>(s2);
          auto sw = __builtin_amdgcn_permlane16_swap(asu(s1), asu(s2), false, false);
          const float t = x32_sum(asf(sw[0]) + asf(sw[1])) * (1.0f / D);
          const float m1 = asf(__builtin_amdgcn_readlane(asu(t), 0)), m2 = asf(__builtin_amdgcn_readlane(asu(t), 16));
          const float var = rbf(fmaxf(fmaf(-m1, m1, m2), 0.f));
          const float den = rbf(__builtin_amdgcn_sqrtf(rbf(var + 1e-5f)));
          if (lane == 0) *(LAS v2u*)(L + L_ST + w * 8) = v2u{asu(rbf(c0 + m1)), asu(__builtin_amdgcn_rcpf(den))};
        }
        *(LAS f32x4*)(size_t)r3w = f32x4{up ? au[0] : ag[0], up ? au[1] : ag[1], up ? au[2] : ag[2], up ? au[3] : ag[3]};
      }
      BAR();
      if (w < 4) {
        const int bb = 2 * w + (lane >> 5), n32 = lane & 31;
        const LAS float* r3 = (const LAS float*)(size_t)r3r;
        const v2u stt = *(const LAS v2u*)(L + L_ST + bb * 8);
        const float csg = ((const LAS float*)(L + L_CS))[n32], csu = ((const LAS float*)(L + L_CS))[32 + n32];
        float gg = (r3[0] + r3[512]) + (r3[1024] + r3[1536]);
        float uu = (r3[256] + r3[768]) + (r3[1280] + r3[1792]);
        gg = rbf(fmaf(-asf(stt.x), csg, gg) * asf(stt.y));
        uu = rbf(fmaf(-asf(stt.x), csu, uu) * asf(stt.y));
        const float sg = rbf(__builtin_amdgcn_rcpf(1.0f + __builtin_amdgcn_exp2f(-1.4426950408889634f * gg)));
        *(LAS unsigned short*)(size_t)hhw = (unsigned short)pack_bf(sg * uu, 0.f);
      }
      BAR();
      {
        const v4u a = lds_ld128((lds_p)(size_t)hhr);
        const f32x4 z = {0.f, 0.f, 0.f, 0.f};
        const f32x4 d0 = mfma(df[0], a, z), d1 = mfma(df[1], a, z);
        __builtin_amdgcn_raw_buffer_store_b128(v4u{pack_bf(up ? d1[0] : d0[0], up ? d1[1] : d0[1]), tag, pack_bf(up ? d1[2] : d0[2], up ? d1[3] : d0[3]), tag}, rs, foff, 0, AUX_SC1);
      }
      STAMP(10);
    }
  } else {
    const int hw = wv, kq = kq0;
    BAR();
    asm volatile("s_waitcnt vmcnt(6)" : "+v"(xa), "+v"(xb));
    if (PRETOUCH) {
      const int k = lane & 3;
      const unsigned char* p;
      if (hw == 0) p = k == 0 ? (const unsigned char*)out : (const unsigned char*)vc + ((size_t)(b * HKV + j) * SK + (SK - WIN) + 256 * s) * (DH * 2);
      else if (hw == 1) p = (const unsigned char*)wo + (size_t)(128 * (4 * j + ((r >> 2) & 3))) * D * 2;
      else if (hw == 2) p = (k < 2 ? (const unsigned char*)wg : (const unsigned char*)wu) + (k & 1) * (D * FF * 2 - 128);
      else p = k < 2 ? (const unsigned char*)wd + (k & 1) * (D * FF * 2 - 128) : (const unsigned char*)ws + (k == 2 ? WS_RP : WS_FP);
      asm volatile("global_load_dword %0, %1, off" : "+v"(pt) : "v"(p));
    }
    STAMP(1);
    {
      float fa[8] = {bf_lo(xa.x), bf_hi(xa.x), bf_lo(xa.y), bf_hi(xa.y), bf_lo(xa.z), bf_hi(xa.z), bf_lo(xa.w), bf_hi(xa.w)};
      float fb[8] = {bf_lo(xb.x), bf_hi(xb.x), bf_lo(xb.y), bf_hi(xb.y), bf_lo(xb.z), bf_hi(xb.z), bf_lo(xb.w), bf_hi(xb.w)};
      layernorm8(fa);
      layernorm8(fb);
      if (g == kq) {
        *(LAS v4u*)(L + L_HN + (2 * hw) * 288 + n16 * 16) = v4u{pack_bf(fa[0], fa[1]), pack_bf(fa[2], fa[3]), pack_bf(fa[4], fa[5]), pack_bf(fa[6], fa[7])};
        *(LAS v4u*)(L + L_HN + (2 * hw + 1) * 288 + n16 * 16) = v4u{pack_bf(fb[0], fb[1]), pack_bf(fb[2], fb[3]), pack_bf(fb[4], fb[5]), pack_bf(fb[6], fb[7])};
      }
    }
    asm volatile("s_waitcnt vmcnt(%2)" : "+v"(ep), "+v"(pf) : "i"(PRETOUCH != 0 ? 1 : 0) : "memory");
    if (SLOWSTART && (r & SLOWMASK) == SLOWMASK) { const u64 t0 = __builtin_amdgcn_s_memrealtime(); while ((unsigned)(__builtin_amdgcn_s_memrealtime() - t0) < (unsigned)SLOWSTART) __builtin_amdgcn_s_sleep(1); }
    STAMP(2);
    const unsigned tag = ep + 1;
    lds_arrive(flags + 0, lane);
    lds_wait(flags + 0, 4);
    {
      const lds_p pa = L + L_HN + (lane & 7) * 288 + g * 16;
      const lds_p pb = L + L_WQ + (8 * g + (n16 >> 2)) * 128 + (16 * hw + 4 * (lane & 3)) * 2;
      f32x4 acc0 = mfma(tr_frag(pb, 128), lds_ld128(pa), f32x4{0.f, 0.f, 0.f, 0.f});
      f32x4 acc1 = mfma(tr_frag(pb + 32 * 128, 128), lds_ld128(pa + 64), f32x4{0.f, 0.f, 0.f, 0.f});
      acc0 = mfma(tr_frag(pb + 64 * 128, 128), lds_ld128(pa + 128), acc0);
      acc1 = mfma(tr_frag(pb + 96 * 128, 128), lds_ld128(pa + 192), acc1);
      if (!ABL_NOQP) publish_tile(acc0 + acc1, rs, WS_QP + (kq * QP_K + 32 * j + 4 * ct8 + hw) * 128, QP_B * 128, tag, lane);
    }
    STAMP(3);
    const u64 t_pub = QAFTER ? __builtin_amdgcn_s_memrealtime() : 0;
    for (;;) {
      const u64 now = __builtin_amdgcn_s_memrealtime();
      if ((unsigned)(now - t_start) >= (unsigned)QDELAY && (QAFTER == 0 || (unsigned)(now - t_pub) >= (unsigned)QAFTER)) break;
      __builtin_amdgcn_s_sleep(1);
    }
    if (UNTHR && !SELFTRIG) lds_arrive(flags + 7, lane);
    {
      const int off = WS_QP + (b * QP_B + 32 * j + 8 * hw) * 128 + lane * 16;
      v4u v[8];
      if (QBUF) {
        for (unsigned spins = 0;; ++spins) {
          asm volatile("" ::: "memory");
#pragma unroll
          for (int i = 0; i < 4; ++i) v[i] = __builtin_amdgcn_raw_buffer_load_b128(rs, off + i * QP_K * 128, 0, AUX_SC1);
          if ((bad4(v[0], tag) | bad4(v[1], tag) | bad4(v[2], tag) | bad4(v[3], tag)) == 0 || spins > SPIN_CAP) break;
        }
      } else if (ABL_NOQG) { for (int i = 0; i < 4; ++i) v[i] = v4u{0u, 0u, 0u, 0u}; } else pollNx4<4, QP_K * 128>(ws, tag, off, v);
      const float q0 = (asf(v[0].x) + asf(v[1].x)) + (asf(v[2].x) + asf(v[3].x)), q1 = (asf(v[0].z) + asf(v[1].z)) + (asf(v[2].z) + asf(v[3].z));
      asm volatile("" : "+v"(pt));
      *(LAS unsigned*)(L + L_Q + hw * 320 + lane * 4) = pack_bf(q0, q1);
      if (hw == 0 && lane == 0) flags[1] = tag;
    }
    lds_arrive(flags + 6, lane);
    STAMP(4);
    __builtin_amdgcn_s_setprio(0);
    if (hw == 0) {
      while (flags[2] < 8u) __builtin_amdgcn_s_sleep(1);
      asm volatile("" ::: "memory");
      const float mw = ((const LAS float*)(L + L_MAX))[lane & 31], lw = ((const LAS float*)(L + L_SUM))[lane & 31];
      float mx = max2(mw, dpp<0x124>(mw));
      mx = max2(mx, dpp<0x128>(mx));
      mx = x16_max(mx);
      const float f = __builtin_amdgcn_exp2f((mw - mx) * 1.4426950408889634f);
      float l = f * lw;
      l += dpp<0x124>(l);
      l += dpp<0x128>(l);
      l = x16_sum(l);
      if (lane < 32) ((LAS float*)(L + L_MSC))[(lane & 3) * 8 + (lane >> 2)] = f * __builtin_amdgcn_rcpf(l);
      lds_arrive(flags + 4, lane);
      if (lane < 4) {
        const float lse = mx * 1.4426950408889634f + __builtin_amdgcn_logf(l);
        __builtin_amdgcn_raw_buffer_store_b64(v2u{asu(lse), tag}, rs, WS_LS + (b * HKV + j) * 128 + (lane * 4 + s) * 8, 0, AUX_SC1);
      }
    }
    if (is_wo) {
      const int h = 4 * j + (r >> 2), ns = r & 3;
      while (flags[4] < DMAFLAG && !(WODELAY && flags[3] == 8u && (unsigned)(__builtin_amdgcn_s_memrealtime() - t_start) >= (unsigned)WODELAY)) __builtin_amdgcn_s_sleep(2);
      if (!ABL_NOW) {
        const unsigned char* src = (const unsigned char*)wo + ((size_t)(128 * h + 32 * hw + g) * D + 128 * ns) * 2 + n16 * 16;
#pragma unroll
        for (int i = 0; i < 8; ++i) glds16(src + (size_t)i * 4 * D * 2, lds0 + L_W + (32 * hw + 4 * i) * 256);
      }
      STAMP(5);
      asm volatile("s_waitcnt vmcnt(0)" ::: "memory");
      STAMP(6);
      BAR();
    } else if (is_mlp && (ABL_NOW || ABL_NOWM)) { BAR(); } else if (is_mlp) {
      while (flags[4] < DMAFLAG_MLP) __builtin_amdgcn_s_sleep(2);
      if (MLPSLEEP) __builtin_amdgcn_s_sleep(MLPSLEEP);
      const int fg = 8 * j + ((r - 16) >> 1), nh = r & 1;
      const size_t go = ((size_t)(128 * hw + (lane >> 2)) * FF + 32 * fg) * 2 + (lane & 3) * 16;
#pragma unroll
      for (int i = 0; i < 8; ++i) {
        if (i >= 2) asm volatile("s_waitcnt vmcnt(3)" ::: "memory");
        glds16((const unsigned char*)wg + go + (size_t)i * 16 * FF * 2, lds0 + L_GT + (128 * hw + 16 * i) * 64);
        if (i >= 2) asm volatile("s_waitcnt vmcnt(3)" ::: "memory");
        glds16((const unsigned char*)wu + go + (size_t)i * 16 * FF * 2, lds0 + L_UP + (128 * hw + 16 * i) * 64);
      }
      const unsigned char* sd = (const unsigned char*)wd + ((size_t)(32 * fg + 8 * hw + (lane >> 5)) * D + 256 * nh) * 2 + (lane & 31) * 16;
#pragma unroll
      for (int i = 0; i < 4; ++i) {
        asm volatile("s_waitcnt vmcnt(5)" ::: "memory");
        glds16(sd + (size_t)i * 2 * D * 2, lds0 + L_DN + (8 * hw + 2 * i) * 512);
      }
      STAMP(5);
      asm volatile("s_waitcnt vmcnt(0)" ::: "memory");
      STAMP(6);
      BAR();
    }
  }
}

}

extern "C" void kernel_launch(void* const* d_in, const int* in_sizes, int n_in, void* d_out, int out_size, void* d_ws,
                              size_t ws_size, hipStream_t stream) {
  (void)in_sizes; (void)n_in; (void)out_size;
  const size_t ws_base = ((WS_TOP) != 0 && ws_size >= (size_t)WS_TOP) ? ((ws_size - (size_t)WS_TOP) & ~(size_t)4095) : 0;
  (void)hipFuncSetAttribute((const void*)fused_decode, hipFuncAttributeMaxDynamicSharedMemorySize, LDS_BYTES);
  fused_decode<<<256, NTHR, LDS_BYTES, stream>>>(
      (const unsigned short*)d_in[0], (const unsigned short*)d_in[1], (const unsigned short*)d_in[2], (const unsigned short*)d_in[3],
      (const unsigned short*)d_in[4], (const unsigned short*)d_in[6], (const unsigned short*)d_in[7], (const unsigned short*)d_in[8],
      (unsigned short*)d_out, (unsigned char*)d_ws + ws_base);
}
```

```cpp
#include <hip/hip_runtime.h>
#include <hip/hip_bf16.h>
#include <stdint.h>

namespace {

typedef unsigned v4u __attribute__((ext_vector_type(4)));
typedef unsigned v2u __attribute__((ext_vector_type(2)));
typedef float f32x4 __attribute__((ext_vector_type(4)));
typedef __bf16 bf16x8 __attribute__((ext_vector_type(8)));
typedef unsigned long long u64;
#define LAS __attribute__((address_space(3)))
typedef LAS unsigned char* lds_p;

constexpr int D = 512, HQ = 32, HKV = 8, SK = 4096, DH = 128, WIN = 1024, FF = 1792;
constexpr int NTHR = 768;
constexpr int AUX_SC1 = 16;
constexpr unsigned SPIN_CAP = 1u << 16;
constexpr int PD = 3;
constexpr int THRN = 10;
constexpr unsigned DMAFLAG = 2;
constexpr unsigned QDELAY = 290;
constexpr unsigned QAFTER = 60;
constexpr int CODE_LINES = 119;

constexpr int WS_EPOCH = 0;
constexpr int PAD = 1;
constexpr int QP_B = 256 + PAD, QP_K = 8 * QP_B;
constexpr int RP_C = 32 + PAD;
constexpr int FP_C = 56 + PAD;
constexpr int WS_QP = 1 << 16;
constexpr int WS_OP = WS_QP + 4 * QP_K * 128;
constexpr int WS_LS = WS_OP + (1 << 19);
constexpr int WS_RP = WS_LS + (1 << 15);
constexpr int WS_GH = WS_RP + 128 * RP_C * 128;
constexpr int WS_FP = WS_GH + (1 << 14);
constexpr int WS_END = WS_FP + 128 * FP_C * 128;

constexpr int L_RED = 0;
constexpr int L_WQ = 0;
constexpr int L_Q = 16384;
constexpr int L_MAX = L_Q + 4 * 320;
constexpr int L_SUM = L_MAX + 128;
constexpr int L_FLAG = L_SUM + 128;
constexpr int L_MSC = L_FLAG + 64;
constexpr int L_CS = L_MSC + 128;
constexpr int L_ST = L_CS + 256;
constexpr int L_OS = L_ST + 64;
constexpr int L_HN = L_OS + 8 * 288;
constexpr int L_R3 = L_HN + 8 * 1056;
constexpr int L_HH = L_R3 + 8192;
constexpr int L_W = ((L_HH + 8 * 96 + 255) / 256) * 256;
constexpr int L_GT = L_W, L_UP = L_W + 32768, L_DN = L_W + 65536;
constexpr int LDS_BYTES = L_W + 98304;
static_assert(LDS_BYTES <= 160 * 1024 && LDS_BYTES > 80 * 1024, "one workgroup per CU");

__device__ __forceinline__ float bf_lo(unsigned u) { return __builtin_bit_cast(float, u << 16); }
__device__ __forceinline__ float bf_hi(unsigned u) { return __builtin_bit_cast(float, u & 0xffff0000u); }
__device__ __forceinline__ unsigned pack_bf(float lo, float hi) {
  typedef __bf16 bf2 __attribute__((ext_vector_type(2)));
  bf2 r = {(__bf16)lo, (__bf16)hi};
  return __builtin_bit_cast(unsigned, r);
}
__device__ __forceinline__ float rbf(float f) { return bf_lo(pack_bf(f, f)); }
__device__ __forceinline__ float asf(unsigned u) { return __builtin_bit_cast(float, u); }
__device__ __forceinline__ unsigned asu(float f) { return __builtin_bit_cast(unsigned, f); }
template <int CTRL> __device__ __forceinline__ float dpp(float x) {
  return __builtin_bit_cast(float, __builtin_amdgcn_mov_dpp(__builtin_bit_cast(int, x), CTRL, 0xf, 0xf, true));
}
__device__ __forceinline__ float max3(float a, float b, float c) {
  float r;
  asm("v_max3_f32 %0, %1, %2, %3" : "=v"(r) : "v"(a), "v"(b), "v"(c));
  return r;
}
__device__ __forceinline__ float max2(float a, float b) {
  float r;
  asm("v_max_f32 %0, %1, %2" : "=v"(r) : "v"(a), "v"(b));
  return r;
}
__device__ __forceinline__ float x16_sum(float x) {
  auto s = __builtin_amdgcn_permlane16_swap(asu(x), asu(x), false, false);
  return asf(s[0]) + asf(s[1]);
}
__device__ __forceinline__ float x32_sum(float x) {
  auto s = __builtin_amdgcn_permlane32_swap(asu(x), asu(x), false, false);
  return asf(s[0]) + asf(s[1]);
}
__device__ __forceinline__ float x16_max(float x) {
  auto s = __builtin_amdgcn_permlane16_swap(asu(x), asu(x), false, false);
  return max2(asf(s[0]), asf(s[1]));
}
__device__ __forceinline__ float x32_max(float x) {
  auto s = __builtin_amdgcn_permlane32_swap(asu(x), asu(x), false, false);
  return max2(asf(s[0]), asf(s[1]));
}
__device__ __forceinline__ f32x4 mfma(v4u a, v4u b, f32x4 c) {
  return __builtin_amdgcn_mfma_f32_16x16x32_bf16(__builtin_bit_cast(bf16x8, a), __builtin_bit_cast(bf16x8, b), c, 0, 0, 0);
}
__device__ __forceinline__ v2u tr_read(lds_p p) {
  typedef short v4s __attribute__((ext_vector_type(4)));
  return __builtin_bit_cast(v2u, __builtin_amdgcn_ds_read_tr16_b64_v4i16((LAS v4s*)p));
}
__device__ __forceinline__ v4u tr_frag(lds_p p, int row_stride) {
  const v2u a = tr_read(p), b = tr_read(p + 4 * row_stride);
  return v4u{a.x, a.y, b.x, b.y};
}
__device__ __forceinline__ v4u lds_ld128(lds_p p) { return *(const LAS v4u*)p; }
__device__ __forceinline__ void glds16(const void* gsrc, unsigned lds_dst) {
  unsigned keep;
  asm volatile("s_mov_b32 %0, m0\n\ts_mov_b32 m0, %2\n\ts_nop 0\n\tglobal_load_lds_dwordx4 %1, off nt\n\ts_mov_b32 m0, %0"
               : "=&s"(keep) : "v"(gsrc), "s"(lds_dst) : "memory");
}
#define BAR() do { asm volatile("s_waitcnt lgkmcnt(0)" ::: "memory"); __builtin_amdgcn_s_barrier(); asm volatile("" ::: "memory"); } while (0)
__device__ __forceinline__ void lds_arrive(volatile LAS unsigned* p, int lane) {
  asm volatile("" ::: "memory");
  if (lane == 0) asm volatile("ds_add_u32 %0, %1" :: "v"((unsigned)(size_t)p), "v"(1u) : "memory");
  asm volatile("" ::: "memory");
}
__device__ __forceinline__ void lds_wait(volatile LAS unsigned* p, unsigned n) {
  while (*p < n) { }
  asm volatile("" ::: "memory");
}
typedef unsigned long long mask_t;
__device__ __forceinline__ mask_t bad4(v4u v, unsigned tag) { return __builtin_amdgcn_ballot_w64(v.y != tag) | __builtin_amdgcn_ballot_w64(v.w != tag); }
__device__ __forceinline__ mask_t bad2(v2u v, unsigned tag) { return __builtin_amdgcn_ballot_w64(v.y != tag); }
#define PL4(n_, o_, m_) "s_mov_b64 exec, %" #m_ "\n\tglobal_load_dwordx4 %" #n_ ", %" #o_ ", %[base] sc1\n\t"
#define PL2(n_, o_, m_) "s_mov_b64 exec, %" #m_ "\n\tglobal_load_dwordx2 %" #n_ ", %" #o_ ", %[base] sc1\n\t"
#define PLEND "s_mov_b64 exec, -1\n\ts_waitcnt vmcnt(0)"
__device__ __forceinline__ void poll1x4(const unsigned char* ws, unsigned tag, unsigned o0, v4u& a) {
  a = v4u{0u, ~tag, 0u, ~tag};
  for (unsigned spins = 0; spins <= SPIN_CAP; ++spins) {
    const mask_t m0 = bad4(a, tag);
    if (m0 == 0) break;
    asm volatile(PL4(0, 1, 2) PLEND : "+v"(a) : "v"(o0), "s"(m0), [base] "s"(ws) : "memory");
    __builtin_amdgcn_s_sleep(2);
  }
}
__device__ __forceinline__ void poll2x4(const unsigned char* ws, unsigned tag, unsigned o0, unsigned o1, v4u& a, v4u& b) {
  a = b = v4u{0u, ~tag, 0u, ~tag};
  for (unsigned spins = 0; spins <= SPIN_CAP; ++spins) {
    const mask_t m0 = bad4(a, tag), m1 = bad4(b, tag);
    if ((m0 | m1) == 0) break;
    asm volatile(PL4(0, 2, 4) PL4(1, 3, 5) PLEND : "+v"(a), "+v"(b) : "v"(o0), "v"(o1), "s"(m0), "s"(m1), [base] "s"(ws) : "memory");
  }
}
__device__ __forceinline__ void poll5x2(const unsigned char* ws, unsigned tag, unsigned oa, unsigned ob, v2u (&v)[5]) {
  unsigned o[4];
#pragma unroll
  for (int i = 0; i < 4; ++i) o[i] = oa + i * 512;
#pragma unroll
  for (int i = 0; i < 5; ++i) v[i] = v2u{0u, ~tag};
  for (unsigned spins = 0; spins <= SPIN_CAP; ++spins) {
    mask_t m[5], any = 0;
#pragma unroll
    for (int i = 0; i < 5; ++i) { m[i] = bad2(v[i], tag); any |= m[i]; }
    if (any == 0) break;
    asm volatile(PL2(0, 5, 10) PL2(1, 6, 11) PL2(2, 7, 12) PL2(3, 8, 13) PL2(4, 9, 14) PLEND
                 : "+v"(v[0]), "+v"(v[1]), "+v"(v[2]), "+v"(v[3]), "+v"(v[4])
                 : "v"(o[0]), "v"(o[1]), "v"(o[2]), "v"(o[3]), "v"(ob),
                   "s"(m[0]), "s"(m[1]), "s"(m[2]), "s"(m[3]), "s"(m[4]), [base] "s"(ws) : "memory");
  }
}
template <int N, int STEP>
__device__ __forceinline__ void pollNx4(const unsigned char* ws, unsigned tag, unsigned o0, v4u (&v)[N]) {
  static_assert(N == 4 || N == 7, "");
  unsigned o[N];
#pragma unroll
  for (int i = 0; i < N; ++i) { v[i] = v4u{0u, ~tag, 0u, ~tag}; o[i] = o0 + i * STEP; }
  for (unsigned spins = 0; spins <= SPIN_CAP; ++spins) {
    mask_t m[N], any = 0;
#pragma unroll
    for (int i = 0; i < N; ++i) { m[i] = bad4(v[i], tag); any |= m[i]; }
    if (any == 0) break;
    if constexpr (N == 4) {
      asm volatile(PL4(0, 4, 8) PL4(1, 5, 9) PL4(2, 6, 10) PL4(3, 7, 11) PLEND
                   : "+v"(v[0]), "+v"(v[1]), "+v"(v[2]), "+v"(v[3])
                   : "v"(o[0]), "v"(o[1]), "v"(o[2]), "v"(o[3]), "s"(m[0]), "s"(m[1]), "s"(m[2]), "s"(m[3]), [base] "s"(ws) : "memory");
    } else {
      asm volatile(PL4(0, 7, 14) PL4(1, 8, 15) PL4(2, 9, 16) PL4(3, 10, 17) PL4(4, 11, 18) PL4(5, 12, 19) PL4(6, 13, 20) PLEND
                   : "+v"(v[0]), "+v"(v[1]), "+v"(v[2]), "+v"(v[3]), "+v"(v[4]), "+v"(v[5]), "+v"(v[6])
                   : "v"(o[0]), "v"(o[1]), "v"(o[2]), "v"(o[3]), "v"(o[4]), "v"(o[5]), "v"(o[6]),
                     "s"(m[0]), "s"(m[1]), "s"(m[2]), "s"(m[3]), "s"(m[4]), "s"(m[5]), "s"(m[6]), [base] "s"(ws) : "memory");
    }
  }
}

__device__ __forceinline__ void layernorm8(float (&f)[8]) {
  const float c0 = asf(__builtin_amdgcn_readfirstlane(asu(f[0])));
  float s1 = 0.f, s2 = 0.f;
#pragma unroll
  for (int i = 0; i < 8; ++i) { const float d = f[i] - c0; s1 += d; s2 = fmaf(d, d, s2); }
  s1 += dpp<0xB1>(s1); s2 += dpp<0xB1>(s2);
  s1 += dpp<0x4E>(s1); s2 += dpp<0x4E>(s2);
  s1 += dpp<0x141>(s1); s2 += dpp<0x141>(s2);
  s1 += dpp<0x140>(s1); s2 += dpp<0x140>(s2);
  s1 = x16_sum(s1); s2 = x16_sum(s2);
  s1 = x32_sum(s1) * (1.0f / D); s2 = x32_sum(s2) * (1.0f / D);
  const float var = rbf(fmaxf(fmaf(-s1, s1, s2), 0.f));
  const float den = rbf(__builtin_amdgcn_sqrtf(rbf(var + 1e-5f)));
  const float inv = __builtin_amdgcn_rcpf(den), mb = rbf(c0 + s1);
#pragma unroll
  for (int i = 0; i < 8; ++i) f[i] = rbf(f[i] - mb) * inv;
}

__device__ __forceinline__ void publish_tile(f32x4 d, __amdgpu_buffer_rsrc_t rs, int base, int stride_b, unsigned tag, int lane) {
  const bool t = lane & 8;
  const int src = ((lane >> 3) + 8 * (lane & 1) + 16 * ((lane >> 1) & 3)) * 4;
  const unsigned v0 = (unsigned)__builtin_amdgcn_ds_bpermute(src, (int)asu(t ? d[2] : d[0]));
  const unsigned v1 = (unsigned)__builtin_amdgcn_ds_bpermute(src, (int)asu(t ? d[3] : d[1]));
  __builtin_amdgcn_raw_buffer_store_b128(v4u{v0, tag, v1, tag}, rs, base + (lane >> 3) * stride_b + (lane & 7) * 16, 0, AUX_SC1);
}

__device__ __forceinline__ void final_cell(const unsigned char* ws, unsigned tag, int cell, int lane, unsigned short* out, unsigned* ws_epoch) {
  const int p = lane & 7, row = lane >> 4;
  v4u hv;
  poll1x4(ws, tag, WS_GH + cell * 128 + p * 16, hv);
  float hsel = row == 0 ? bf_lo(hv.x) : row == 1 ? bf_hi(hv.x) : row == 2 ? bf_lo(hv.z) : bf_hi(hv.z);
  unsigned oo = (p * D + 4 * cell + (row & 2)) * 2;
  asm volatile("" : "+v"(hsel), "+v"(oo));
  v4u v[7];
  pollNx4<7, 8 * 128>(ws, tag, WS_FP + (cell * FP_C + (lane >> 3)) * 128 + p * 16, v);
  float a0 = 0.f, a1 = 0.f, a2 = 0.f, a3 = 0.f;
#pragma unroll
  for (int i = 0; i < 7; ++i) { a0 += bf_lo(v[i].x); a1 += bf_hi(v[i].x); a2 += bf_lo(v[i].z); a3 += bf_hi(v[i].z); }
  auto s01 = __builtin_amdgcn_permlane16_swap(asu(a0), asu(a1), false, false);
  auto s23 = __builtin_amdgcn_permlane16_swap(asu(a2), asu(a3), false, false);
  const float c0 = asf(s01[0]) + asf(s01[1]), c1 = asf(s23[0]) + asf(s23[1]);
  auto sc = __builtin_amdgcn_permlane32_swap(asu(c0), asu(c1), false, false);
  float e = asf(sc[0]) + asf(sc[1]);
  e += dpp<0x128>(e);
  const unsigned ob = pack_bf(hsel + rbf(e), 0.f) & 0xffffu;
  auto op = __builtin_amdgcn_permlane16_swap(ob, ob, false, false);
  if ((lane & 24) == 0) *(__attribute__((address_space(1))) unsigned*)((unsigned char*)out + oo) = op[0] | (op[1] << 16);
  if (cell == 0 && lane == 0) __hip_atomic_store(ws_epoch, tag, __ATOMIC_RELAXED, __HIP_MEMORY_SCOPE_AGENT);
}

__global__ void __launch_bounds__(NTHR, 3)
fused_decode(const unsigned short* __restrict__ x, const unsigned short* __restrict__ kc, const unsigned short* __restrict__ vc,
             const unsigned short* __restrict__ wq, const unsigned short* __restrict__ wo, const unsigned short* __restrict__ wg,
             const unsigned short* __restrict__ wu, const unsigned short* __restrict__ wd, unsigned short* __restrict__ out,
             unsigned char* __restrict__ ws) {
  unsigned long long code_base;
  asm volatile("s_getpc_b64 %0" : "=s"(code_base));
  code_base &= ~0xffull;
  extern __shared__ __attribute__((aligned(16))) unsigned char smem[];
  const lds_p L = (lds_p)smem;
  const unsigned lds0 = (unsigned)(size_t)L;
  const int tid = threadIdx.x, lane = tid & 63;
  const int wv = __builtin_amdgcn_readfirstlane(tid >> 6);
  const int w = wv - 4;
  const int c = blockIdx.x, j = c & 7, r = c >> 3;
  const int b = r >> 2, s = r & 3;
  const int g = lane >> 4, n16 = lane & 15;
  const bool is_wo = r < 16, is_mlp = (r >= 16) && (j < 7);
  const __amdgpu_buffer_rsrc_t rs = __builtin_amdgcn_make_buffer_rsrc((void*)ws, 0, WS_END, 0x00020000);
  volatile LAS unsigned* flags = (volatile LAS unsigned*)(L + L_FLAG);

  const u64 t_start = __builtin_amdgcn_s_memrealtime();
  const int kq0 = r >> 3, ct8 = r & 7;
  v4u xa, xb; unsigned ep, pf;
  asm volatile("" : "=v"(xa), "=v"(xb), "=v"(ep), "=v"(pf));
  if (wv < 4) {
    const int hw = wv;
    __builtin_amdgcn_s_setprio(2);
    const unsigned zero = 0;
    const unsigned xo = (2 * hw) * (D * 2) + lane * 16;
    asm volatile("global_load_dwordx4 %0, %1, %2" : "+v"(xa) : "v"(xo), "s"(x));
    asm volatile("global_load_dwordx4 %0, %1, %2 offset:1024" : "+v"(xb) : "v"(xo), "s"(x));
    const unsigned char* src = (const unsigned char*)wq + ((size_t)(128 * kq0 + 32 * hw + (lane >> 3)) * (HQ * DH) + 512 * j + 64 * ct8) * 2 + (lane & 7) * 16;
#pragma unroll
    for (int i = 0; i < 4; ++i) glds16(src + (size_t)i * 8 * (HQ * DH) * 2, lds0 + L_WQ + (32 * hw + 8 * i) * 128);
    asm volatile("global_load_dword %0, %1, %2" : "+v"(ep) : "v"(zero), "s"(ws));
    const unsigned po = min((r * 4 + hw) * 2 + (lane & 1), CODE_LINES - 1) * 128;
    asm volatile("global_load_dword %0, %1, %2" : "+v"(pf) : "v"(po), "s"(code_base));
  }
  int wv2 = wv;
  asm volatile("" : "+s"(wv2));

  if (__builtin_expect(wv2 >= 4, 1)) {
    const size_t tile = ((size_t)(b * HKV + j) * SK + (SK - WIN) + 256 * s + 32 * w) * (DH * 2);
    const unsigned char* kb = (const unsigned char*)kc + tile + 4096;
    const unsigned char* vb = (const unsigned char*)vc + tile;
    const unsigned koff = (lane & 7) * 256 + (2 * g + ((lane >> 3) & 1)) * 16;
    const unsigned voff = (16 * (g >> 1) + 4 * (g & 1)) * 256 + n16 * 16;
    v4u K[8], V[8];
#define LDK(i, imm) asm volatile("global_load_dwordx4 %0, %1, %2 offset:" #imm " nt" : "=v"(K[i]) : "v"(koff), "s"(kb))
#define LDV(i, imm) asm volatile("global_load_dwordx4 %0, %1, %2 offset:" #imm " nt" : "=v"(V[i]) : "v"(voff), "s"(vb))
#define THR(n) do { if (n < THRN || __builtin_amdgcn_readfirstlane(flags[6]) < 4) asm volatile("s_waitcnt vmcnt(%0)" :: "i"(PD - 1)); } while (0)
    static_assert(PD == 3, "three loads go out in front of the barrier");
    LDK(0, -4096); LDK(1, -3968); LDV(0, 0);
    if (w == 0 && lane < 8) flags[lane] = 0;
    BAR();
    THR(3); LDK(2, -2048); THR(4); LDK(3, -1920); THR(5); LDK(4, 0); THR(6); LDK(5, 128); THR(7); LDK(6, 2048); THR(8); LDK(7, 2176);
    THR(9); LDV(1, 256); THR(10); LDV(2, 512); THR(11); LDV(3, 768);
    THR(12); LDV(4, 2048); THR(13); LDV(5, 2304); THR(14); LDV(6, 2560); THR(15); LDV(7, 2816);
    while (flags[6] < 4u) __builtin_amdgcn_s_sleep(1);
    asm volatile("" ::: "memory");
    const unsigned tag = flags[1];
    const lds_p qa = L + L_Q + (n16 & 3) * 320 + (2 * g + ((n16 >> 2) & 1)) * 16;
    const v4u q0 = lds_ld128(qa), q1 = lds_ld128(qa + 128);
    asm volatile("s_waitcnt vmcnt(7)" : "+v"(K[0]), "+v"(K[1]), "+v"(K[2]), "+v"(K[3]), "+v"(K[4]), "+v"(K[5]), "+v"(K[6]), "+v"(K[7]));
    f32x4 sa[4];
#pragma unroll
    for (int o = 0; o < 4; ++o) {
      sa[o] = mfma(K[2 * o], q0, f32x4{0.f, 0.f, 0.f, 0.f});
      sa[o] = mfma(K[2 * o + 1], q1, sa[o]);
    }
    float sc[8];
    {
      const bool par = lane & 4;
      float raw[8];
#pragma unroll
      for (int p = 0; p < 2; ++p)
#pragma unroll
        for (int i = 0; i < 4; ++i) {
          auto sw = __builtin_amdgcn_permlane32_swap(asu(sa[p][i]), asu(sa[p + 2][i]), false, false);
          const float a = par ? asf(sw[1]) : asf(sw[0]);
          raw[4 * p + i] = a + dpp<0x124>(a);
        }
#pragma unroll
      for (int i = 0; i < 8; i += 2) {
        const unsigned u = pack_bf(raw[i], raw[i + 1]);
        const unsigned t = pack_bf(bf_lo(u) * 0.08838834764831845f, bf_hi(u) * 0.08838834764831845f);
        sc[i] = bf_lo(t); sc[i + 1] = bf_hi(t);
      }
    }
    float m = max3(max3(sc[0], sc[1], sc[2]), max3(sc[3], sc[4], sc[5]), max2(sc[6], sc[7]));
    m = x32_max(x16_max(m));
    float pe[8], ls;
    {
      const float mc = m * 1.4426950408889634f;
#pragma unroll
      for (int i = 0; i < 8; ++i) pe[i] = __builtin_amdgcn_exp2f(fmaf(sc[i], 1.4426950408889634f, -mc));
      ls = ((pe[0] + pe[1]) + (pe[2] + pe[3])) + ((pe[4] + pe[5]) + (pe[6] + pe[7]));
    }
    v4u PA[4];
    {
      const v4u P = {pack_bf(pe[0], pe[1]), pack_bf(pe[2], pe[3]), pack_bf(pe[4], pe[5]), pack_bf(pe[6], pe[7])};
#pragma unroll
      for (int mq = 0; mq < 4; ++mq) {
        const bool on = (n16 >> 2) == mq;
        PA[mq] = v4u{on ? P.x : 0u, on ? P.y : 0u, on ? P.z : 0u, on ? P.w : 0u};
      }
    }
    ls = x32_sum(x16_sum(ls));
    if (lane < 4) {
      ((LAS float*)(L + L_MAX))[w * 4 + lane] = m;
      ((LAS float*)(L + L_SUM))[w * 4 + lane] = ls;
    }
    lds_arrive(flags + 2, lane);
    unsigned T[8][4];
#define XPOSE(n_)                                                                                         \
  _Pragma("unroll") for (int mm = 0; mm < 4; ++mm) {                                                      \
    T[2 * mm][n_] = __builtin_amdgcn_perm(V[2 * n_ + 1][mm], V[2 * n_][mm], 0x05040100u);                 \
    T[2 * mm + 1][n_] = __builtin_amdgcn_perm(V[2 * n_ + 1][mm], V[2 * n_][mm], 0x07060302u);             \
  }                                                                                                       \
  asm volatile("" : "+v"(T[0][n_]), "+v"(T[1][n_]), "+v"(T[2][n_]), "+v"(T[3][n_]), "+v"(T[4][n_]), "+v"(T[5][n_]), "+v"(T[6][n_]), "+v"(T[7][n_]));
    int ooff = WS_OP + ((b * HQ + 4 * j + (w >> 1)) * 4 + s) * 512 + (w & 1) * 256 + n16 * 16;
    const int dr = 64 * (w & 1) + 4 * n16 + g, dw = 8 * n16 + g;
    unsigned redr_a = lds0 + L_RED + ((w >> 1) * 128 + (dr ^ ((dr >> 5) << 1))) * 4;
    unsigned redw_a = lds0 + L_RED + w * 2048 + (dw ^ ((dw >> 5) << 1)) * 4;
    const int h = 4 * j + (r >> 2), ns = r & 3;
    unsigned doff = WS_OP + (w * HQ + h) * 4 * 512 + lane * 8;
    unsigned loff = WS_LS + (w * HKV + j) * 128 + ((h & 3) * 4 + (lane & 3)) * 8;
    int roff = WS_RP + ((32 * ns + 4 * w + g) * RP_C + h) * 128 + (lane & 7) * 16;
    unsigned osw = lds0 + L_OS + w * 288 + lane * 4, osr = lds0 + L_OS + (lane & 7) * 288 + g * 16;
    unsigned wfa = lds0 + L_W + (8 * g + (n16 >> 2)) * 256 + (16 * w + 4 * (lane & 3)) * 2;
    asm volatile("" : "+v"(PA[0]), "+v"(PA[1]), "+v"(PA[2]), "+v"(PA[3]), "+v"(ooff), "+v"(redr_a), "+v"(redw_a));
    asm volatile("" : "+v"(doff), "+v"(loff), "+v"(roff), "+v"(osw), "+v"(osr), "+v"(wfa));
    asm volatile("s_waitcnt vmcnt(6)" : "+v"(V[0]), "+v"(V[1])); XPOSE(0)
    asm volatile("s_waitcnt vmcnt(4)" : "+v"(V[2]), "+v"(V[3])); XPOSE(1)
    asm volatile("s_waitcnt vmcnt(2)" : "+v"(V[4]), "+v"(V[5])); XPOSE(2)
    asm volatile("s_waitcnt vmcnt(0)" : "+v"(V[6]), "+v"(V[7]));
    XPOSE(3)
    {
      f32x4 oa = {0.f, 0.f, 0.f, 0.f}, ob = {0.f, 0.f, 0.f, 0.f};
#pragma unroll
      for (int i = 0; i < 4; ++i) {
        oa = mfma(PA[i], v4u{T[i][0], T[i][1], T[i][2], T[i][3]}, oa);
        ob = mfma(PA[i], v4u{T[i + 4][0], T[i + 4][1], T[i + 4][2], T[i + 4][3]}, ob);
      }
      LAS float* red = (LAS float*)(size_t)redw_a;
#pragma unroll
      for (int hh = 0; hh < 4; ++hh) { red[hh * 128] = oa[hh]; *(LAS float*)(size_t)((redw_a ^ 16) + hh * 512) = ob[hh]; }
    }
    lds_arrive(flags + 4, lane);
    const LAS float* redr = (const LAS float*)(size_t)redr_a;
    lds_wait(flags + 4, 9);
    {
      const f32x4 ma = *(const LAS f32x4*)(L + L_MSC + (w >> 1) * 32), mb = *(const LAS f32x4*)(L + L_MSC + (w >> 1) * 32 + 16);
      float o = redr[0] * ma[0], o2 = redr[512] * ma[1];
      o = fmaf(redr[2 * 512], ma[2], o); o2 = fmaf(redr[3 * 512], ma[3], o2);
      o = fmaf(redr[4 * 512], mb[0], o); o2 = fmaf(redr[5 * 512], mb[1], o2);
      o = fmaf(redr[6 * 512], mb[2], o); o2 = fmaf(redr[7 * 512], mb[3], o2);
      o += o2;
      auto s16 = __builtin_amdgcn_permlane16_swap(asu(o), asu(o), false, false);
      const unsigned pr = pack_bf(asf(s16[0]), asf(s16[1]));
      auto s32 = __builtin_amdgcn_permlane32_swap(pr, pr, false, false);
      if (lane < 16) __builtin_amdgcn_raw_buffer_store_b128(v4u{s32[0], tag, s32[1], tag}, rs, ooff, 0, AUX_SC1);
    }

    if (is_wo) {
      BAR();
      v4u wf[4];
      {
        const lds_p p = (lds_p)(size_t)wfa;
#pragma unroll
        for (int t = 0; t < 4; ++t) wf[t] = tr_frag(p + t * 32 * 256, 256);
      }
      v2u dv[5];
      poll5x2(ws, tag, doff, loff, dv);
      {
        const float lse = asf(dv[4].x);
        float mx = max2(lse, dpp<0xB1>(lse));
        mx = max2(mx, dpp<0x4E>(mx));
        const float e = __builtin_amdgcn_exp2f(lse - mx);
        float es = e + dpp<0xB1>(e);
        es += dpp<0x4E>(es);
        const float wt = e * __builtin_amdgcn_rcpf(es);
        const float e0 = dpp<0x00>(wt), e1 = dpp<0x55>(wt), e2 = dpp<0xAA>(wt), e3 = dpp<0xFF>(wt);
        const float o0 = fmaf(e0, bf_lo(dv[0].x), e1 * bf_lo(dv[1].x)) + fmaf(e2, bf_lo(dv[2].x), e3 * bf_lo(dv[3].x));
        const float o1 = fmaf(e0, bf_hi(dv[0].x), e1 * bf_hi(dv[1].x)) + fmaf(e2, bf_hi(dv[2].x), e3 * bf_hi(dv[3].x));
        *(LAS unsigned*)(size_t)osw = pack_bf(o0, o1);
      }
      BAR();
      {
        const lds_p pa = (lds_p)(size_t)osr;
        f32x4 acc0 = mfma(wf[0], lds_ld128(pa), f32x4{0.f, 0.f, 0.f, 0.f});
        f32x4 acc1 = mfma(wf[1], lds_ld128(pa + 64), f32x4{0.f, 0.f, 0.f, 0.f});
        acc0 = mfma(wf[2], lds_ld128(pa + 128), acc0);
        acc1 = mfma(wf[3], lds_ld128(pa + 192), acc1);
        acc0 += acc1;
        if ((lane & 8) == 0) __builtin_amdgcn_raw_buffer_store_b128(v4u{pack_bf(acc0[0], acc0[1]), tag, pack_bf(acc0[2], acc0[3]), tag}, rs, roff, 0, AUX_SC1);
      }
      if (w == 0) {
        const int rho = 16 * j + r;
        const unsigned xv = *(const unsigned*)(x + (lane & 7) * D + 4 * rho + (g & 2));
        float xsel = (g & 1) ? bf_hi(xv) : bf_lo(xv);
        asm volatile("" : "+v"(xsel));
        v4u v[4];
        pollNx4<4, 8 * 128>(ws, tag, WS_RP + (rho * RP_C + (lane >> 3)) * 128 + (lane & 7) * 16, v);
        const float a0 = (bf_lo(v[0].x) + bf_lo(v[1].x)) + (bf_lo(v[2].x) + bf_lo(v[3].x)), a1 = (bf_hi(v[0].x) + bf_hi(v[1].x)) + (bf_hi(v[2].x) + bf_hi(v[3].x));
        const float a2 = (bf_lo(v[0].z) + bf_lo(v[1].z)) + (bf_lo(v[2].z) + bf_lo(v[3].z)), a3 = (bf_hi(v[0].z) + bf_hi(v[1].z)) + (bf_hi(v[2].z) + bf_hi(v[3].z));
        auto s01 = __builtin_amdgcn_permlane16_swap(asu(a0), asu(a1), false, false);
        auto s23 = __builtin_amdgcn_permlane16_swap(asu(a2), asu(a3), false, false);
        auto sc = __builtin_amdgcn_permlane32_swap(asu(asf(s01[0]) + asf(s01[1])), asu(asf(s23[0]) + asf(s23[1])), false, false);
        float e = asf(sc[0]) + asf(sc[1]);
        e += dpp<0x128>(e);
        const unsigned hb = pack_bf(xsel + rbf(e), 0.f) & 0xffffu;
        auto hp = __builtin_amdgcn_permlane16_swap(hb, hb, false, false);
        const unsigned hq = (unsigned)__builtin_amdgcn_ds_bpermute((((lane >> 1) & 7) + 32 * (lane & 1)) * 4, (int)(hp[0] | (hp[1] << 16)));
        if (lane < 16) __builtin_amdgcn_raw_buffer_store_b64(v2u{hq, tag}, rs, WS_GH + rho * 128 + lane * 8, 0, AUX_SC1);
      } else if (w == 1) {
        final_cell(ws, tag, 16 * j + r, lane, out, (unsigned*)(ws + WS_EPOCH));
      }
    } else if (is_mlp) {
      const int fg = 8 * j + ((r - 16) >> 1), nh = r & 1;
      const int kq = w >> 1, cth = w & 1;
      BAR();
      v4u gf[4], uf[4], df[2];
      {
        const lds_p p = L + L_GT + (128 * kq + 8 * g + (n16 >> 2)) * 64 + (16 * cth + 4 * (lane & 3)) * 2;
#pragma unroll
        for (int t = 0; t < 4; ++t) { gf[t] = tr_frag(p + t * 32 * 64, 64); uf[t] = tr_frag(p + 32768 + t * 32 * 64, 64); }
        const lds_p pd = L + L_DN + (8 * g + (n16 >> 2)) * 512 + (32 * w + 4 * (lane & 3)) * 2;
#pragma unroll
        for (int t = 0; t < 2; ++t) df[t] = tr_frag(pd + t * 32, 512);
      }
      const bool up = lane & 8;
      unsigned r3w = lds0 + L_R3 + ((((kq * 2 + (up ? 1 : 0)) * 2 + cth) * 8 + (lane & 7)) * 16 + 4 * g) * 4;
      {
        const v4u ones = {0x3f803f80u, 0x3f803f80u, 0x3f803f80u, 0x3f803f80u};
        f32x4 cg = {0.f, 0.f, 0.f, 0.f}, cu = {0.f, 0.f, 0.f, 0.f};
#pragma unroll
        for (int t = 0; t < 4; ++t) { cg = mfma(gf[t], ones, cg); cu = mfma(uf[t], ones, cu); }
        *(LAS f32x4*)(size_t)r3w = f32x4{up ? cu[0] : cg[0], up ? cu[1] : cg[1], up ? cu[2] : cg[2], up ? cu[3] : cg[3]};
        BAR();
        if (w == 0) {
          const LAS float* r3 = (const LAS float*)(L + L_R3) + (lane >> 5) * 256 + ((lane >> 4) & 1) * 128 + (lane & 15);
          ((LAS float*)(L + L_CS))[lane] = (r3[0] + r3[512]) + (r3[1024] + r3[1536]);
        }
      }
      unsigned goff = WS_GH + 16 * w * 128 + lane * 16;
      unsigned hnw = lds0 + L_HN + (lane & 7) * 1056 + (16 * w + (lane >> 3)) * 8, hrow = lds0 + L_HN + w * 1056 + lane * 16, hnr = lds0 + L_HN + (lane & 7) * 1056 + (128 * kq + 8 * g) * 2;
      unsigned r3r = lds0 + L_R3 + (((lane >> 4) & 1) * 8 + 2 * (w & 3) + (lane >> 5)) * 64 + (lane & 15) * 4;
      unsigned hhw = lds0 + L_HH + (2 * (w & 3) + (lane >> 5)) * 96 + (lane & 31) * 2, hhr = lds0 + L_HH + (lane & 7) * 96 + g * 16;
      int foff = WS_FP + ((64 * nh + 8 * w + (up ? 4 : 0) + g) * FP_C + fg) * 128 + (lane & 7) * 16;
      asm volatile("" : "+v"(goff), "+v"(hnw), "+v"(hrow), "+v"(hnr), "+v"(r3w), "+v"(r3r), "+v"(hhw), "+v"(hhr), "+v"(foff));
      asm volatile("" : "+v"(gf[0]), "+v"(gf[1]), "+v"(gf[2]), "+v"(gf[3]), "+v"(uf[0]), "+v"(uf[1]), "+v"(uf[2]), "+v"(uf[3]));
      asm volatile("" : "+v"(df[0]), "+v"(df[1]));
      v4u h0, h1;
      poll2x4(ws, tag, goff, goff + 1024, h0, h1);
      *(LAS v2u*)(size_t)hnw = v2u{h0.x, h0.z};
      *(LAS v2u*)(size_t)(hnw + 64) = v2u{h1.x, h1.z};
      BAR();
      {
        const lds_p pa = (lds_p)(size_t)hnr;
        f32x4 ag = {0.f, 0.f, 0.f, 0.f}, au = {0.f, 0.f, 0.f, 0.f};
#pragma unroll
        for (int t = 0; t < 4; ++t) {
          const v4u a = lds_ld128(pa + t * 64);
          ag = mfma(gf[t], a, ag);
          au = mfma(uf[t], a, au);
        }
        {
          const v4u hr = lds_ld128((lds_p)(size_t)hrow);
          const float f[8] = {bf_lo(hr.x), bf_hi(hr.x), bf_lo(hr.y), bf_hi(hr.y), bf_lo(hr.z), bf_hi(hr.z), bf_lo(hr.w), bf_hi(hr.w)};
          const float c0 = asf(__builtin_amdgcn_readfirstlane(asu(f[0])));
          float s1 = 0.f, s2 = 0.f;
#pragma unroll
          for (int i = 0; i < 8; ++i) { const float d = f[i] - c0; s1 += d; s2 = fmaf(d, d, s2); }
          s1 += dpp<0xB1>(s1); s2 += dpp<0xB1>(s2);
          s1 += dpp<0x4E>(s1); s2 += dpp<0x4E>(s2);
          s1 += dpp<0x141>(s1); s2 += dpp<0x141>(s2);
          s1 += dpp<0x140>(s1); s2 += dpp<0x140>(s2);
          auto sw = __builtin_amdgcn_permlane16_swap(asu(s1), asu(s2), false, false);
          const float t = x32_sum(asf(sw[0]) + asf(sw[1])) * (1.0f / D);
          const float m1 = asf(__builtin_amdgcn_readlane(asu(t), 0)), m2 = asf(__builtin_amdgcn_readlane(asu(t), 16));
          const float var = rbf(fmaxf(fmaf(-m1, m1, m2), 0.f));
          const float den = rbf(__builtin_amdgcn_sqrtf(rbf(var + 1e-5f)));
          if (lane == 0) *(LAS v2u*)(L + L_ST + w * 8) = v2u{asu(rbf(c0 + m1)), asu(__builtin_amdgcn_rcpf(den))};
        }
        *(LAS f32x4*)(size_t)r3w = f32x4{up ? au[0] : ag[0], up ? au[1] : ag[1], up ? au[2] : ag[2], up ? au[3] : ag[3]};
      }
      BAR();
      if (w < 4) {
        const int bb = 2 * w + (lane >> 5), n32 = lane & 31;
        const LAS float* r3 = (const LAS float*)(size_t)r3r;
        const v2u stt = *(const LAS v2u*)(L + L_ST + bb * 8);
        const float csg = ((const LAS float*)(L + L_CS))[n32], csu = ((const LAS float*)(L + L_CS))[32 + n32];
        float gg = (r3[0] + r3[512]) + (r3[1024] + r3[1536]);
        float uu = (r3[256] + r3[768]) + (r3[1280] + r3[1792]);
        gg = rbf(fmaf(-asf(stt.x), csg, gg) * asf(stt.y));
        uu = rbf(fmaf(-asf(stt.x), csu, uu) * asf(stt.y));
        const float sg = rbf(__builtin_amdgcn_rcpf(1.0f + __builtin_amdgcn_exp2f(-1.4426950408889634f * gg)));
        *(LAS unsigned short*)(size_t)hhw = (unsigned short)pack_bf(sg * uu, 0.f);
      }
      BAR();
      {
        const v4u a = lds_ld128((lds_p)(size_t)hhr);
        const f32x4 z = {0.f, 0.f, 0.f, 0.f};
        const f32x4 d0 = mfma(df[0], a, z), d1 = mfma(df[1], a, z);
        __builtin_amdgcn_raw_buffer_store_b128(v4u{pack_bf(up ? d1[0] : d0[0], up ? d1[1] : d0[1]), tag, pack_bf(up ? d1[2] : d0[2], up ? d1[3] : d0[3]), tag}, rs, foff, 0, AUX_SC1);
      }
    }
  } else {
    const int hw = wv, kq = kq0;
    BAR();
    asm volatile("s_waitcnt vmcnt(6)" : "+v"(xa), "+v"(xb));
    {
      float fa[8] = {bf_lo(xa.x), bf_hi(xa.x), bf_lo(xa.y), bf_hi(xa.y), bf_lo(xa.z), bf_hi(xa.z), bf_lo(xa.w), bf_hi(xa.w)};
      float fb[8] = {bf_lo(xb.x), bf_hi(xb.x), bf_lo(xb.y), bf_hi(xb.y), bf_lo(xb.z), bf_hi(xb.z), bf_lo(xb.w), bf_hi(xb.w)};
      layernorm8(fa);
      layernorm8(fb);
      if (g == kq) {
        *(LAS v4u*)(L + L_HN + (2 * hw) * 288 + n16 * 16) = v4u{pack_bf(fa[0], fa[1]), pack_bf(fa[2], fa[3]), pack_bf(fa[4], fa[5]), pack_bf(fa[6], fa[7])};
        *(LAS v4u*)(L + L_HN + (2 * hw + 1) * 288 + n16 * 16) = v4u{pack_bf(fb[0], fb[1]), pack_bf(fb[2], fb[3]), pack_bf(fb[4], fb[5]), pack_bf(fb[6], fb[7])};
      }
    }
    asm volatile("s_waitcnt vmcnt(0)" : "+v"(ep), "+v"(pf) :: "memory");
    const unsigned tag = ep + 1;
    lds_arrive(flags + 0, lane);
    lds_wait(flags + 0, 4);
    {
      const lds_p pa = L + L_HN + (lane & 7) * 288 + g * 16;
      const lds_p pb = L + L_WQ + (8 * g + (n16 >> 2)) * 128 + (16 * hw + 4 * (lane & 3)) * 2;
      f32x4 acc0 = mfma(tr_frag(pb, 128), lds_ld128(pa), f32x4{0.f, 0.f, 0.f, 0.f});
      f32x4 acc1 = mfma(tr_frag(pb + 32 * 128, 128), lds_ld128(pa + 64), f32x4{0.f, 0.f, 0.f, 0.f});
      acc0 = mfma(tr_frag(pb + 64 * 128, 128), lds_ld128(pa + 128), acc0);
      acc1 = mfma(tr_frag(pb + 96 * 128, 128), lds_ld128(pa + 192), acc1);
      publish_tile(acc0 + acc1, rs, WS_QP + (kq * QP_K + 32 * j + 4 * ct8 + hw) * 128, QP_B * 128, tag, lane);
    }
    {
      const u64 t_pub = __builtin_amdgcn_s_memrealtime();
      for (;;) {
        const u64 now = __builtin_amdgcn_s_memrealtime();
        if ((unsigned)(now - t_start) >= QDELAY && (unsigned)(now - t_pub) >= QAFTER) break;
        __builtin_amdgcn_s_sleep(1);
      }
    }
    {
      const int off = WS_QP + (b * QP_B + 32 * j + 8 * hw) * 128 + lane * 16;
      v4u v[4];
      pollNx4<4, QP_K * 128>(ws, tag, off, v);
      const float q0 = (asf(v[0].x) + asf(v[1].x)) + (asf(v[2].x) + asf(v[3].x)), q1 = (asf(v[0].z) + asf(v[1].z)) + (asf(v[2].z) + asf(v[3].z));
      *(LAS unsigned*)(L + L_Q + hw * 320 + lane * 4) = pack_bf(q0, q1);
      if (hw == 0 && lane == 0) flags[1] = tag;
    }
    lds_arrive(flags + 6, lane);
    __builtin_amdgcn_s_setprio(0);
    if (hw == 0) {
      while (flags[2] < 8u) __builtin_amdgcn_s_sleep(1);
      asm volatile("" ::: "memory");
      const float mw = ((const LAS float*)(L + L_MAX))[lane & 31], lw = ((const LAS float*)(L + L_SUM))[lane & 31];
      float mx = max2(mw, dpp<0x124>(mw));
      mx = max2(mx, dpp<0x128>(mx));
      mx = x16_max(mx);
      const float f = __builtin_amdgcn_exp2f((mw - mx) * 1.4426950408889634f);
      float l = f * lw;
      l += dpp<0x124>(l);
      l += dpp<0x128>(l);
      l = x16_sum(l);
      if (lane < 32) ((LAS float*)(L + L_MSC))[(lane & 3) * 8 + (lane >> 2)] = f * __builtin_amdgcn_rcpf(l);
      lds_arrive(flags + 4, lane);
      if (lane < 4) {
        const float lse = mx * 1.4426950408889634f + __builtin_amdgcn_logf(l);
        __builtin_amdgcn_raw_buffer_store_b64(v2u{asu(lse), tag}, rs, WS_LS + (b * HKV + j) * 128 + (lane * 4 + s) * 8, 0, AUX_SC1);
      }
    }
    if (is_wo) {
      const int h = 4 * j + (r >> 2), ns = r & 3;
      while (flags[4] < DMAFLAG) __builtin_amdgcn_s_sleep(2);
      const unsigned char* src = (const unsigned char*)wo + ((size_t)(128 * h + 32 * hw + g) * D + 128 * ns) * 2 + n16 * 16;
#pragma unroll
      for (int i = 0; i < 8; ++i) glds16(src + (size_t)i * 4 * D * 2, lds0 + L_W + (32 * hw + 4 * i) * 256);
      asm volatile("s_waitcnt vmcnt(0)" ::: "memory");
      BAR();
    } else if (is_mlp) {
      while (flags[4] < DMAFLAG) __builtin_amdgcn_s_sleep(2);
      const int fg = 8 * j + ((r - 16) >> 1), nh = r & 1;
      const size_t go = ((size_t)(128 * hw + (lane >> 2)) * FF + 32 * fg) * 2 + (lane & 3) * 16;
#pragma unroll
      for (int i = 0; i < 8; ++i) {
        if (i >= 2) asm volatile("s_waitcnt vmcnt(3)" ::: "memory");
        glds16((const unsigned char*)wg + go + (size_t)i * 16 * FF * 2, lds0 + L_GT + (128 * hw + 16 * i) * 64);
        if (i >= 2) asm volatile("s_waitcnt vmcnt(3)" ::: "memory");
        glds16((const unsigned char*)wu + go + (size_t)i * 16 * FF * 2, lds0 + L_UP + (128 * hw + 16 * i) * 64);
      }
      const unsigned char* sd = (const unsigned char*)wd + ((size_t)(32 * fg + 8 * hw + (lane >> 5)) * D + 256 * nh) * 2 + (lane & 31) * 16;
#pragma unroll
      for (int i = 0; i < 4; ++i) {
        asm volatile("s_waitcnt vmcnt(5)" ::: "memory");
        glds16(sd + (size_t)i * 2 * D * 2, lds0 + L_DN + (8 * hw + 2 * i) * 512);
      }
      asm volatile("s_waitcnt vmcnt(0)" ::: "memory");
      BAR();
    }
  }
}

}

extern "C" void kernel_launch(void* const* d_in, const int* in_sizes, int n_in, void* d_out, int out_size, void* d_ws,
                              size_t ws_size, hipStream_t stream) {
  (void)in_sizes; (void)n_in; (void)out_size; (void)ws_size;
  (void)hipFuncSetAttribute((const void*)fused_decode, hipFuncAttributeMaxDynamicSharedMemorySize, LDS_BYTES);
  fused_decode<<<256, NTHR, LDS_BYTES, stream>>>(
      (const unsigned short*)d_in[0], (const unsigned short*)d_in[1], (const unsigned short*)d_in[2], (const unsigned short*)d_in[3],
      (const unsigned short*)d_in[4], (const unsigned short*)d_in[6], (const unsigned short*)d_in[7], (const unsigned short*)d_in[8],
      (unsigned short*)d_out, (unsigned char*)d_ws);
}
```
